```python
import torch

def _softmax(x, axis=-1):
    return torch.softmax(x, dim=axis)
def _gelu(x):
    return torch.nn.functional.gelu(x, approximate="tanh")
def _silu(x):
    return torch.nn.functional.silu(x)
def _sigmoid(x):
    return torch.sigmoid(x)
def _layernorm(x, axis=-1, eps=1e-5):
    m = x.mean(dim=axis, keepdim=True)
    v = x.var(dim=axis, keepdim=True, unbiased=False)
    return (x - m) / torch.sqrt(v + eps)
def _scatter_add(tgt, idx, src, axis=0):
    return tgt.clone().index_add_(axis, idx, src)
def _segment_sum(src, seg, n):
    out = torch.zeros((n,) + src.shape[1:], dtype=src.dtype)
    return out.index_add_(0, seg, src)
def _pad(x, pw):
    flat = tuple(v for pair in reversed(pw) for v in pair)
    return torch.nn.functional.pad(x, flat)

kernel_name = 'interleave_layers__ssm_conv_s4d__glu_gated_geglu__prompt'

def setup_inputs():

    _w = (lambda a: (lambda t: t.to(torch.bfloat16) if t.is_floating_point() else t)(torch.as_tensor(a)))
    def _A_setup():
        Bsz, L, D, N = 1, 2048, 512, 128
        return {
            "u": _w((lambda *s: torch.randn(*s, dtype=torch.bfloat16) * 0.05)(Bsz, L, D)),
            "log_A_real": _w((lambda *s: torch.randn(*s, dtype=torch.bfloat16) * 3.2)(D, N) * 0.5),
            "A_imag": _w((lambda *s: torch.randn(*s, dtype=torch.bfloat16) * 0.05)(D, N) * 3.14),
            "C_real": _w((lambda *s: torch.randn(*s, dtype=torch.bfloat16) * 0.05)(D, N) / N ** 0.5),
            "C_imag": _w((lambda *s: torch.randn(*s, dtype=torch.bfloat16) * 0.05)(D, N) / N ** 0.5),
            "log_dt": _w((lambda *s: torch.randn(*s, dtype=torch.bfloat16) * 0.05)(D) * 0.5 - 4.0),
            "D_skip": _w((lambda *s: torch.randn(*s, dtype=torch.bfloat16) * 0.05)(D)),
        }
    _A_in = _A_setup()

    def _B_setup():
        B, S, Dm, Df = 1, 2048, 512, 1376
        return {
            "x": _w((lambda *s: torch.randn(*s, dtype=torch.bfloat16) * 0.05)(B, S, Dm)),
            "w_gate": _w((lambda *s: torch.randn(*s, dtype=torch.bfloat16) * 0.8)(Dm, Df) * (Dm ** -0.5)),
            "w_up": _w((lambda *s: torch.randn(*s, dtype=torch.bfloat16) * 0.8)(Dm, Df) * (Dm ** -0.5)),
            "w_down": _w((lambda *s: torch.randn(*s, dtype=torch.bfloat16) * 0.8)(Df, Dm) * (Df ** -0.5)),
        }
    _B_in = _B_setup()

    return {
        "A_u": _A_in["u"],
        "A_log_A_real": _A_in["log_A_real"],
        "A_A_imag": _A_in["A_imag"],
        "A_C_real": _A_in["C_real"],
        "A_C_imag": _A_in["C_imag"],
        "A_log_dt": _A_in["log_dt"],
        "A_D_skip": _A_in["D_skip"],
        "B_x": _B_in["x"],
        "B_w_gate": _B_in["w_gate"],
        "B_w_up": _B_in["w_up"],
        "B_w_down": _B_in["w_down"],
    }

def reference(A_u, A_log_A_real, A_A_imag, A_C_real, A_C_imag, A_log_dt, A_D_skip, B_x, B_w_gate, B_w_up, B_w_down):
    def _A_ref(u, log_A_real, A_imag, C_real, C_imag, log_dt, D_skip):
        L = u.shape[1]
        dt = torch.exp(log_dt)
        A = -torch.exp(log_A_real) + 1j * A_imag
        C = (C_real + 1j * C_imag) * dt[:, None]
        pos = (lambda x, dt: x.to(dt))(torch.arange(L), log_dt.dtype)
        P = torch.exp((A * dt[:, None])[:, :, None] * pos[None, None, :])
        K = 2.0 * torch.einsum("dn,dnl->dl", C, P).real
        L2 = 2 * L
        u_t = (lambda x, *d: x.permute(*d))(u, 0, 2, 1)
        Kf = (lambda x, *a, **k: torch.fft.rfft(x.float(), *a, **k))(K, L2)
        Uf = (lambda x, *a, **k: torch.fft.rfft(x.float(), *a, **k))(u_t, L2)
        y = (lambda x, *a, **k: torch.fft.irfft(x, *a, **k).to(torch.bfloat16))(Uf * Kf[None, :, :], L2)[:, :, :L]
        y = (lambda x, *d: x.permute(*d))(y, 0, 2, 1)
        return y + u * D_skip[None, None, :]

    def _B_ref(x, w_gate, w_up, w_down):
        g = torch.einsum("bsd,df->bsf", x, w_gate)
        u = torch.einsum("bsd,df->bsf", x, w_up)
        h = _gelu(g) * u
        return torch.einsum("bsf,fd->bsd", h, w_down)

    x = A_u
    x = x + _A_ref(_layernorm(x, axis=-1), A_log_A_real, A_A_imag, A_C_real, A_C_imag, A_log_dt, A_D_skip)
    x = x + _B_ref(_layernorm(x, axis=-1), B_w_gate, B_w_up, B_w_down)
    x = x + _A_ref(_layernorm(x, axis=-1), A_log_A_real, A_A_imag, A_C_real, A_C_imag, A_log_dt, A_D_skip)
    x = x + _B_ref(_layernorm(x, axis=-1), B_w_gate, B_w_up, B_w_down)
    return x
```

```cpp
#include <hip/hip_runtime.h>
#include <hip/hip_bf16.h>
#include <stdint.h>
typedef unsigned short u16;
#define L 2048
#define D 512
#define N 128
#define DF 1376
__device__ inline float b2f(u16 v){ return __uint_as_float(((uint32_t)v)<<16); }
__device__ inline u16 f2b(float f){ uint32_t u=__float_as_uint(f); u += 0x7fff + ((u>>16)&1); return (u16)(u>>16); }
__device__ inline float rb(float f){ return b2f(f2b(f)); }

__global__ void kgen(const u16* logA,const u16* Aim,const u16* Cr,const u16* Ci,const u16* logdt,float* K){
  int d=blockIdx.x;
  __shared__ float ar[N],ai[N],cr[N],ci[N];
  float dt=rb(expf(b2f(logdt[d])));
  for(int n=threadIdx.x;n<N;n+=blockDim.x){
    ar[n]=-rb(expf(b2f(logA[d*N+n])))*dt; ai[n]=b2f(Aim[d*N+n])*dt; cr[n]=b2f(Cr[d*N+n])*dt; ci[n]=b2f(Ci[d*N+n])*dt;
  }
  __syncthreads();
  for(int l=threadIdx.x;l<L;l+=blockDim.x){
    float p=rb((float)l); float s=0;
    for(int n=0;n<N;n++){ float e=expf(ar[n]*p); float sn,cs; sincosf(ai[n]*p,&sn,&cs); s+=e*(cr[n]*cs-ci[n]*sn);}
    K[d*L+l]=2.f*s;
  }
}
__global__ void ln(const u16* x,float* un){
  int t=blockIdx.x; __shared__ float red[64];
  int lane=threadIdx.x; float v[8]; float s=0;
  for(int i=0;i<8;i++){ v[i]=b2f(x[t*D+lane*8+i]); s+=v[i]; }
  for(int o=32;o;o>>=1) s+=__shfl_xor(s,o);
  float m=s/D; float q=0; for(int i=0;i<8;i++){ q+=(v[i]-m)*(v[i]-m);}
  for(int o=32;o;o>>=1) q+=__shfl_xor(q,o);
  float r=rsqrtf(q/D+1e-5f);
  for(int i=0;i<8;i++) un[t*D+lane*8+i]=rb((v[i]-m)*r);
}
__global__ void conv(const u16* x,const float* un,const float* K,const u16* Dsk,u16* out){
  int d=blockIdx.x; __shared__ float su[L],sk[L];
  for(int i=threadIdx.x;i<L;i+=blockDim.x){ su[i]=un[i*D+d]; sk[i]=K[d*L+i]; }
  __syncthreads();
  float ds=b2f(Dsk[d]);
  for(int t=threadIdx.x;t<L;t+=blockDim.x){ float s=0; for(int j=0;j<=t;j++) s+=sk[j]*su[t-j];
    float y=rb(rb(s)+rb(su[t]*ds)); out[t*D+d]=f2b(b2f(x[t*D+d])+y); }
}
__global__ void g1(const float* un,const u16* wg,const u16* wu,float* h){
  int t=blockIdx.x; __shared__ float sx[D];
  for(int i=threadIdx.x;i<D;i+=blockDim.x) sx[i]=un[t*D+i];
  __syncthreads();
  for(int f=threadIdx.x;f<DF;f+=blockDim.x){ float g=0,u=0; for(int k=0;k<D;k++){ g+=sx[k]*b2f(wg[k*DF+f]); u+=sx[k]*b2f(wu[k*DF+f]); }
    g=rb(g); u=rb(u); float z=0.7978845608f*(g+0.044715f*g*g*g); float ge=rb(0.5f*g*(1.f+tanhf(z))); h[t*DF+f]=rb(ge*u); }
}
__global__ void g2(const float* h,const u16* wd,const u16* x,u16* out){
  int t=blockIdx.x; __shared__ float sh[DF];
  for(int i=threadIdx.x;i<DF;i+=blockDim.x) sh[i]=h[t*DF+i];
  __syncthreads();
  for(int d=threadIdx.x;d<D;d+=blockDim.x){ float s=0; for(int k=0;k<DF;k++) s+=sh[k]*b2f(wd[k*D+d]); out[t*D+d]=f2b(b2f(x[t*D+d])+rb(s)); }
}
extern "C" void kernel_launch(void* const* d_in, const int* in_sizes, int n_in, void* d_out, int out_size, void* d_ws, size_t ws_size, hipStream_t stream) {
  const u16* u=(const u16*)d_in[0];
  char* ws=(char*)d_ws;
  float* K=(float*)ws; float* un=(float*)(ws+(8<<20)); float* h=(float*)(ws+(16<<20)); u16* x1=(u16*)(ws+(32<<20)); u16* x2=(u16*)(ws+(36<<20)); u16* x3=(u16*)(ws+(40<<20));
  kgen<<<D,256,0,stream>>>((const u16*)d_in[1],(const u16*)d_in[2],(const u16*)d_in[3],(const u16*)d_in[4],(const u16*)d_in[5],K);
  ln<<<L,64,0,stream>>>(u,un);
  conv<<<D,256,0,stream>>>(u,un,K,(const u16*)d_in[6],x1);
  ln<<<L,64,0,stream>>>(x1,un);
  g1<<<L,256,0,stream>>>(un,(const u16*)d_in[8],(const u16*)d_in[9],h);
  g2<<<L,256,0,stream>>>(h,(const u16*)d_in[10],x1,x2);
  ln<<<L,64,0,stream>>>(x2,un);
  conv<<<D,256,0,stream>>>(x2,un,K,(const u16*)d_in[6],x3);
  ln<<<L,64,0,stream>>>(x3,un);
  g1<<<L,256,0,stream>>>(un,(const u16*)d_in[8],(const u16*)d_in[9],h);
  g2<<<L,256,0,stream>>>(h,(const u16*)d_in[10],x3,(u16*)d_out);
}
```

```cpp
#include <hip/hip_runtime.h>
#include <hip/hip_bf16.h>
#include <stdint.h>
typedef unsigned short u16;
#define L 2048
#define D 512
#define N 128
#define DF 1376
__device__ inline float b2f(u16 v){ return __uint_as_float(((uint32_t)v)<<16); }
__device__ inline u16 f2b(float f){ uint32_t u=__float_as_uint(f); u += 0x7fff + ((u>>16)&1); return (u16)(u>>16); }
__device__ inline float rb(float f){ return b2f(f2b(f)); }

__global__ void kgen(const u16* logA,const u16* Aim,const u16* Cr,const u16* Ci,const u16* logdt,float* K){
  int d=blockIdx.x;
  __shared__ float ar[N],ai[N],cr[N],ci[N];
  float dt=rb(expf(b2f(logdt[d])));
  for(int n=threadIdx.x;n<N;n+=blockDim.x){
    ar[n]=-rb(expf(b2f(logA[d*N+n])))*dt; ai[n]=b2f(Aim[d*N+n])*dt; cr[n]=b2f(Cr[d*N+n])*dt; ci[n]=b2f(Ci[d*N+n])*dt;
  }
  __syncthreads();
  for(int l=threadIdx.x;l<L;l+=blockDim.x){
    float p=rb((float)l); float s=0;
    for(int n=0;n<N;n++){ float e=expf(ar[n]*p); float sn,cs; sincosf(ai[n]*p,&sn,&cs); s+=e*(cr[n]*cs-ci[n]*sn);}
    K[d*L+l]=2.f*s;
  }
}
__global__ void ln(const u16* x,float* un){
  int t=blockIdx.x; __shared__ float red[64];
  int lane=threadIdx.x; float v[8]; float s=0;
  for(int i=0;i<8;i++){ v[i]=b2f(x[t*D+lane*8+i]); s+=v[i]; }
  for(int o=32;o;o>>=1) s+=__shfl_xor(s,o);
  float m=s/D; float q=0; for(int i=0;i<8;i++){ q+=(v[i]-m)*(v[i]-m);}
  for(int o=32;o;o>>=1) q+=__shfl_xor(q,o);
  float mb=rb(m); float den=rb(sqrtf(rb(rb(q/D)+1e-5f)));
  for(int i=0;i<8;i++) un[t*D+lane*8+i]=(v[i]-mb)/den;
}
__global__ void conv(const u16* x,const float* un,const float* K,const u16* Dsk,u16* out){
  int d=blockIdx.x; __shared__ float su[L],sk[L];
  for(int i=threadIdx.x;i<L;i+=blockDim.x){ su[i]=un[i*D+d]; sk[i]=K[d*L+i]; }
  __syncthreads();
  float ds=b2f(Dsk[d]);
  for(int t=threadIdx.x;t<L;t+=blockDim.x){ float s=0; for(int j=0;j<=t;j++) s+=sk[j]*su[t-j];
    float y=s+su[t]*ds; out[t*D+d]=f2b(b2f(x[t*D+d])+y); }
}
__global__ void g1(const float* un,const u16* wg,const u16* wu,float* h){
  int t=blockIdx.x; __shared__ float sx[D];
  for(int i=threadIdx.x;i<D;i+=blockDim.x) sx[i]=un[t*D+i];
  __syncthreads();
  for(int f=threadIdx.x;f<DF;f+=blockDim.x){ float g=0,u=0; for(int k=0;k<D;k++){ g+=sx[k]*b2f(wg[k*DF+f]); u+=sx[k]*b2f(wu[k*DF+f]); }
     float z=0.7978845608f*(g+0.044715f*g*g*g); float ge=(0.5f*g*(1.f+tanhf(z))); h[t*DF+f]=rb(ge*u); }
}
__global__ void g2(const float* h,const u16* wd,const u16* x,u16* out){
  int t=blockIdx.x; __shared__ float sh[DF];
  for(int i=threadIdx.x;i<DF;i+=blockDim.x) sh[i]=h[t*DF+i];
  __syncthreads();
  for(int d=threadIdx.x;d<D;d+=blockDim.x){ float s=0; for(int k=0;k<DF;k++) s+=sh[k]*b2f(wd[k*D+d]); out[t*D+d]=f2b(b2f(x[t*D+d])+s); }
}
extern "C" void kernel_launch(void* const* d_in, const int* in_sizes, int n_in, void* d_out, int out_size, void* d_ws, size_t ws_size, hipStream_t stream) {
  const u16* u=(const u16*)d_in[0];
  char* ws=(char*)d_ws;
  float* K=(float*)ws; float* un=(float*)(ws+(8<<20)); float* h=(float*)(ws+(16<<20)); u16* x1=(u16*)(ws+(32<<20)); u16* x2=(u16*)(ws+(36<<20)); u16* x3=(u16*)(ws+(40<<20));
  kgen<<<D,256,0,stream>>>((const u16*)d_in[1],(const u16*)d_in[2],(const u16*)d_in[3],(const u16*)d_in[4],(const u16*)d_in[5],K);
  ln<<<L,64,0,stream>>>(u,un);
  conv<<<D,256,0,stream>>>(u,un,K,(const u16*)d_in[6],x1);
  ln<<<L,64,0,stream>>>(x1,un);
  g1<<<L,256,0,stream>>>(un,(const u16*)d_in[8],(const u16*)d_in[9],h);
  g2<<<L,256,0,stream>>>(h,(const u16*)d_in[10],x1,x2);
  ln<<<L,64,0,stream>>>(x2,un);
  conv<<<D,256,0,stream>>>(x2,un,K,(const u16*)d_in[6],x3);
  ln<<<L,64,0,stream>>>(x3,un);
  g1<<<L,256,0,stream>>>(un,(const u16*)d_in[8],(const u16*)d_in[9],h);
  g2<<<L,256,0,stream>>>(h,(const u16*)d_in[10],x3,(u16*)d_out);
}
```

```cpp
#include <hip/hip_runtime.h>
#include <stdint.h>

typedef unsigned short u16;
typedef uint32_t u32;
typedef __attribute__((ext_vector_type(2))) float f32x2;
typedef __attribute__((ext_vector_type(4))) float f32x4;
typedef __attribute__((ext_vector_type(16))) float f32x16;
typedef __attribute__((ext_vector_type(2))) _Float16 f16x2;
typedef __attribute__((ext_vector_type(8))) _Float16 f16x8;
typedef __attribute__((ext_vector_type(2))) __bf16 bf16x2;
typedef __attribute__((ext_vector_type(8))) __bf16 bf16x8;
typedef __attribute__((ext_vector_type(4))) short s16x4;
typedef __attribute__((ext_vector_type(2))) u32 u32x2;
typedef __attribute__((ext_vector_type(4))) u32 u32x4;
#define LDSP __attribute__((address_space(3)))
#define GLBP __attribute__((address_space(1)))
#define DEVI __device__ __forceinline__

constexpr int SL = 2048, SD = 512, NM = 128, DF = 1376, FT = 86;
constexpr int KCOPY = 2080;
constexpr int KCH_B = 2 * KCOPY * 2;
constexpr size_t O_KT = 0;
constexpr size_t O_KS = 4352 * 1024;
constexpr size_t O_CS = O_KS + 4096;
constexpr size_t O_ST0 = O_CS + 16384;
constexpr size_t O_ST2 = O_ST0 + 16384;
constexpr size_t O_XP0 = O_ST2 + 131072;
constexpr size_t O_W1F = O_XP0 + (2 << 20);
constexpr size_t O_W2F = O_W1F + (size_t)FT * 32 * 1024;
constexpr size_t O_X1T = O_W2F + (size_t)16 * FT * 1024;
constexpr size_t O_H1 = O_X1T + (2 << 20);
constexpr size_t O_XP2 = O_H1 + (size_t)64 * FT * 1024;
constexpr size_t O_X3T = O_XP2 + (2 << 20);
constexpr size_t O_H2 = O_X3T + (2 << 20);

DEVI float bflo(u32 v) { return __uint_as_float(v << 16); }
DEVI float bfhi(u32 v) { return __uint_as_float(v & 0xffff0000u); }
DEVI float b2f(u16 v) { return __uint_as_float(((u32)v) << 16); }
DEVI u32 pkbf(float a, float b) { bf16x2 r = __builtin_convertvector((f32x2){a, b}, bf16x2); return __builtin_bit_cast(u32, r); }
DEVI u32 pkf16(float a, float b) { f16x2 r = __builtin_convertvector((f32x2){a, b}, f16x2); return __builtin_bit_cast(u32, r); }
DEVI float rbf(float a) { return bflo(pkbf(a, 0.f)); }
DEVI float wsum(float v) {
#pragma unroll
  for (int o = 32; o; o >>= 1) v += __shfl_xor(v, o);
  return v;
}
DEVI void glds16(const void* g, void* l) {
  __builtin_amdgcn_global_load_lds((const GLBP void*)g, (LDSP void*)l, 16, 0, 0);
}
#define WAITV(n) asm volatile("s_waitcnt vmcnt(" #n ")" ::: "memory")
#define WAITL0() asm volatile("s_waitcnt lgkmcnt(0)" ::: "memory")
#define BAR() __builtin_amdgcn_s_barrier()
#define SCHED() __builtin_amdgcn_sched_barrier(0)

DEVI void cexp2(float are, float aim, float p, float& re, float& im) {
  float e = __builtin_amdgcn_exp2f(fmaf(are, p, 14.f));
  float ph = __builtin_amdgcn_fractf(aim * p);
  re = e * __builtin_amdgcn_cosf(ph);
  im = e * __builtin_amdgcn_sinf(ph);
}

DEVI void transpose8x8(const u32x4 (&in)[8], u32x4 (&out)[8]) {
#pragma unroll
  for (int f = 0; f < 8; ++f)
#pragma unroll
    for (int i = 0; i < 4; ++i)
      out[f][i] = __builtin_amdgcn_perm(in[2 * i + 1][f >> 1], in[2 * i][f >> 1], (f & 1) ? 0x07060302u : 0x05040100u);
}

constexpr int NB_LN = 64, NB_W1 = 86, NB_W2 = 43, NB_K = 256;

__global__ __launch_bounds__(256) void k_prep(const u16* __restrict__ u, const u16* __restrict__ logA,
                                              const u16* __restrict__ Aim, const u16* __restrict__ Cre,
                                              const u16* __restrict__ Cim, const u16* __restrict__ logdt,
                                              const u16* __restrict__ wg, const u16* __restrict__ wu,
                                              const u16* __restrict__ wd, char* __restrict__ ws) {
  __shared__ __attribute__((aligned(16))) float sm[2 * 128 * 4 + 16 + 4 * 656];
  const int tid = threadIdx.x, lane = tid & 63, w = tid >> 6;
  int b = blockIdx.x;
  if (b < NB_LN) {
    const int t0 = b * 32 + w * 8;
    u32x4 v[8];
#pragma unroll
    for (int i = 0; i < 8; ++i) v[i] = *(const u32x4*)(u + (size_t)(t0 + i) * SD + lane * 8);
    u32* xp = (u32*)(ws + O_XP0);
#pragma unroll
    for (int c = 0; c < 4; ++c) {
      u32* dst = xp + (size_t)(lane * 4 + c) * SL + t0;
      *(u32x4*)dst = (u32x4){v[0][c], v[1][c], v[2][c], v[3][c]};
      *(u32x4*)(dst + 4) = (u32x4){v[4][c], v[5][c], v[6][c], v[7][c]};
    }
    f32x2* st = (f32x2*)(ws + O_ST0);
#pragma unroll
    for (int i = 0; i < 8; ++i) {
      float s = 0.f;
#pragma unroll
      for (int c = 0; c < 4; ++c) s += bflo(v[i][c]) + bfhi(v[i][c]);
      float m = wsum(s) * (1.f / SD), q = 0.f;
#pragma unroll
      for (int c = 0; c < 4; ++c) {
        float a = bflo(v[i][c]) - m, d = bfhi(v[i][c]) - m;
        q += a * a + d * d;
      }
      q = wsum(q);
      if (lane == 0) st[t0 + i] = (f32x2){m, rsqrtf(q * (1.f / SD) + 1e-5f)};
    }
    return;
  }
  b -= NB_LN;
  if (b < NB_W1) {
    const int mat = b & 1, fb = b >> 1;
    const u16* src = mat ? wu : wg;
    const int kg = w * 16 + (lane >> 2), fq = lane & 3, f0 = fb * 32 + fq * 8;
    u32x4 in[8], out[8];
#pragma unroll
    for (int i = 0; i < 8; ++i) in[i] = *(const u32x4*)(src + (size_t)(kg * 8 + i) * DF + f0);
    transpose8x8(in, out);
    u32x4* dst = (u32x4*)(ws + O_W1F) + ((size_t)((f0 >> 4) * 32 + (kg >> 1)) * 64 + (kg & 1) * 32 + (f0 & 15) + 16 * mat);
#pragma unroll
    for (int i = 0; i < 8; ++i) dst[i] = out[i];
#pragma unroll
    for (int f = 0; f < 8; ++f) {
      float s = 0.f;
#pragma unroll
      for (int i = 0; i < 4; ++i) s += bflo(out[f][i]) + bfhi(out[f][i]);
      s += __shfl_xor(s, 4); s += __shfl_xor(s, 8); s += __shfl_xor(s, 16); s += __shfl_xor(s, 32);
      if (lane < 4) sm[w * 32 + fq * 8 + f] = s;
    }
    __syncthreads();
    if (tid < 32) ((float*)(ws + O_CS))[mat * DF + fb * 32 + tid] = (sm[tid] + sm[32 + tid]) + (sm[64 + tid] + sm[96 + tid]);
    return;
  }
  b -= NB_W1;
  if (b < NB_W2) {
    const int ft = b * 2 + (w >> 1), h = w & 1, d0 = lane * 8;
    u32x4 in[8], out[8];
#pragma unroll
    for (int j = 0; j < 8; ++j) in[j] = *(const u32x4*)(wd + (size_t)(ft * 16 + 8 * (j >> 2) + 4 * h + (j & 3)) * SD + d0);
    transpose8x8(in, out);
    u32x4* dst = (u32x4*)(ws + O_W2F) + ((size_t)((d0 >> 5) * FT + ft) * 64 + h * 32 + (d0 & 31));
#pragma unroll
    for (int i = 0; i < 8; ++i) dst[i] = out[i];
    return;
  }
  b -= NB_W2;
  f32x4* prm = (f32x4*)sm;
  float* wmax = sm + 1024;
  float* tab = sm + 1024 + 16;
  const int ch = w >> 1;
  const int d = b * 2 + ch;
  float dt;
  {
    const int idx = d * NM + (tid & 127);
    const float la = b2f(logA[idx]), ai = b2f(Aim[idx]), cr = b2f(Cre[idx]), ci = b2f(Cim[idx]);
    dt = rbf(expf(b2f(logdt[d])));
    const float ar = -rbf(expf(la)) * dt;
    prm[tid] = (f32x4){ar * 1.44269504088896f, (ai * dt) * 0.159154943091895f, cr, ci};
    float mx = fmaxf(fabsf(cr), fabsf(ci));
#pragma unroll
    for (int o = 32; o; o >>= 1) mx = fmaxf(mx, __shfl_xor(mx, o));
    if (lane == 0) wmax[w] = mx;
  }
  __syncthreads();
  const int ce = __builtin_amdgcn_frexp_expf(fmaxf(wmax[ch * 2], wmax[ch * 2 + 1]));
  if ((tid & 127) == 0) ((float*)(ws + O_KS))[d] = ldexpf(dt, ce - 7);
  const int r = lane & 15, h = lane >> 4;
  const float p0 = (float)r;
  const float p1 = (float)(r < 8 ? 256 + 2 * r : 512 + 256 * ((r - 8) >> 2) + 4 * (r & 3));
  const float p2 = (float)(r < 8 ? 1024 + 256 * (r >> 1) + 8 * (r & 1) : 2048);
  const float pc = (float)(16 * r);
  f32x4 a0 = {0, 0, 0, 0}, a1 = {0, 0, 0, 0}, a2 = {0, 0, 0, 0};
#pragma unroll 1
  for (int s = 0; s < 4; ++s) {
    u32x4 A0, A1, A2, B;
#pragma unroll
    for (int m = 0; m < 4; ++m) {
      const f32x4 p = prm[ch * 128 + ((w & 1) * 4 + s) * 16 + h * 4 + m];
      float re, im;
      cexp2(p[0], p[1], p0, re, im); A0[m] = pkf16(re, im);
      cexp2(p[0], p[1], p1, re, im); A1[m] = pkf16(re, im);
      cexp2(p[0], p[1], p2, re, im); A2[m] = pkf16(re, im);
      cexp2(p[0], p[1], pc, re, im);
      const float cr = ldexpf(p[2], -ce), ci = ldexpf(p[3], -ce);
      B[m] = pkf16(cr * re - ci * im, -(cr * im + ci * re));
    }
    const f16x8 bb = __builtin_bit_cast(f16x8, B);
    a0 = __builtin_amdgcn_mfma_f32_16x16x32_f16(__builtin_bit_cast(f16x8, A0), bb, a0, 0, 0, 0);
    a1 = __builtin_amdgcn_mfma_f32_16x16x32_f16(__builtin_bit_cast(f16x8, A1), bb, a1, 0, 0, 0);
    a2 = __builtin_amdgcn_mfma_f32_16x16x32_f16(__builtin_bit_cast(f16x8, A2), bb, a2, 0, 0, 0);
  }
  {
    float* T = tab + w * 656;
    *(f32x4*)(T + 16 * r + 4 * h) = a0;
    if (h < 2) *(f32x4*)(T + 256 + 8 * r + 4 * h) = a1;
    else *(f32x4*)(T + 384 + 64 * (h - 2) + 4 * r) = a1;
    if (h < 2) {
      *(f32x2*)(T + 512 + 32 * (2 * h) + 2 * r) = (f32x2){a2[0], a2[1]};
      *(f32x2*)(T + 512 + 32 * (2 * h + 1) + 2 * r) = (f32x2){a2[2], a2[3]};
    } else if (lane == 32) T[640] = a2[0];
  }
  __syncthreads();
#pragma unroll
  for (int c = 0; c < 2; ++c) {
    const float* T0 = tab + (2 * c) * 656;
    float kv[9];
#pragma unroll
    for (int i = 0; i < 9; ++i) {
      const int l = 2047 - (tid * 8 + i);
      const int lc = l < 0 ? 0 : l;
      const int idx = lc < 128 ? lc : (int)(pkbf((float)lc, 0.f) & 0xffffu) - 0x4300 + 128;
      const float v = (T0[idx] + T0[656 + idx]) * 9.5367431640625e-07f;
      kv[i] = l < 0 ? 0.f : v;
    }
    char* dst = ws + O_KT + (size_t)(b * 2 + c) * KCH_B;
    *(u32x4*)(dst + tid * 16) = (u32x4){pkf16(kv[0], kv[1]), pkf16(kv[2], kv[3]), pkf16(kv[4], kv[5]), pkf16(kv[6], kv[7])};
    *(u32x4*)(dst + KCOPY * 2 + tid * 16) = (u32x4){pkf16(kv[1], kv[2]), pkf16(kv[3], kv[4]), pkf16(kv[5], kv[6]), pkf16(kv[7], kv[8])};
    if (tid < 8) *(u32x4*)(dst + (tid >> 2) * KCOPY * 2 + 4096 + (tid & 3) * 16) = (u32x4){0, 0, 0, 0};
  }
}

constexpr int CV_K = 0, CV_U = 17 * 1024, CV_UCH = (240 + 2048) * 2, CV_Y = CV_U + 2 * CV_UCH, CV_SZ = CV_Y + 4 * 8192;

template <bool PART>
__global__ __launch_bounds__(256) void k_conv(const u32* __restrict__ xp, const float* __restrict__ st,
                                              const char* __restrict__ kt, const float* __restrict__ ks,
                                              const u16* __restrict__ dsk, u16* __restrict__ xt) {
  __shared__ __attribute__((aligned(16))) char sm[CV_SZ];
  const int tid = threadIdx.x, lane = tid & 63, w = __builtin_amdgcn_readfirstlane(tid >> 6), b = blockIdx.x;
  {
    const char* src = kt + (size_t)b * 2 * KCH_B + lane * 16;
#pragma unroll
    for (int i = 0; i < 4; ++i) glds16(src + (w * 4 + i) * 1024, sm + CV_K + (w * 4 + i) * 1024);
    if (w == 0) glds16(src + 16 * 1024, sm + CV_K + 16 * 1024);
  }
  const u32x4 xa = *(const u32x4*)(xp + (size_t)b * SL + tid * 8), xb = *(const u32x4*)(xp + (size_t)b * SL + tid * 8 + 4);
  float mean[8], rstd[8];
  if (PART) {
#pragma unroll
    for (int i = 0; i < 8; ++i) {
      f32x4 p[4];
#pragma unroll
      for (int j = 0; j < 4; ++j) p[j] = *(const f32x4*)(st + (size_t)(tid * 8 + i) * 16 + j * 4);
      const float m = (((p[0][0] + p[0][2]) + (p[1][0] + p[1][2])) + ((p[2][0] + p[2][2]) + (p[3][0] + p[3][2]))) * 0.125f;
      float q = 0.f, dq = 0.f;
#pragma unroll
      for (int j = 0; j < 4; ++j) {
        q += p[j][1] + p[j][3];
        const float d0 = p[j][0] - m, d1 = p[j][2] - m;
        dq += d0 * d0 + d1 * d1;
      }
      mean[i] = m;
      rstd[i] = rsqrtf((q + 64.f * dq) * (1.f / SD) + 1e-5f);
    }
  } else {
#pragma unroll
    for (int i = 0; i < 4; ++i) {
      const f32x4 p = *(const f32x4*)(st + (size_t)tid * 16 + i * 4);
      mean[2 * i] = p[0]; rstd[2 * i] = p[1]; mean[2 * i + 1] = p[2]; rstd[2 * i + 1] = p[3];
    }
  }
  const float ks0 = ks[2 * b], ks1 = ks[2 * b + 1];
  const u32 dpair = *(const u32*)(dsk + 2 * b);
  float base0[8], base1[8];
  {
    const float d0 = bflo(dpair), d1 = bfhi(dpair);
    float u0[8], u1[8];
#pragma unroll
    for (int i = 0; i < 8; ++i) {
      const u32 v = i < 4 ? xa[i & 3] : xb[i & 3];
      const float x0 = bflo(v), x1 = bfhi(v);
      const u32 un = pkbf((x0 - mean[i]) * rstd[i], (x1 - mean[i]) * rstd[i]);
      u0[i] = bflo(un); u1[i] = bfhi(un);
      base0[i] = fmaf(u0[i], d0, x0); base1[i] = fmaf(u1[i], d1, x1);
    }
    *(u32x4*)(sm + CV_U + 480 + tid * 16) = (u32x4){pkf16(u0[0], u0[1]), pkf16(u0[2], u0[3]), pkf16(u0[4], u0[5]), pkf16(u0[6], u0[7])};
    *(u32x4*)(sm + CV_U + CV_UCH + 480 + tid * 16) = (u32x4){pkf16(u1[0], u1[1]), pkf16(u1[2], u1[3]), pkf16(u1[4], u1[5]), pkf16(u1[6], u1[7])};
    if (tid < 60) *(u32x4*)(sm + CV_U + (tid >= 30 ? CV_UCH + (tid - 30) * 16 : tid * 16)) = (u32x4){0, 0, 0, 0};
  }
  WAITV(0);
  __syncthreads();
  const int ch = w >> 1, r = lane & 15, h = lane >> 4;
  f32x4 acc[8];
#pragma unroll
  for (int g = 0; g < 8; ++g) acc[g] = (f32x4){0, 0, 0, 0};
  const u32* kb = (const u32*)(sm + CV_K + ch * KCH_B) + (((2031 - r + 8 * h) >> 1) + ((r & 1) ? 0 : KCOPY / 2));
  const char* ub = sm + CV_U + ch * CV_UCH + 2 * (224 + 16 * r + 8 * h);
#pragma unroll 2
  for (int rr = 0; rr < 4; ++rr) {
    const int rho = (w & 1) * 4 + rr;
    f16x8 A[8], B[8];
#pragma unroll
    for (int a = 0; a < 8; ++a) {
      const u32* p = kb - 16 * (8 * a + rho);
      A[a] = __builtin_bit_cast(f16x8, (u32x4){p[0], p[1], p[2], p[3]});
      B[a] = *(const f16x8*)(ub + 64 * (8 * a - rho));
    }
#pragma unroll
    for (int a = 0; a < 8; ++a)
#pragma unroll
      for (int q = 0; q < 8 - a; ++q)
        acc[a + q] = __builtin_amdgcn_mfma_f32_16x16x32_f16(A[a], B[q], acc[a + q], 0, 0, 0);
  }
  {
    f32x4* y = (f32x4*)(sm + CV_Y + w * 8192);
#pragma unroll
    for (int g = 0; g < 8; ++g) y[64 * g + 16 * h + (r ^ (8 * (h >> 1)))] = acc[g];
  }
  __syncthreads();
  {
    const int g = tid >> 5, c = (tid >> 1) & 15, hh = 2 * (tid & 1);
    const int s0 = 64 * g + 16 * hh + (c ^ (8 * (tid & 1)));
    const f32x4* y = (const f32x4*)(sm + CV_Y);
    const f32x4 p0 = y[s0] + y[512 + s0], p1 = y[s0 + 16] + y[512 + s0 + 16];
    const f32x4 q0 = y[1024 + s0] + y[1536 + s0], q1 = y[1024 + s0 + 16] + y[1536 + s0 + 16];
    float o0[8], o1[8];
#pragma unroll
    for (int i = 0; i < 4; ++i) {
      o0[i] = fmaf(p0[i], ks0, base0[i]); o0[4 + i] = fmaf(p1[i], ks0, base0[4 + i]);
      o1[i] = fmaf(q0[i], ks1, base1[i]); o1[4 + i] = fmaf(q1[i], ks1, base1[4 + i]);
    }
    *(u32x4*)(xt + (size_t)(2 * b) * SL + tid * 8) = (u32x4){pkbf(o0[0], o0[1]), pkbf(o0[2], o0[3]), pkbf(o0[4], o0[5]), pkbf(o0[6], o0[7])};
    *(u32x4*)(xt + (size_t)(2 * b + 1) * SL + tid * 8) = (u32x4){pkbf(o1[0], o1[1]), pkbf(o1[2], o1[3]), pkbf(o1[4], o1[5]), pkbf(o1[6], o1[7])};
  }
}

constexpr int G1_SLOT = 20 * 1024, G1_NS = 7, G1_ST = G1_SLOT * G1_NS, G1_SZ = G1_ST + 4096;

struct G1Frag { bf16x8 x[2]; bf16x8 w[3]; };

DEVI s16x4 trread(const char* p) {
  return __builtin_amdgcn_ds_read_tr16_b64_v4i16((LDSP s16x4*)p);
}
DEVI void g1_load(G1Frag& F, const char* x0, const char* x1, const char* wb, int kk) {
  const s16x4 a0 = trread(x0 + kk * 4096), a1 = trread(x0 + kk * 4096 + 1024);
  const s16x4 b0 = trread(x1 + kk * 4096), b1 = trread(x1 + kk * 4096 + 1024);
  F.x[0] = __builtin_bit_cast(bf16x8, __builtin_shufflevector(a0, a1, 0, 1, 2, 3, 4, 5, 6, 7));
  F.x[1] = __builtin_bit_cast(bf16x8, __builtin_shufflevector(b0, b1, 0, 1, 2, 3, 4, 5, 6, 7));
#pragma unroll
  for (int j = 0; j < 3; ++j) F.w[j] = *(const bf16x8*)(wb + (kk * 6 + j) * 1024);
}
DEVI void g1_mma(f32x16 (&acc)[2][3], const G1Frag& F) {
#pragma unroll
  for (int j = 0; j < 3; ++j)
#pragma unroll
    for (int t = 0; t < 2; ++t) acc[t][j] = __builtin_amdgcn_mfma_f32_32x32x16_bf16(F.w[j], F.x[t], acc[t][j], 0, 0, 0);
}
DEVI float geglu(float g, float u) {
  const float e = __builtin_amdgcn_exp2f(g * fmaf(g * g, -0.102943239f, -2.302208198f));
  return g * u * __builtin_amdgcn_rcpf(1.f + e);
}

__global__ __launch_bounds__(512, 2) void k_gemm1(const u16* __restrict__ xt, const char* __restrict__ w1f,
                                                  const float* __restrict__ cs, char* __restrict__ hf) {
  __shared__ __attribute__((aligned(16))) char sm[G1_SZ];
  const int tid = threadIdx.x, lane = tid & 63, w = __builtin_amdgcn_readfirstlane(tid >> 6);
  const int xcd = blockIdx.x & 7, idx = blockIdx.x >> 3;
  const int mt = (xcd >> 1) * 4 + (idx >> 3), sl = (xcd & 1) * 8 + (idx & 7);
  if (sl >= 15) return;
  if (w >= 4) {
    const int p = w - 4, q = lane >> 4, c = lane & 15;
    const char* xsrc = (const char*)xt + ((size_t)(p * 8 + q) * SL + mt * 128) * 2 + 16 * (c ^ (4 * q));
    const char* wsrc[3];
#pragma unroll
    for (int j = 0; j < 3; ++j) {
      const int fr = p * 3 + j;
      const int ft = min(sl * 6 + fr % 6, FT - 1);
      wsrc[j] = w1f + (size_t)(ft * 32 + fr / 6) * 1024 + lane * 16;
    }
    auto issue = [&](int ck, int slot) {
      char* s = sm + slot * G1_SLOT;
      glds16(xsrc + (size_t)ck * 32 * SL * 2, s + (p * 2) * 1024);
      glds16(xsrc + (size_t)(ck * 32 + 4) * SL * 2, s + (p * 2 + 1) * 1024);
#pragma unroll
      for (int j = 0; j < 3; ++j) glds16(wsrc[j] + ck * 2048, s + 8192 + (p * 3 + j) * 1024);
    };
#pragma unroll
    for (int i = 0; i < 7; ++i) issue(i, i);
    float s1[8], s2[8];
#pragma unroll
    for (int i = 0; i < 8; ++i) s1[i] = s2[i] = 0.f;
    const int soff = p * 2048 + 256 * q + 16 * (c ^ (4 * q));
#pragma unroll
    for (int i = 0; i < 16; ++i) {
      if (i == 0) WAITV(30);
      else if (i <= 10) WAITV(25);
      else if (i == 11) WAITV(20);
      else if (i == 12) WAITV(15);
      else if (i == 13) WAITV(10);
      else if (i == 14) WAITV(5);
      else WAITV(0);
      BAR();
      if (i >= 1 && i + 6 <= 15) issue(i + 6, (i + 6) % 7);
      const char* s = sm + (i % 7) * G1_SLOT + soff;
      u32x4 v0, v1;
      asm volatile("ds_read_b128 %0, %2\n\tds_read_b128 %1, %2 offset:1024\n\ts_waitcnt lgkmcnt(0)"
                   : "=&v"(v0), "=&v"(v1) : "v"((u32)(size_t)(LDSP char*)s) : "memory");
#pragma unroll
      for (int k = 0; k < 4; ++k) {
        const float a = bflo(v0[k]), bq = bfhi(v0[k]), cq = bflo(v1[k]), dq = bfhi(v1[k]);
        s1[2 * k] += a + cq; s1[2 * k + 1] += bq + dq;
        s2[2 * k] = fmaf(a, a, fmaf(cq, cq, s2[2 * k]));
        s2[2 * k + 1] = fmaf(bq, bq, fmaf(dq, dq, s2[2 * k + 1]));
      }
    }
    float r8[8];
#pragma unroll
    for (int i = 0; i < 8; ++i) {
      const auto sw = __builtin_amdgcn_permlane32_swap(__float_as_uint(s1[i]), __float_as_uint(s2[i]), false, false);
      r8[i] = __uint_as_float(sw[0]) + __uint_as_float(sw[1]);
    }
    f32x4 r4;
#pragma unroll
    for (int i = 0; i < 4; ++i) {
      const auto sw = __builtin_amdgcn_permlane16_swap(__float_as_uint(r8[i]), __float_as_uint(r8[4 + i]), false, false);
      r4[i] = __uint_as_float(sw[0]) + __uint_as_float(sw[1]);
    }
    *(f32x4*)(sm + G1_ST + p * 1024 + (q >> 1) * 512 + (8 * c + 4 * (q & 1)) * 4) = r4;
    WAITL0();
    BAR();
    return;
  }
  const int wm = w >> 1, wf = w & 1, h = lane >> 5, r = lane & 31;
  f32x4 csg[3][2], csu[3][2];
#pragma unroll
  for (int j = 0; j < 3; ++j) {
    const int ft = min(sl * 6 + wf * 3 + j, FT - 1);
#pragma unroll
    for (int q = 0; q < 2; ++q) {
      csg[j][q] = *(const f32x4*)(cs + ft * 16 + 8 * q + 4 * h);
      csu[j][q] = *(const f32x4*)(cs + DF + ft * 16 + 8 * q + 4 * h);
    }
  }
  f32x16 acc[2][3];
#pragma unroll
  for (int t = 0; t < 2; ++t)
#pragma unroll
    for (int j = 0; j < 3; ++j)
#pragma unroll
      for (int e = 0; e < 16; ++e) acc[t][j][e] = 0.f;
  const int q = (lane & 15) >> 2;
  const int xo = 2048 * h + 256 * q + 32 * ((lane >> 4) & 1) + 8 * (lane & 3);
  const int xo0 = xo + 64 * ((2 * wm) ^ q), xo1 = xo + 64 * ((2 * wm + 1) ^ q);
  const int wo = 8192 + wf * 3072 + lane * 16;
  G1Frag F0, F1;
  BAR();
  g1_load(F0, sm + xo0, sm + xo1, sm + wo, 0);
  int so = 0;
#pragma unroll 1
  for (int i = 0; i < 16; ++i) {
    g1_load(F1, sm + so + xo0, sm + so + xo1, sm + so + wo, 1);
    SCHED();
    g1_mma(acc, F0);
    SCHED();
    WAITL0();
    BAR();
    so += G1_SLOT;
    if (so == G1_ST) so = 0;
    g1_load(F0, sm + so + xo0, sm + so + xo1, sm + so + wo, 0);
    SCHED();
    g1_mma(acc, F1);
    SCHED();
  }
#pragma unroll
  for (int t = 0; t < 2; ++t) {
    const float* sp = (const float*)(sm + G1_ST) + (wm * 2 + t) * 32 + r;
    const float s1 = (sp[0] + sp[256]) + (sp[512] + sp[768]);
    const float s2 = (sp[128] + sp[384]) + (sp[640] + sp[896]);
    const float m = s1 * (1.f / SD);
    const float rs = rsqrtf(fmaxf(s2 * (1.f / SD) - m * m, 0.f) + 1e-5f);
    const float mr = -m * rs;
#pragma unroll
    for (int j = 0; j < 3; ++j) {
      float o[8];
#pragma unroll
      for (int e = 0; e < 8; ++e) {
        const float g = fmaf(acc[t][j][e], rs, mr * csg[j][e >> 2][e & 3]);
        const float u = fmaf(acc[t][j][8 + e], rs, mr * csu[j][e >> 2][e & 3]);
        o[e] = geglu(g, u);
      }
      const int ft = sl * 6 + wf * 3 + j;
      if (ft < FT)
        *(u32x4*)(hf + ((size_t)((mt * 4 + wm * 2 + t) * FT + ft) * 64 + lane) * 16) =
            (u32x4){pkbf(o[0], o[1]), pkbf(o[2], o[3]), pkbf(o[4], o[5]), pkbf(o[6], o[7])};
    }
  }
}

constexpr int G2_PITCH = 68;
template <bool LAST>
__global__ __launch_bounds__(256, 1) void k_gemm2(const char* __restrict__ hf, const char* __restrict__ w2f,
                                                  const u16* __restrict__ xt, u32* __restrict__ xp,
                                                  float* __restrict__ st, u16* __restrict__ out) {
  __shared__ __attribute__((aligned(16))) float sm[4 * 64 * G2_PITCH];
  const int tid = threadIdx.x, lane = tid & 63, w = __builtin_amdgcn_readfirstlane(tid >> 6);
  const int xcd = blockIdx.x & 7, idx = blockIdx.x >> 3;
  const int mt = xcd * 4 + (idx >> 3), nt = idx & 7;
  const char* hp = hf + (size_t)(mt * 2) * FT * 1024 + lane * 16;
  const char* wp = w2f + (size_t)(nt * 2) * FT * 1024 + lane * 16;
  const int r16 = lane & 15, cg = lane >> 4, tok = mt * 64 + w * 16 + r16, ch0 = nt * 64 + cg * 16;
  constexpr int NS = 22, WIN = 10;
  bf16x8 fh[WIN][2], fw[WIN][2];
  auto ld = [&](int s, int slot) {
    int ks = w + 4 * s;
    ks = ks < FT ? ks : FT - 1;
    fh[slot][0] = *(const bf16x8*)(hp + (size_t)ks * 1024);
    fw[slot][0] = *(const bf16x8*)(wp + (size_t)ks * 1024);
    fh[slot][1] = *(const bf16x8*)(hp + (size_t)(FT + ks) * 1024);
    fw[slot][1] = *(const bf16x8*)(wp + (size_t)(FT + ks) * 1024);
  };
#pragma unroll
  for (int s = 0; s < WIN; ++s) ld(s, s);
  u16 res[16];
#pragma unroll
  for (int i = 0; i < 16; ++i) res[i] = xt[(size_t)(ch0 + i) * SL + tok];
  f32x16 acc[2][2];
#pragma unroll
  for (int a = 0; a < 2; ++a)
#pragma unroll
    for (int c = 0; c < 2; ++c)
#pragma unroll
      for (int e = 0; e < 16; ++e) acc[a][c][e] = 0.f;
  SCHED();
#pragma unroll
  for (int s = 0; s < NS; ++s) {
    const int slot = s % WIN;
    if (s < NS - 1 || w < 2) {
#pragma unroll
      for (int t = 0; t < 2; ++t)
#pragma unroll
        for (int c = 0; c < 2; ++c) acc[t][c] = __builtin_amdgcn_mfma_f32_32x32x16_bf16(fw[slot][c], fh[slot][t], acc[t][c], 0, 0, 0);
    }
    if (s + WIN < NS) ld(s + WIN, slot);
    SCHED();
  }
  {
    const int h = lane >> 5, r = lane & 31;
    float* P = sm + w * 64 * G2_PITCH;
#pragma unroll
    for (int t = 0; t < 2; ++t)
#pragma unroll
      for (int c = 0; c < 2; ++c)
#pragma unroll
        for (int q = 0; q < 4; ++q)
          *(f32x4*)(P + (t * 32 + r) * G2_PITCH + c * 32 + 8 * q + 4 * h) =
              (f32x4){acc[t][c][4 * q], acc[t][c][4 * q + 1], acc[t][c][4 * q + 2], acc[t][c][4 * q + 3]};
  }
  __syncthreads();
  u32 o[8];
  {
    const float* P = sm + (w * 16 + r16) * G2_PITCH + cg * 16;
#pragma unroll
    for (int i = 0; i < 4; ++i) {
      const f32x4 v = (*(const f32x4*)(P + 4 * i) + *(const f32x4*)(P + 64 * G2_PITCH + 4 * i)) +
                      (*(const f32x4*)(P + 128 * G2_PITCH + 4 * i) + *(const f32x4*)(P + 192 * G2_PITCH + 4 * i));
      o[2 * i] = pkbf(v[0] + b2f(res[4 * i]), v[1] + b2f(res[4 * i + 1]));
      o[2 * i + 1] = pkbf(v[2] + b2f(res[4 * i + 2]), v[3] + b2f(res[4 * i + 3]));
    }
  }
  if (LAST) {
    u32x4* dst = (u32x4*)(out + (size_t)tok * SD + ch0);
    dst[0] = (u32x4){o[0], o[1], o[2], o[3]};
    dst[1] = (u32x4){o[4], o[5], o[6], o[7]};
  } else {
#pragma unroll
    for (int i = 0; i < 8; ++i) xp[(size_t)(ch0 / 2 + i) * SL + tok] = o[i];
    float s = 0.f;
#pragma unroll
    for (int i = 0; i < 8; ++i) s += bflo(o[i]) + bfhi(o[i]);
    s += __shfl_xor(s, 16); s += __shfl_xor(s, 32);
    const float m = s * (1.f / 64.f);
    float q = 0.f;
#pragma unroll
    for (int i = 0; i < 8; ++i) {
      const float a = bflo(o[i]) - m, d = bfhi(o[i]) - m;
      q += a * a + d * d;
    }
    q += __shfl_xor(q, 16); q += __shfl_xor(q, 32);
    if (lane < 16) *(f32x2*)(st + (size_t)tok * 16 + nt * 2) = (f32x2){m, q};
  }
}

extern "C" void kernel_launch(void* const* d_in, const int* in_sizes, int n_in, void* d_out, int out_size,
                              void* d_ws, size_t ws_size, hipStream_t stream) {
  char* ws = (char*)d_ws;
  const u16* dsk = (const u16*)d_in[6];
  k_prep<<<NB_LN + NB_W1 + NB_W2 + NB_K, 256, 0, stream>>>(
      (const u16*)d_in[0], (const u16*)d_in[1], (const u16*)d_in[2], (const u16*)d_in[3], (const u16*)d_in[4],
      (const u16*)d_in[5], (const u16*)d_in[8], (const u16*)d_in[9], (const u16*)d_in[10], ws);
  k_conv<false><<<256, 256, 0, stream>>>((const u32*)(ws + O_XP0), (const float*)(ws + O_ST0), ws + O_KT,
                                         (const float*)(ws + O_KS), dsk, (u16*)(ws + O_X1T));
  k_gemm1<<<256, 512, 0, stream>>>((const u16*)(ws + O_X1T), ws + O_W1F, (const float*)(ws + O_CS), ws + O_H1);
  k_gemm2<false><<<256, 256, 0, stream>>>(ws + O_H1, ws + O_W2F, (const u16*)(ws + O_X1T), (u32*)(ws + O_XP2),
                                          (float*)(ws + O_ST2), nullptr);
  k_conv<true><<<256, 256, 0, stream>>>((const u32*)(ws + O_XP2), (const float*)(ws + O_ST2), ws + O_KT,
                                        (const float*)(ws + O_KS), dsk, (u16*)(ws + O_X3T));
  k_gemm1<<<256, 512, 0, stream>>>((const u16*)(ws + O_X3T), ws + O_W1F, (const float*)(ws + O_CS), ws + O_H2);
  k_gemm2<true><<<256, 256, 0, stream>>>(ws + O_H2, ws + O_W2F, (const u16*)(ws + O_X3T), nullptr, nullptr,
                                         (u16*)d_out);
}
```

```cpp
#include <hip/hip_runtime.h>
#include <stdint.h>

typedef unsigned short u16;
typedef uint32_t u32;
typedef unsigned long long u64;
typedef __attribute__((ext_vector_type(2))) float f32x2;
typedef __attribute__((ext_vector_type(4))) float f32x4;
typedef __attribute__((ext_vector_type(16))) float f32x16;
typedef __attribute__((ext_vector_type(2))) _Float16 f16x2;
typedef __attribute__((ext_vector_type(8))) _Float16 f16x8;
typedef __attribute__((ext_vector_type(2))) __bf16 bf16x2;
typedef __attribute__((ext_vector_type(8))) __bf16 bf16x8;
typedef __attribute__((ext_vector_type(4))) short s16x4;
typedef __attribute__((ext_vector_type(2))) u32 u32x2;
typedef __attribute__((ext_vector_type(4))) u32 u32x4;
typedef __amdgpu_buffer_rsrc_t rsrc_t;
#define LDSP __attribute__((address_space(3)))
#define GLBP __attribute__((address_space(1)))
#define DEVI __device__ __forceinline__

constexpr int SL = 2048, SD = 512, NM = 128, DF = 1376, FT = 86;
constexpr int KCOPY = 2080;
constexpr int KCH_B = 2 * KCOPY * 2;
constexpr u32 O_EP = 0;
constexpr u32 O_FW = 4096;
constexpr u32 O_F1 = O_FW + 32768;
constexpr u32 O_F3 = O_F1 + 65536;
constexpr u32 O_G0 = O_F3 + 32768;
constexpr u32 O_G1 = O_G0 + 32768;
constexpr u32 O_S2 = O_G1 + 65536;
constexpr u32 O_FH = O_S2 + 16384;
constexpr u32 FH_SZ = 64 * FT * 128;
constexpr u32 O_KT = O_FH + 2 * FH_SZ;
constexpr u32 KT_SZ = 24576;
constexpr u32 O_W1F = O_KT + 256 * KT_SZ;
constexpr u32 O_W2F = O_W1F + FT * 32768;
constexpr u32 O_XT = O_W2F + 8 * FT * 2048 + 4096;
constexpr u32 O_H = O_XT + 2 * (2u << 20);
constexpr u32 H_SZ = 64 * FT * 1024;
constexpr u32 O_XP = O_H + 2 * H_SZ;
constexpr u32 O_XG = O_XP + (2u << 20);
constexpr u32 WS_END = O_XG + (4u << 20);
constexpr int L_Y = 0;
constexpr int TBP = 1088, L_TB = 0, TB_CH = 25 * TBP;
constexpr int L_RS = 2 * TB_CH;
constexpr int L_TAB = L_RS + 256;
constexpr int L_TH = L_TAB + 8 * 3072;
constexpr int L_K = 98304;
constexpr int L_U = L_K + 24576, U_CH = (240 + 2048) * 2;
constexpr int L_R3 = 98304;
constexpr int L_ST = 155648;
constexpr int L_CS = L_ST + 4096;
constexpr int L_P = 0;
constexpr int L_SZ = 163840;
constexpr int SPIN_MAX = 1 << 16;

DEVI float bflo(u32 v) { return __uint_as_float(v << 16); }
DEVI float bfhi(u32 v) { return __uint_as_float(v & 0xffff0000u); }
DEVI float b2f(u16 v) { return __uint_as_float(((u32)v) << 16); }
DEVI u32 pkbf(float a, float b) { bf16x2 r = __builtin_convertvector((f32x2){a, b}, bf16x2); return __builtin_bit_cast(u32, r); }
DEVI u32 pkf16(float a, float b) { f16x2 r = __builtin_convertvector((f32x2){a, b}, f16x2); return __builtin_bit_cast(u32, r); }
DEVI float rbf(float a) { return bflo(pkbf(a, 0.f)); }
template <int C> DEVI float dppf(float v) { return __uint_as_float(__builtin_amdgcn_update_dpp(0u, __float_as_uint(v), C, 0xf, 0xf, true)); }
template <int C> DEVI u32 dppu(u32 v) { return __builtin_amdgcn_update_dpp(0u, v, C, 0xf, 0xf, true); }
DEVI float sw16(float v) { auto r = __builtin_amdgcn_permlane16_swap(__float_as_uint(v), __float_as_uint(v), false, false); return __uint_as_float(r[0]) + __uint_as_float(r[1]); }
DEVI float sw32(float a, float b) { auto r = __builtin_amdgcn_permlane32_swap(__float_as_uint(a), __float_as_uint(b), false, false); return __uint_as_float(r[0]) + __uint_as_float(r[1]); }
DEVI float wsum(float v) { v += dppf<0xB1>(v); v += dppf<0x4E>(v); v += dppf<0x141>(v); v += dppf<0x140>(v); v = sw16(v); return sw32(v, v); }
DEVI float wmax(float v) {
  v = fmaxf(v, dppf<0xB1>(v)); v = fmaxf(v, dppf<0x4E>(v)); v = fmaxf(v, dppf<0x141>(v)); v = fmaxf(v, dppf<0x140>(v));
  auto r = __builtin_amdgcn_permlane16_swap(__float_as_uint(v), __float_as_uint(v), false, false);
  v = fmaxf(__uint_as_float(r[0]), __uint_as_float(r[1]));
  r = __builtin_amdgcn_permlane32_swap(__float_as_uint(v), __float_as_uint(v), false, false);
  return fmaxf(__uint_as_float(r[0]), __uint_as_float(r[1]));
}
DEVI float dot2(u32 a, u32 b, float c) { return __builtin_amdgcn_fdot2_f32_bf16(__builtin_bit_cast(bf16x2, a), __builtin_bit_cast(bf16x2, b), c, false); }

#define WAITV(n) asm volatile("s_waitcnt vmcnt(" #n ")" ::: "memory")
#define WAITL0() asm volatile("s_waitcnt lgkmcnt(0)" ::: "memory")
#define SCHED() __builtin_amdgcn_sched_barrier(0)
#define BAR() do { asm volatile("s_waitcnt lgkmcnt(0)\n\ts_barrier" ::: "memory"); } while (0)

#define STAMP(k, i)

DEVI u32x4 ld16(rsrc_t rs, u32 voff, u32 soff) { return __builtin_amdgcn_raw_buffer_load_b128(rs, voff, soff, 16); }
DEVI u32 ld4(rsrc_t rs, u32 voff, u32 soff) { return __builtin_amdgcn_raw_buffer_load_b32(rs, voff, soff, 16); }
DEVI u16 ld2(rsrc_t rs, u32 voff, u32 soff) { return __builtin_amdgcn_raw_buffer_load_b16(rs, voff, soff, 16); }
DEVI void st16(rsrc_t rs, u32 off, u32x4 v) { __builtin_amdgcn_raw_buffer_store_b128(v, rs, off, 0, 16); }
DEVI void st4(rsrc_t rs, u32 voff, u32 soff, u32 v) { __builtin_amdgcn_raw_buffer_store_b32(v, rs, voff, soff, 16); }
template <int N> DEVI void dma(const char* g, u32 lds, u32 lane16) {
  if constexpr (N >= 4) {
    asm volatile("s_mov_b32 m0, %0\n\ts_nop 0\n\tglobal_load_lds_dwordx4 %1, %2 sc1\n\tglobal_load_lds_dwordx4 %1, %2 offset:1024 sc1\n\t"
                 "global_load_lds_dwordx4 %1, %2 offset:2048 sc1\n\tglobal_load_lds_dwordx4 %1, %2 offset:3072 sc1"
                 :: "s"(lds), "v"(lane16), "s"(g) : "memory");
    dma<N - 4>(g + 4096, lds + 4096, lane16);
  } else if constexpr (N == 3) {
    asm volatile("s_mov_b32 m0, %0\n\ts_nop 0\n\tglobal_load_lds_dwordx4 %1, %2 sc1\n\tglobal_load_lds_dwordx4 %1, %2 offset:1024 sc1\n\t"
                 "global_load_lds_dwordx4 %1, %2 offset:2048 sc1"
                 :: "s"(lds), "v"(lane16), "s"(g) : "memory");
  } else if constexpr (N == 2) {
    asm volatile("s_mov_b32 m0, %0\n\ts_nop 0\n\tglobal_load_lds_dwordx4 %1, %2 sc1\n\tglobal_load_lds_dwordx4 %1, %2 offset:1024 sc1"
                 :: "s"(lds), "v"(lane16), "s"(g) : "memory");
  }
}
DEVI bool none(bool p) { return __builtin_amdgcn_ballot_w64(p) == 0; }
constexpr u32 OOB = 0x80000000u;
DEVI void poll(rsrc_t rs, u32 f, u32 voff, u32 soff, u32 tag) {
#pragma unroll 1
  for (int s = 0; s < SPIN_MAX; ++s) {
    if (none(f != tag)) break;
    asm volatile("" ::: "memory");
    f = ld4(rs, voff, soff);
  }
  asm volatile("" ::: "memory");
}
DEVI void poll(rsrc_t rs, u32 voff, u32 soff, u32 tag) {
  asm volatile("" ::: "memory");
  poll(rs, ld4(rs, voff, soff), voff, soff, tag);
}
DEVI void delay(int n) { for (int i = 0; i < n; ++i) __builtin_amdgcn_s_sleep(2); }

DEVI f32x2 cexp(float are, float aim, float p) {
  const float e = __builtin_amdgcn_exp2f(are * p), ph = __builtin_amdgcn_fractf(aim * p);
  return (f32x2){__builtin_amdgcn_cosf(ph), __builtin_amdgcn_sinf(ph)} * (f32x2){e, e};
}
DEVI f32x2 cmul(f32x2 a, f32x2 b) {
  f32x2 r;
  asm("v_pk_mul_f32 %0, %1, %2 op_sel:[0,0] op_sel_hi:[0,1]\n\t"
      "v_pk_fma_f32 %0, %1, %2, %0 op_sel:[1,1,0] op_sel_hi:[1,0,1] neg_lo:[1,0,0]" : "=&v"(r) : "v"(a), "v"(b));
  return r;
}
DEVI f16x8 cmul4(f32x4 a0, f32x4 a1, f32x4 b0, f32x4 b1) {
  f32x2 r0, r1, r2, r3;
  asm("v_pk_mul_f32 %0, %4, %8 op_sel:[0,0] op_sel_hi:[0,1]\n\tv_pk_mul_f32 %1, %5, %9 op_sel:[0,0] op_sel_hi:[0,1]\n\t"
      "v_pk_mul_f32 %2, %6, %10 op_sel:[0,0] op_sel_hi:[0,1]\n\tv_pk_mul_f32 %3, %7, %11 op_sel:[0,0] op_sel_hi:[0,1]\n\t"
      "v_pk_fma_f32 %0, %4, %8, %0 op_sel:[1,1,0] op_sel_hi:[1,0,1] neg_lo:[1,0,0]\n\t"
      "v_pk_fma_f32 %1, %5, %9, %1 op_sel:[1,1,0] op_sel_hi:[1,0,1] neg_lo:[1,0,0]\n\t"
      "v_pk_fma_f32 %2, %6, %10, %2 op_sel:[1,1,0] op_sel_hi:[1,0,1] neg_lo:[1,0,0]\n\t"
      "v_pk_fma_f32 %3, %7, %11, %3 op_sel:[1,1,0] op_sel_hi:[1,0,1] neg_lo:[1,0,0]"
      : "=&v"(r0), "=&v"(r1), "=&v"(r2), "=&v"(r3)
      : "v"(a0.xy), "v"(a0.zw), "v"(a1.xy), "v"(a1.zw), "v"(b0.xy), "v"(b0.zw), "v"(b1.xy), "v"(b1.zw));
  return __builtin_bit_cast(f16x8, (u32x4){pkf16(r0[0], r0[1]), pkf16(r1[0], r1[1]), pkf16(r2[0], r2[1]), pkf16(r3[0], r3[1])});
}
DEVI s16x4 trread(u32 a) { return __builtin_amdgcn_ds_read_tr16_b64_v4i16((LDSP s16x4*)(size_t)a); }
DEVI bf16x8 trfrag(u32 a) {
  const s16x4 lo = trread(a), hi = trread(a + 256);
  return __builtin_bit_cast(bf16x8, __builtin_shufflevector(lo, hi, 0, 1, 2, 3, 4, 5, 6, 7));
}
template <class T> DEVI T lds_ld(u32 a) { return *(const LDSP T*)(size_t)a; }
template <class T> DEVI void lds_st(u32 a, T v) { *(LDSP T*)(size_t)a = v; }

constexpr int PD = 4;
DEVI void g1_pre(bf16x8 (&fr)[PD], u32 xb) {
#pragma unroll
  for (int i = 0; i < PD; ++i) fr[i] = trfrag(xb + i * 1024);
}
template <bool NEXT> DEVI void g1_chain(f32x16& acc, const bf16x8 (&W)[32], bf16x8 (&fr)[PD], u32 xb) {
  asm volatile("" : "+v"(xb));
#pragma unroll
  for (int e = 0; e < 16; ++e) acc[e] = 0.f;
  SCHED();
#pragma unroll
  for (int i = 0; i < 32; ++i) {
    acc = __builtin_amdgcn_mfma_f32_32x32x16_bf16(W[i], fr[i % PD], acc, 0, 0, 0);
    if (i + PD < 32 || NEXT) fr[i % PD] = trfrag(xb + (i + PD) * 1024);
    SCHED();
  }
}
DEVI u32x4 g1_epi(const f32x16& acc, const u32x4& g, u32 cssrc) {
  f32x4 cs[4];
#pragma unroll
  for (int q = 0; q < 4; ++q) cs[q] = lds_ld<f32x4>(cssrc + q * 16);
  const float nm = __uint_as_float(g[0]), v = __uint_as_float(g[2]);
  const float rs = __builtin_amdgcn_rsqf(v);
  const float k0 = rs * -2.302208198f, k1 = rs * rs * rs * -0.102943239f;
  u32x4 o;
#pragma unroll
  for (int q = 0; q < 2; ++q) {
    float g0 = cs[q][0], g1 = cs[q][1], g2 = cs[q][2], g3 = cs[q][3];
    float u0 = cs[2 + q][0], u1 = cs[2 + q][1], u2 = cs[2 + q][2], u3 = cs[2 + q][3];
    float t0, t1, t2, t3;
    asm("v_fma_f32 %0, %20, %0, %12\n\tv_fma_f32 %1, %20, %1, %13\n\tv_fma_f32 %2, %20, %2, %14\n\tv_fma_f32 %3, %20, %3, %15\n\t"
        "v_mul_f32 %8, %0, %0\n\tv_mul_f32 %9, %1, %1\n\tv_mul_f32 %10, %2, %2\n\tv_mul_f32 %11, %3, %3\n\t"
        "v_fma_f32 %8, %8, %22, %21\n\tv_fma_f32 %9, %9, %22, %21\n\tv_fma_f32 %10, %10, %22, %21\n\tv_fma_f32 %11, %11, %22, %21\n\t"
        "v_mul_f32 %8, %0, %8\n\tv_mul_f32 %9, %1, %9\n\tv_mul_f32 %10, %2, %10\n\tv_mul_f32 %11, %3, %11\n\t"
        "v_exp_f32 %8, %8\n\tv_exp_f32 %9, %9\n\tv_exp_f32 %10, %10\n\tv_exp_f32 %11, %11\n\t"
        "v_fma_f32 %4, %20, %4, %16\n\tv_fma_f32 %5, %20, %5, %17\n\tv_fma_f32 %6, %20, %6, %18\n\tv_fma_f32 %7, %20, %7, %19\n\t"
        "v_fma_f32 %8, %8, %23, %23\n\tv_fma_f32 %9, %9, %23, %23\n\tv_fma_f32 %10, %10, %23, %23\n\tv_fma_f32 %11, %11, %23, %23\n\t"
        "v_rcp_f32 %8, %8\n\tv_rcp_f32 %9, %9\n\tv_rcp_f32 %10, %10\n\tv_rcp_f32 %11, %11\n\t"
        "v_mul_f32 %0, %0, %4\n\tv_mul_f32 %1, %1, %5\n\tv_mul_f32 %2, %2, %6\n\tv_mul_f32 %3, %3, %7\n\t"
        "v_mul_f32 %0, %0, %8\n\tv_mul_f32 %1, %1, %9\n\tv_mul_f32 %2, %2, %10\n\tv_mul_f32 %3, %3, %11"
        : "+v"(g0), "+v"(g1), "+v"(g2), "+v"(g3), "+v"(u0), "+v"(u1), "+v"(u2), "+v"(u3), "=&v"(t0), "=&v"(t1), "=&v"(t2), "=&v"(t3)
        : "v"(acc[4 * q]), "v"(acc[4 * q + 1]), "v"(acc[4 * q + 2]), "v"(acc[4 * q + 3]),
          "v"(acc[8 + 4 * q]), "v"(acc[9 + 4 * q]), "v"(acc[10 + 4 * q]), "v"(acc[11 + 4 * q]), "v"(nm), "v"(k0), "v"(k1), "v"(v));
    o[2 * q] = pkbf(g0, g1);
    o[2 * q + 1] = pkbf(g2, g3);
  }
  return o;
}
#define MFMA_DRAIN(a) asm volatile("s_nop 15\n\ts_nop 3" : "+v"(a))

__global__ __launch_bounds__(512, 2) void k_all(const u16* __restrict__ u, const u16* __restrict__ logA,
                                                const u16* __restrict__ Aim, const u16* __restrict__ Cre,
                                                const u16* __restrict__ Cim, const u16* __restrict__ logdt,
                                                const u16* __restrict__ dsk, const u16* __restrict__ wg,
                                                const u16* __restrict__ wu, const u16* __restrict__ wd,
                                                char* ws, u16* __restrict__ out) {
  __shared__ __attribute__((aligned(16))) char sm[L_SZ];
  const int tid0 = threadIdx.x, w0 = __builtin_amdgcn_readfirstlane(tid0 >> 6), b = blockIdx.x;
  const int n0 = (b & 7) * 32 + (b >> 3);
  asm volatile("" :: "v"((LDSP char*)sm) : "memory");
  STAMP(0, 0);
  const rsrc_t rs = __builtin_amdgcn_make_buffer_rsrc(ws, 0, WS_END, 0x00020000);
#define PHASE_IDS()                                                                               \
  int tid = tid0, w = w0, n = n0;                                                                 \
  asm volatile("" : "+v"(tid));                                                                   \
  asm volatile("" : "+s"(w), "+s"(n));                                                            \
  const int lane = tid & 63, r32 = lane & 31, hh = lane >> 5, ch = w >> 2;                        \
  const u32 lane16 = lane * 16;                                                                   \
  const int mt = n / 11, sl = n - mt * 11, ntile = n < 253 ? (mt % 5 == 2 ? 2 : 3) : 0;           \
  const int ftw = sl * 8 + w, ftc = ftw < FT ? ftw : FT - 1;                                      \
  const int nt = n & 7, tt = w >> 2, kq = w & 3, T2 = (n >> 3) + 32 * tt;                         \
  (void)r32; (void)hh; (void)ch; (void)lane16; (void)ntile; (void)ftc; (void)nt; (void)kq; (void)T2

  u32 tag, dsk2;
  float ks0, ks1;
  u32x4 xraw;
  float mean[4], rstd[4];
  {
    PHASE_IDS();
    const int pidx = n * 2 * NM + (tid & 255);
    const u16 pla = logA[pidx], pai = Aim[pidx], pcr = Cre[pidx], pci = Cim[pidx];
    const u32x4 row = *(const u32x4*)(u + (size_t)(4 * (8 * (b >> 2) + w) + (b & 3)) * SD + lane * 8);
    const u32x4 til = *(const u32x4*)(u + (size_t)((b >> 2) * 32 + r32) * SD + (b & 3) * 128 + (w * 2 + hh) * 8);
    const u32 ep = ld4(rs, O_EP, 0);
    const u32 ldt2 = *(const u32*)(logdt + 2 * n);
    dsk2 = *(const u32*)(dsk + 2 * n);
    SCHED();
    __builtin_amdgcn_s_barrier();
    SCHED();
    u32 wt[2][8], wo[2], wo2[2];
#pragma unroll
    for (int k = 0; k < 2; ++k) {
      const int task = b * 8 + w + 2048 * k;
      const bool isg = task < 1376;
      const int mk = task / 43, fp = task - mk * 43, mat = mk >> 4, kp = mk & 15;
      const int t2 = task - 1376, ft = t2 >> 3, nb = t2 & 7;
      const rsrc_t wr = __builtin_amdgcn_make_buffer_rsrc((void*)(isg ? (mat ? wu : wg) : wd), 0, task < 2064 ? SD * DF * 2 : 0, 0x00020000);
      const u32 so = isg ? (kp * 32 * DF + fp * 32) * 2 : ((32 * (ft >> 1) + (ft & 1)) * SD + nb * 64) * 2;
      const u32 s1 = isg ? DF * 2 : 2 * SD * 2, s4 = isg ? 4 * DF * 2 : 16 * SD * 2;
      const u32 vo = isg ? (lane >> 4) * (8 * DF * 2) + (lane & 15) * 4 : hh * (8 * SD * 2) + r32 * 4;
#pragma unroll
      for (int j = 0; j < 8; ++j) wt[k][j] = __builtin_amdgcn_raw_buffer_load_b32(wr, vo, so + (j & 3) * s1 + (j >> 2) * s4, 0);
      const u32 sb = task >= 2064 ? 0x80000000u : isg ? O_W1F + (fp * 64 + kp * 2) * 1024 + mat * 256 : O_W2F + (nb * FT + ft) * 2048;
      wo[k] = sb + (isg ? (lane >> 4) * 512 + (lane & 15) * 16 : lane16);
      wo2[k] = isg ? 32768 : 1024;
    }
    u32 kix[9];
#pragma unroll
    for (int j = 0; j < 9; ++j) {
      const int l = 2047 - ((tid & 255) * 8 + j), lc = l < 0 ? 0 : l;
      kix[j] = L_TH + ch * 1312 + 2 * (lc < 128 ? lc : (int)(pkbf((float)lc, 0.f) & 0xffffu) - 0x4300 + 128);
    }
    SCHED();
    tag = __builtin_amdgcn_readfirstlane(ep) + 1u;
    STAMP(0, 1);
    {
      const bool odd = lane & 1;
      const u32 r0 = dppu<0xB1>(odd ? til[0] : til[2]), r1 = dppu<0xB1>(odd ? til[1] : til[3]);
      const u32 pr = (b & 3) * 64 + (w * 2 + hh) * 4 + (odd ? 2 : 0);
      const u32 o = O_XG + pr * 16384 + ((r32 >> 1) & 1) * 8192 + ((b >> 2) * 8 + (r32 >> 2)) * 16;
      st16(rs, o, (u32x4){odd ? r0 : til[0], tag, odd ? til[2] : r0, tag});
      st16(rs, o + 16384, (u32x4){odd ? r1 : til[1], tag, odd ? til[3] : r1, tag});
      float s = 0.f;
#pragma unroll
      for (int c = 0; c < 4; ++c) s = dot2(row[c], 0x3f803f80u, s);
      const float m = wsum(s) * (1.f / SD);
      float q = 0.f;
#pragma unroll
      for (int c = 0; c < 4; ++c) {
        const float a = bflo(row[c]) - m, d = bfhi(row[c]) - m;
        q += a * a + d * d;
      }
      q = wsum(q);
      if (lane == 0) lds_st<f32x2>(L_RS + w * 8, (f32x2){m, __builtin_amdgcn_rsqf(q * (1.f / SD) + 1e-5f)});
    }
    const float dt0 = rbf(__expf(bflo(ldt2))), dt1 = rbf(__expf(bfhi(ldt2)));
    {
      const float dt = (w & 2) ? dt1 : dt0;
      const float ar = -rbf(__expf(b2f(pla))) * dt * 1.44269504088896f, ai = (b2f(pai) * dt) * 0.159154943091895f;
      const u32 tb = L_TB + ((w >> 1) & 1) * TB_CH + (tid & 127) * 8;
      if (w < 4) {
        const f32x2 e1 = cexp(ar, ai, 1.f), e256 = cexp(ar, ai, 256.f), e1024 = cexp(ar, ai, 1024.f);
        const f32x2 e2 = cmul(e1, e1), e512 = cmul(e256, e256), e768 = cmul(e512, e256);
        lds_st<f32x2>(tb + 0 * TBP, (f32x2){1.f, 0.f});
        lds_st<f32x2>(tb + 1 * TBP, e1);
        lds_st<f32x2>(tb + 2 * TBP, e2);
        lds_st<f32x2>(tb + 3 * TBP, cmul(e2, e1));
        lds_st<f32x2>(tb + 4 * TBP, e256);
        lds_st<f32x2>(tb + 5 * TBP, cmul(e256, e2));
        lds_st<f32x2>(tb + 6 * TBP, e512);
        lds_st<f32x2>(tb + 7 * TBP, e768);
        lds_st<f32x2>(tb + 8 * TBP, e1024);
        lds_st<f32x2>(tb + 9 * TBP, cmul(e1024, e256));
        lds_st<f32x2>(tb + 10 * TBP, cmul(e1024, e512));
        lds_st<f32x2>(tb + 11 * TBP, cmul(e1024, e768));
        lds_st<f32x2>(tb + 12 * TBP, cexp(ar, ai, 2044.f));
      } else {
        const float cr = b2f(pcr), ci = b2f(pci);
        const float mx = wmax(fmaxf(fabsf(cr), fabsf(ci)));
        const int ce = __builtin_amdgcn_frexp_expf(mx);
        if (lane == 0) lds_st<int>(L_RS + 64 + kq * 4, ce);
        const f32x2 c = {ldexpf(cr, 14 - ce), ldexpf(-ci, 14 - ce)};
        const f32x2 e4 = cexp(ar, ai, 4.f), e16 = cexp(ar, -ai, 16.f), e64 = cexp(ar, -ai, 64.f);
        const f32x2 e4s = e4 * 16384.f, e8s = cmul(e4s, e4), e32 = cmul(e16, e16), c64 = cmul(c, e64), c128 = cmul(c64, e64);
        lds_st<f32x2>(tb + 13 * TBP, (f32x2){16384.f, 0.f});
        lds_st<f32x2>(tb + 14 * TBP, e4s);
        lds_st<f32x2>(tb + 15 * TBP, e8s);
        lds_st<f32x2>(tb + 16 * TBP, cmul(e8s, e4));
        lds_st<f32x2>(tb + 17 * TBP, (f32x2){1.f, 0.f});
        lds_st<f32x2>(tb + 18 * TBP, e16);
        lds_st<f32x2>(tb + 19 * TBP, e32);
        lds_st<f32x2>(tb + 20 * TBP, cmul(e32, e16));
        lds_st<f32x2>(tb + 21 * TBP, c);
        lds_st<f32x2>(tb + 22 * TBP, c64);
        lds_st<f32x2>(tb + 23 * TBP, c128);
        lds_st<f32x2>(tb + 24 * TBP, cmul(c128, e64));
      }
    }
    BAR();
    STAMP(0, 2);
    if (tid < 8) {
      const f32x2 v = lds_ld<f32x2>(L_RS + tid * 8);
      st16(rs, O_G0 + (b & 3) * 8192 + ((b >> 2) * 8 + tid) * 16, (u32x4){__float_as_uint(v[0]), tag, __float_as_uint(v[1]), tag});
    }
    if (b < 128 && tid >= 64 && tid < 72) st16(rs, O_S2 + b * 128 + (tid - 64) * 16, (u32x4){0, 0, 0, 0});
#pragma unroll
    for (int k = 0; k < 2; ++k) {
      u32x4 lo, hi;
#pragma unroll
      for (int j = 0; j < 4; ++j) {
        lo[j] = __builtin_amdgcn_perm(wt[k][2 * j + 1], wt[k][2 * j], 0x05040100u);
        hi[j] = __builtin_amdgcn_perm(wt[k][2 * j + 1], wt[k][2 * j], 0x07060302u);
      }
      st16(rs, wo[k], lo);
      st16(rs, wo[k] + wo2[k], hi);
    }
    {
      const int r = lane & 15, h = lane >> 4;
      const u32 tb = L_TB + ch * TB_CH + (kq * 32 + h * 4) * 8;
      const u32 aL = tb + (r & 3) * TBP, aM = tb + (r >> 2) * TBP, a2x = r == 4 ? tb + 4 * TBP : aL;
      f32x4 a0 = {0, 0, 0, 0}, a1 = {0, 0, 0, 0}, a2 = {0, 0, 0, 0};
#pragma unroll
      for (int s = 0; s < 2; ++s) {
        auto L = [&](u32 a) { return lds_ld<f32x4>(a + s * 128); };
        const f32x4 m0 = L(aM + 13 * TBP), m1 = L(aM + 13 * TBP + 16);
        const f16x8 bb = cmul4(L(aL + 17 * TBP), L(aL + 17 * TBP + 16), L(aM + 21 * TBP), L(aM + 21 * TBP + 16));
        a0 = __builtin_amdgcn_mfma_f32_16x16x32_f16(cmul4(L(aL), L(aL + 16), m0, m1), bb, a0, 0, 0, 0);
        a1 = __builtin_amdgcn_mfma_f32_16x16x32_f16(cmul4(L(aL + 4 * TBP), L(aL + 4 * TBP + 16), m0, m1), bb, a1, 0, 0, 0);
        a2 = __builtin_amdgcn_mfma_f32_16x16x32_f16(cmul4(L(a2x + 8 * TBP), L(a2x + 8 * TBP + 16), m0, m1), bb, a2, 0, 0, 0);
      }
      const u32 T = L_TAB + w * 3072 + lane16;
      lds_st<f32x4>(T, a0);
      lds_st<f32x4>(T + 1024, a1);
      lds_st<f32x4>(T + 2048, a2);
    }
    STAMP(0, 3);
    WAITV(0);
    BAR();
    STAMP(0, 4);
    if (tid == 0) st4(rs, O_FW + b * 128, 0, tag);
    u32x4 g[6];
#pragma unroll
    for (int i = 0; i < 4; ++i) g[i] = ld16(rs, tid * 16, O_G0 + i * 8192);
#pragma unroll
    for (int i = 0; i < 2; ++i) g[4 + i] = ld16(rs, tid * 16, O_XG + n * 16384 + i * 8192);
    {
      const __attribute__((ext_vector_type(4))) int ce = lds_ld<__attribute__((ext_vector_type(4))) int>(L_RS + 64);
      const int c0 = max(ce[0], ce[1]), c1 = max(ce[2], ce[3]);
      ks0 = __uint_as_float(__builtin_amdgcn_readfirstlane(__float_as_uint(ldexpf(dt0, c0 - 7))));
      ks1 = __uint_as_float(__builtin_amdgcn_readfirstlane(__float_as_uint(ldexpf(dt1, c1 - 7))));
      if (kq < 3) {
        const float fa = ldexpf(1.f, (ch ? ce[2] - c1 : ce[0] - c0) - 20), fb = ldexpf(1.f, (ch ? ce[3] - c1 : ce[1] - c0) - 20);
        const u32 T = L_TAB + ch * 4 * 3072 + kq * 1024 + lane16;
        const f32x4 v = (lds_ld<f32x4>(T) + lds_ld<f32x4>(T + 3072)) * fa + (lds_ld<f32x4>(T + 2 * 3072) + lds_ld<f32x4>(T + 3 * 3072)) * fb;
        const u32 p0 = pkf16(v[0], v[1]), p1 = pkf16(v[2], v[3]);
        const int j = lane & 15, h = lane >> 4;
        const u32 H = L_TH + ch * 1312;
        if (kq == 0) {
          lds_st<u32x2>(H + 2 * (16 * j + 4 * h), (u32x2){p0, p1});
        } else if (kq == 1) {
          lds_st<u32>(H + 2 * (256 + 8 * j + 2 * h), p0);
          lds_st<u16>(H + 2 * (384 + 4 * j + h), (u16)p1);
          lds_st<u16>(H + 2 * (448 + 4 * j + h), (u16)(p1 >> 16));
        } else if (!(h & 1)) {
          const u32 a = H + 2 * (512 + 2 * j + (h >> 1));
          lds_st<u16>(a, (u16)p0);
          lds_st<u16>(a + 64, (u16)(p0 >> 16));
          lds_st<u16>(a + 128, (u16)p1);
          lds_st<u16>(a + 192, (u16)(p1 >> 16));
        } else if (lane == 16) {
          lds_st<u16>(H + 2 * 640, (u16)p0);
        }
      }
    }
    BAR();
    u32x4 c0, c1;
    const int i = tid & 255;
    {
      u32 kv[9];
#pragma unroll
      for (int j = 0; j < 9; ++j) kv[j] = lds_ld<u16>(kix[j]);
      if (i == 255) kv[8] = 0;
      c0 = (u32x4){kv[0] | (kv[1] << 16), kv[2] | (kv[3] << 16), kv[4] | (kv[5] << 16), kv[6] | (kv[7] << 16)};
      c1 = (u32x4){kv[1] | (kv[2] << 16), kv[3] | (kv[4] << 16), kv[5] | (kv[6] << 16), kv[7] | (kv[8] << 16)};
      lds_st<u32x4>(L_K + ch * KCH_B + i * 16, c0);
      lds_st<u32x4>(L_K + ch * KCH_B + i * 16 + KCOPY * 2, c1);
      if (i < 8) lds_st<u32x4>(L_K + ch * KCH_B + (i >> 2) * KCOPY * 2 + 4096 + (i & 3) * 16, (u32x4){0, 0, 0, 0});
    }
    STAMP(0, 5);
    u32 fw = tag;
    if (w < 4) fw = ld4(rs, O_FW + tid * 128, 0);
  #pragma unroll 1
  for (int s = 0; s < SPIN_MAX; ++s) {
      u32 bad = 0;
#pragma unroll
      for (int k = 0; k < 6; ++k) bad |= (g[k][1] ^ tag) | (g[k][3] ^ tag);
      if (none(bad != 0)) break;
      asm volatile("" ::: "memory");
#pragma unroll
      for (int k = 0; k < 4; ++k) g[k] = ld16(rs, tid * 16, O_G0 + k * 8192);
#pragma unroll
      for (int k = 0; k < 2; ++k) g[4 + k] = ld16(rs, tid * 16, O_XG + n * 16384 + k * 8192);
    }
#pragma unroll
    for (int k = 0; k < 4; ++k) { mean[k] = __uint_as_float(g[k][0]); rstd[k] = __uint_as_float(g[k][2]); }
    xraw = (u32x4){g[4][0], g[4][2], g[5][0], g[5][2]};
    STAMP(0, 6);
    poll(rs, fw, O_FW + tid * 128, 0, tag);
    {
      const u32 o = O_KT + b * KT_SZ + ch * KCH_B;
      st16(rs, o + i * 16, c0);
      st16(rs, o + i * 16 + KCOPY * 2, c1);
      if (i < 8) st16(rs, o + (i >> 2) * KCOPY * 2 + 4096 + (i & 3) * 16, (u32x4){0, 0, 0, 0});
    }
    STAMP(0, 7);
  }

  bf16x8 W[32];
#pragma unroll 1
  for (int layer = 0; layer < 2; ++layer) {
    const u32 xt_off = O_XT + layer * (2u << 20), h_off = O_H + layer * H_SZ, fh_off = O_FH + layer * FH_SZ;
    float base[2][4];
    {
      PHASE_IDS();
      const float d0 = bflo(dsk2), d1 = bfhi(dsk2);
      float u0[4], u1[4];
#pragma unroll
      for (int i = 0; i < 4; ++i) {
        const float x0 = bflo(xraw[i]), x1 = bfhi(xraw[i]);
        u0[i] = (x0 - mean[i]) * rstd[i]; u1[i] = (x1 - mean[i]) * rstd[i];
        base[0][i] = fmaf(u0[i], d0, x0); base[1][i] = fmaf(u1[i], d1, x1);
      }
      lds_st<u32x2>(L_U + 480 + tid * 8, (u32x2){pkf16(u0[0], u0[1]), pkf16(u0[2], u0[3])});
      lds_st<u32x2>(L_U + U_CH + 480 + tid * 8, (u32x2){pkf16(u1[0], u1[1]), pkf16(u1[2], u1[3])});
      u32 z = 0;
      asm volatile("" : "+v"(z));
      if (tid < 60) lds_st<u32x4>(L_U + (tid >= 30 ? U_CH + (tid - 30) * 16 : tid * 16), (u32x4){z, z, z, z});
      asm volatile("" : "+v"(base[0][0]), "+v"(base[0][1]), "+v"(base[0][2]), "+v"(base[0][3]),
                        "+v"(base[1][0]), "+v"(base[1][1]), "+v"(base[1][2]), "+v"(base[1][3]));
      const int r = lane & 15, h = lane >> 4, rho0 = kq * 2;
      f32x4 acc[8];
#pragma unroll
      for (int g = 0; g < 8; ++g) acc[g] = (f32x4){0, 0, 0, 0};
      u32 kb[4];
#pragma unroll
      for (int p = 0; p < 4; ++p) {
        kb[p] = L_K + ch * KCH_B + 4 * (((2031 - r + 8 * h) >> 1) + ((r & 1) ? 0 : KCOPY / 2)) - 64 * rho0 - 64 * (16 * p + 9);
        asm volatile("" : "+v"(kb[p]));
      }
      u32 ub = L_U + ch * U_CH + 2 * (224 + 16 * r + 8 * h) - 64 * rho0 - 64;
      asm volatile("" : "+v"(ub));
      auto ldA = [&](int st) {
        const u32 a = kb[(st & 7) >> 1] + 64 * (9 - (8 * (st & 1) + (st >> 3)));
        return __builtin_bit_cast(f16x8, (u32x4){lds_ld<u32>(a), lds_ld<u32>(a + 4), lds_ld<u32>(a + 8), lds_ld<u32>(a + 12)});
      };
      auto ldB = [&](int rr, int q) { return lds_ld<f16x8>(ub + 64 * (8 * q + 1 - rr)); };
      f16x8 A[3], B[8];
      BAR();
      STAMP(1 + 3 * layer, 0);
      A[0] = ldA(0);
#pragma unroll
      for (int q = 7; q >= 0; --q) B[q] = ldB(0, q);
      A[1] = ldA(1);
      A[2] = ldA(2);
      SCHED();
#pragma unroll
      for (int st = 0; st < 16; ++st) {
        const int a = st & 7;
#pragma unroll
        for (int q = 7 - a; q >= 0; --q)
          acc[a + q] = __builtin_amdgcn_mfma_f32_16x16x32_f16(A[st % 3], B[q], acc[a + q], 0, 0, 0);
        if (st + 3 < 16) A[st % 3] = ldA(st + 3);
        if (st < 8) B[7 - a] = ldB(1, 7 - a);
        if (a < 4 && layer == 0) {
          const int i = (st >> 3) * 8 + a * 2;
          W[i] = __builtin_bit_cast(bf16x8, ld16(rs, lane16, O_W1F + (ftc * 32 + i) * 1024));
          W[i + 1] = __builtin_bit_cast(bf16x8, ld16(rs, lane16, O_W1F + (ftc * 32 + i + 1) * 1024));
        }
        SCHED();
      }
      STAMP(1 + 3 * layer, 1);
#pragma unroll
      for (int g = 0; g < 8; ++g) lds_st<f32x4>(L_Y + w * 8192 + (64 * g + 16 * h + (r ^ (4 * h))) * 16, acc[g]);
    }
    BAR();
    {
      PHASE_IDS();
      STAMP(1 + 3 * layer, 2);
      const int c = lane >> 2, h = lane & 3;
      const u32 y = L_Y + (64 * w + 16 * h + (c ^ (4 * h))) * 16;
      const f32x4 y0 = (lds_ld<f32x4>(y) + lds_ld<f32x4>(y + 8192)) + (lds_ld<f32x4>(y + 16384) + lds_ld<f32x4>(y + 24576));
      const f32x4 y1 = (lds_ld<f32x4>(y + 32768) + lds_ld<f32x4>(y + 40960)) + (lds_ld<f32x4>(y + 49152) + lds_ld<f32x4>(y + 57344));
      const u32 a0 = pkbf(fmaf(y0[0], ks0, base[0][0]), fmaf(y0[1], ks0, base[0][1]));
      const u32 a1 = pkbf(fmaf(y0[2], ks0, base[0][2]), fmaf(y0[3], ks0, base[0][3]));
      const u32 c0 = pkbf(fmaf(y1[0], ks1, base[1][0]), fmaf(y1[1], ks1, base[1][1]));
      const u32 c1 = pkbf(fmaf(y1[2], ks1, base[1][2]), fmaf(y1[3], ks1, base[1][3]));
      const bool odd = lane & 1;
      const u32 r0 = dppu<0xB1>(odd ? a0 : c0), r1 = dppu<0xB1>(odd ? a1 : c1);
      const u32x4 o = {odd ? r0 : a0, odd ? r1 : a1, odd ? c0 : r0, odd ? c1 : r1};
      st16(rs, xt_off + n * 128 + (tid >> 3) * 32768 + (tid & 1) * 64 + ((tid >> 1) & 3) * 16, o);
      STAMP(1 + 3 * layer, 3);
      WAITV(0);
      BAR();
      if (tid == 0) st4(rs, O_F1 + (layer * 256 + b) * 128, 0, tag);
      STAMP(1 + 3 * layer, 4);
      if (layer == 0) {
#pragma unroll
        for (int i = 16; i < 32; ++i) W[i] = __builtin_bit_cast(bf16x8, ld16(rs, lane16, O_W1F + (ftc * 32 + i) * 1024));
        f32x16 cs;
#pragma unroll
        for (int e = 0; e < 16; ++e) cs[e] = 0.f;
        const bf16x8 ones = __builtin_bit_cast(bf16x8, (u32x4){0x3f803f80u, 0x3f803f80u, 0x3f803f80u, 0x3f803f80u});
        u32 f = tag;
        if (w < 4) f = ld4(rs, O_F1 + tid * 128, 0);
#pragma unroll
        for (int i = 0; i < 32; ++i) cs = __builtin_amdgcn_mfma_f32_32x32x16_bf16(W[i], ones, cs, 0, 0, 0);
        if (r32 == 0) {
#pragma unroll
          for (int q = 0; q < 4; ++q)
            lds_st<f32x4>(L_CS + w * 128 + hh * 64 + q * 16, (f32x4){cs[4 * q], cs[4 * q + 1], cs[4 * q + 2], cs[4 * q + 3]});
        }
        poll(rs, f, O_F1 + tid * 128, 0, tag);
      } else {
        delay(6);
        if (w < 4) poll(rs, O_F1 + (256 + tid) * 128, 0, tag);
      }
      __builtin_amdgcn_s_waitcnt(0x0F70);
      BAR();
      if (layer == 0 && b == 0 && tid == 0) st4(rs, O_EP, 0, tag);
      STAMP(1 + 3 * layer, 5);
    }
    {
      PHASE_IDS();
      const int Ta = n < 253 ? mt : 55 + 3 * (n - 253), Tb = n < 253 ? 23 + mt : Ta + 1;
      const int Tc = ntile == 3 ? 46 + mt - (mt + 2) / 5 : ntile == 2 ? (mt / 5) * 11 + sl : Ta + 2;
      const char* xs = ws + xt_off + w * 4096;
      const u32 xb = hh * 512 + ((lane & 15) >> 2) * 64 + ((lane >> 4) & 1) * 32 + (lane & 3) * 8;
      const u32 g1_off = O_G1 + layer * 32768;
      if (ntile == 3) {
        dma<4>(xs + Ta * 32768, w * 4096, lane16);
        dma<4>(xs + Tb * 32768, 32768 + w * 4096, lane16);
        dma<4>(xs + Tc * 32768, 65536 + w * 4096, lane16);
        WAITV(8);
      } else {
        dma<4>(xs + Tc * 32768, 65536 + w * 4096, lane16);
        dma<4>(xs + Ta * 32768, w * 4096, lane16);
        dma<4>(xs + Tb * 32768, 32768 + w * 4096, lane16);
#pragma unroll 1
        for (int k = 0; k < (ntile ? 1 : 3); ++k) {
          if (k == 0) WAITV(8); else WAITV(0);
          const u32 a = xb + (k == 0 ? 65536 : (k - 1) * 32768) + w * 4096;
          float s1 = 0.f, s2 = 0.f;
#pragma unroll
          for (int i = 0; i < 4; ++i) {
            const u32x4 v = __builtin_bit_cast(u32x4, trfrag(a + i * 1024));
#pragma unroll
            for (int j = 0; j < 4; ++j) { s1 = dot2(v[j], 0x3f803f80u, s1); s2 = dot2(v[j], v[j], s2); }
          }
          lds_st<float>(L_ST + lane * 32 + w * 4, sw32(s1, s2));
          BAR();
          if (w == 7) {
            const f32x4 p = lds_ld<f32x4>(L_ST + lane * 32) + lds_ld<f32x4>(L_ST + lane * 32 + 16);
            const float s = ((p[0] + p[1]) + (p[2] + p[3])) * (1.f / SD);
            const auto sw = __builtin_amdgcn_permlane32_swap(__float_as_uint(s), __float_as_uint(s), false, false);
            const float m = __uint_as_float(sw[0]);
            const float v = fmaxf(fmaf(-m, m, __uint_as_float(sw[1])), 0.f) + 1e-5f;
            st16(rs, lane < 32 ? g1_off + ((k == 0 ? Tc : Ta + k - 1) * 32 + lane) * 16 : OOB, (u32x4){__float_as_uint(-m), tag, __float_as_uint(v), tag});
          }
          if (ntile == 0) BAR();
        }
        if (w == 7) WAITV(5); else WAITV(4);
      }
      BAR();
      STAMP(1 + 3 * layer, 6);
      const char* w2s = ws + O_W2F + (nt * FT + 12) * 2048;
      const u32 csr = L_CS + w * 128 + hh * 64;
      const bool wv = ftw < FT && ntile > 0;
      const u32 hso = h_off + ftc * 1024 + lane16, fso = fh_off + ftc * 128;
      f32x16 acc0, acc1;
      bf16x8 fr[PD];
      u32x4 ga, gb, gc;
#define G1_DEFS() asm volatile("" : "=v"(acc0), "=v"(acc1), "=v"(fr[0]), "=v"(fr[1]), "=v"(fr[2]), "=v"(fr[3]))
      auto stat_loads = [&]() {
        ga = ld16(rs, r32 * 16, g1_off + Ta * 512);
        gb = ld16(rs, r32 * 16, g1_off + Tb * 512);
        gc = ld16(rs, r32 * 16, g1_off + Tc * 512);
      };
      auto stat_wait = [&]() {
#pragma unroll 1
        for (int s = 0; s < SPIN_MAX; ++s) {
          if (none(((ga[1] ^ tag) | (ga[3] ^ tag) | (gb[1] ^ tag) | (gb[3] ^ tag) | (gc[1] ^ tag) | (gc[3] ^ tag)) != 0)) break;
          asm volatile("" ::: "memory");
          stat_loads();
        }
        __builtin_amdgcn_s_waitcnt(0x0F70);
      };
      if (w < 4) {
        G1_DEFS();
        if (wv) { g1_pre(fr, xb); g1_chain<false>(acc0, W, fr, xb); }
        WAITV(0);
        BAR();
        STAMP(2 + 3 * layer, 0);
        if (wv) { g1_pre(fr, xb + 32768); g1_chain<true>(acc1, W, fr, xb + 32768); }
        STAMP(2 + 3 * layer, 1);
        stat_loads();
        stat_wait();
        if (wv) {
          st16(rs, hso + Ta * (FT * 1024), g1_epi(acc0, ga, csr));
          st16(rs, hso + Tb * (FT * 1024), g1_epi(acc1, gb, csr));
        }
        STAMP(2 + 3 * layer, 2);
        BAR();
        STAMP(2 + 3 * layer, 3);
        if (wv && ntile == 3) g1_chain<false>(acc0, W, fr, xb + 65536);
        WAITV(0);
        if (wv) st4(rs, lane == 0 ? Ta * (FT * 128) : lane == 1 ? Tb * (FT * 128) : OOB, fso, tag);
        STAMP(2 + 3 * layer, 4);
        if (wv && ntile == 3) {
          MFMA_DRAIN(acc0);
          st16(rs, hso + Tc * (FT * 1024), g1_epi(acc0, gc, csr));
        }
        dma<16>(w2s + 57344 + w * 16384, w * 16384, lane16);
        WAITV(16);
        if (wv && ntile == 3) st4(rs, lane == 0 ? Tc * (FT * 128) : OOB, fso, tag);
        STAMP(2 + 3 * layer, 5);
        BAR();
        dma<7>(w2s + 57344 + 65536 + w * 7168, 65536 + w * 7168, lane16);
      } else {
        dma<4>(w2s + kq * 14336, L_R3 + kq * 14336, lane16);
        WAITV(4);
        BAR();
        STAMP(2 + 3 * layer, 0);
        dma<10>(w2s + kq * 14336 + 4096, L_R3 + kq * 14336 + 4096, lane16);
        STAMP(2 + 3 * layer, 1);
        G1_DEFS();
        if (wv) { g1_pre(fr, xb); g1_chain<true>(acc0, W, fr, xb); }
        stat_loads();
        if (wv) g1_chain<true>(acc1, W, fr, xb + 32768);
        STAMP(2 + 3 * layer, 2);
        BAR();
        STAMP(2 + 3 * layer, 3);
        stat_wait();
        if (wv) {
          st16(rs, hso + Ta * (FT * 1024), g1_epi(acc0, ga, csr));
          st16(rs, hso + Tb * (FT * 1024), g1_epi(acc1, gb, csr));
        }
        STAMP(2 + 3 * layer, 4);
        if (wv && ntile == 3) {
          g1_chain<false>(acc0, W, fr, xb + 65536);
        }
        WAITV(0);
        if (wv) st4(rs, lane == 0 ? Ta * (FT * 128) : lane == 1 ? Tb * (FT * 128) : OOB, fso, tag);
        STAMP(2 + 3 * layer, 5);
        BAR();
        if (wv && ntile == 3) {
          MFMA_DRAIN(acc0);
          st16(rs, hso + Tc * (FT * 1024), g1_epi(acc0, gc, csr));
          WAITV(0);
          st4(rs, lane == 0 ? Tc * (FT * 128) : OOB, fso, tag);
        }
        STAMP(2 + 3 * layer, 6);
        BAR();
      }
    }
    PHASE_IDS();
    bf16x8 Wd[3][2];
#pragma unroll
    for (int i = 0; i < 3; ++i)
#pragma unroll
      for (int c = 0; c < 2; ++c)
        Wd[i][c] = __builtin_bit_cast(bf16x8, ld16(rs, lane16, O_W2F + ((nt * FT + kq + 4 * i) * 2 + c) * 1024));
    if (T2 >= 46) delay(4);
    poll(rs, min(kq + 4 * lane, FT - 4 + (kq ^ 2)) * 128, fh_off + T2 * (FT * 128), tag);
    STAMP(3 + 3 * layer, 0);
    {
      constexpr int HW = 12;
      u16 rsd[8];
      bf16x8 fh[HW];
      auto ldH = [&](int i) { return __builtin_bit_cast(bf16x8, ld16(rs, lane16, h_off + (T2 * FT + min(kq + 4 * i, FT - 1)) * 1024)); };
#pragma unroll
      for (int i = 0; i < HW; ++i) fh[i] = ldH(i);
      if (w < 4) BAR();
      f32x16 acc[2];
#pragma unroll
      for (int c = 0; c < 2; ++c)
#pragma unroll
        for (int e = 0; e < 16; ++e) acc[c][e] = 0.f;
      u32 wb0 = kq * 2048 + lane16, wb1 = wb0 + 65536, wb3 = wb0 + L_R3;
      asm volatile("" : "+v"(wb1), "+v"(wb3));
      auto ldW = [&](int i, int c) {
        return lds_ld<bf16x8>((i < 10 ? wb3 + (i - 3) * 8192 : i < 18 ? wb0 + (i - 10) * 8192 : wb1 + (i - 18) * 8192) + c * 1024);
      };
      STAMP(3 + 3 * layer, 1);
      SCHED();
#pragma unroll
      for (int i = 0; i < 22; ++i) {
        if (i < 21 || kq < 2) {
          acc[0] = __builtin_amdgcn_mfma_f32_32x32x16_bf16(Wd[i % 3][0], fh[i % HW], acc[0], 0, 0, 0);
          acc[1] = __builtin_amdgcn_mfma_f32_32x32x16_bf16(Wd[i % 3][1], fh[i % HW], acc[1], 0, 0, 0);
        }
        if (i + HW < 22) fh[i % HW] = ldH(i + HW);
        if (i == 12) {
#pragma unroll
          for (int j = 0; j < 8; ++j) rsd[j] = ld2(rs, (8 * hh + j) * 64 + r32 * 2, xt_off + T2 * 32768 + (nt * 64 + kq * 16) * 64);
        }
        if (i + 3 < 22) { Wd[i % 3][0] = ldW(i + 3, 0); Wd[i % 3][1] = ldW(i + 3, 1); }
        SCHED();
      }
      STAMP(3 + 3 * layer, 2);
      BAR();
      const u32 pw = L_P + tt * 32768 + kq * 2048 + lane16;
      f32x4 e4, o4;
      asm volatile("" : "=v"(e4), "=v"(o4));
#pragma unroll
      for (int q = 0; q < 4; ++q) {
        const f32x4 ev = {acc[0][4 * q], acc[0][4 * q + 1], acc[0][4 * q + 2], acc[0][4 * q + 3]};
        const f32x4 od = {acc[1][4 * q], acc[1][4 * q + 1], acc[1][4 * q + 2], acc[1][4 * q + 3]};
        if (q != kq) { lds_st<f32x4>(pw + q * 8192, ev); lds_st<f32x4>(pw + q * 8192 + 1024, od); }
        else { e4 = ev; o4 = od; }
      }
#pragma unroll
      for (int e = 0; e < 4; ++e) { e4[e] += b2f(rsd[2 * e]); o4[e] += b2f(rsd[2 * e + 1]); }
      BAR();
      STAMP(3 + 3 * layer, 3);
      const u32 pr = L_P + tt * 32768 + kq * 8192 + lane16;
#pragma unroll
      for (int k = 1; k < 4; ++k) {
        const u32 a = pr + ((kq + k) & 3) * 2048;
        e4 += lds_ld<f32x4>(a);
        o4 += lds_ld<f32x4>(a + 1024);
      }
      u32x4 o = {pkbf(e4[0], o4[0]), pkbf(e4[1], o4[1]), pkbf(e4[2], o4[2]), pkbf(e4[3], o4[3])};
      int last = layer;
      asm volatile("" : "+s"(last));
      asm volatile("" : "=v"(xraw), "=v"(mean[0]), "=v"(mean[1]), "=v"(mean[2]), "=v"(mean[3]), "=v"(rstd[0]), "=v"(rstd[1]), "=v"(rstd[2]), "=v"(rstd[3]));
      if (last == 1) {
        *(u32x4*)(out + (size_t)(T2 * 32 + r32) * SD + nt * 64 + kq * 16 + hh * 8) = o;
        STAMP(6, 4);
        continue;
      }
      float s1 = 0.f, s2 = 0.f;
#pragma unroll
      for (int k = 0; k < 4; ++k) { s1 = dot2(o[k], 0x3f803f80u, s1); s2 = dot2(o[k], o[k], s2); }
      __hip_atomic_fetch_add((GLBP float*)(ws + O_S2) + hh * 2048 + T2 * 32 + r32, sw32(s1, s2), __ATOMIC_RELAXED, __HIP_MEMORY_SCOPE_AGENT);
      {
        const bool b0 = lane & 1, b1 = lane & 2;
        const u32 s0 = dppu<0xB1>(b0 ? o[0] : o[1]), s1_ = dppu<0xB1>(b0 ? o[2] : o[3]);
        const u32 p0 = b0 ? s0 : o[0], p1 = b0 ? o[1] : s0, p2 = b0 ? s1_ : o[2], p3 = b0 ? o[3] : s1_;
        const u32 t0 = dppu<0x4E>(b1 ? p0 : p2), t1 = dppu<0x4E>(b1 ? p1 : p3);
        o = (u32x4){b1 ? t0 : p0, b1 ? t1 : p1, b1 ? p2 : t0, b1 ? p3 : t1};
      }
      st16(rs, O_XP + (nt * 32 + kq * 8 + hh * 4 + (lane & 3)) * 8192 + (T2 * 32 + (r32 & ~3)) * 4, o);
      dma<3>(ws + O_KT + b * KT_SZ + w * 3072, L_K + w * 3072, lane16);
    }
    STAMP(3, 4);
    WAITV(3);
    BAR();
    if (tid == 0) st4(rs, O_F3 + b * 128, 0, tag);
    STAMP(3, 5);
    delay(6);
    if (w < 4) poll(rs, O_F3 + tid * 128, 0, tag);
    BAR();
    STAMP(3, 6);
    xraw = ld16(rs, tid * 16, O_XP + n * 8192);
    {
      const u32x4 sa = ld16(rs, tid * 16, O_S2), sb = ld16(rs, tid * 16, O_S2 + 8192);
#pragma unroll
      for (int i = 0; i < 4; ++i) {
        mean[i] = __uint_as_float(sa[i]) * (1.f / SD);
        rstd[i] = __builtin_amdgcn_rsqf(fmaxf(fmaf(__uint_as_float(sb[i]), 1.f / SD, -mean[i] * mean[i]), 0.f) + 1e-5f);
      }
    }
    STAMP(3, 7);
  }
}

extern "C" void kernel_launch(void* const* d_in, const int* in_sizes, int n_in, void* d_out, int out_size,
                              void* d_ws, size_t ws_size, hipStream_t stream) {
  k_all<<<256, 512, 0, stream>>>((const u16*)d_in[0], (const u16*)d_in[1], (const u16*)d_in[2], (const u16*)d_in[3],
                                 (const u16*)d_in[4], (const u16*)d_in[5], (const u16*)d_in[6], (const u16*)d_in[8],
                                 (const u16*)d_in[9], (const u16*)d_in[10], (char*)d_ws, (u16*)d_out);
}
```

```cpp
#include <hip/hip_runtime.h>
#include <stdint.h>

typedef unsigned short u16;
typedef uint32_t u32;
typedef unsigned long long u64;
typedef __attribute__((ext_vector_type(2))) float f32x2;
typedef __attribute__((ext_vector_type(4))) float f32x4;
typedef __attribute__((ext_vector_type(16))) float f32x16;
typedef __attribute__((ext_vector_type(2))) _Float16 f16x2;
typedef __attribute__((ext_vector_type(8))) _Float16 f16x8;
typedef __attribute__((ext_vector_type(2))) __bf16 bf16x2;
typedef __attribute__((ext_vector_type(8))) __bf16 bf16x8;
typedef __attribute__((ext_vector_type(4))) short s16x4;
typedef __attribute__((ext_vector_type(2))) u32 u32x2;
typedef __attribute__((ext_vector_type(4))) u32 u32x4;
typedef __amdgpu_buffer_rsrc_t rsrc_t;
#define LDSP __attribute__((address_space(3)))
#define GLBP __attribute__((address_space(1)))
#define DEVI __device__ __forceinline__

constexpr int SL = 2048, SD = 512, NM = 128, DF = 1376, FT = 86;
constexpr int KCOPY = 2080;
constexpr int KCH_B = 2 * KCOPY * 2;
constexpr u32 O_EP = 0;
constexpr u32 O_F3 = 4096;
constexpr u32 O_G0 = O_F3 + 32768;
constexpr u32 O_G1 = O_G0 + 32768;
constexpr u32 O_S2 = O_G1 + 65536;
constexpr u32 O_FW = O_S2 + 16384;
constexpr u32 O_W1F = O_FW + 262144;
constexpr u32 O_W2F = O_W1F + FT * 32768;
constexpr u32 O_XT = O_W2F + 8 * FT * 2048 + 4096;
constexpr u32 O_H = O_XT + 2 * (2u << 20);
constexpr u32 H_SZ = 64 * FT * 1024;
constexpr u32 O_XP = O_H + 2 * H_SZ;
constexpr u32 O_XG = O_XP + (2u << 20);
constexpr u32 WS_END = O_XG + (4u << 20);
constexpr int L_Y = 0;
constexpr int TBP = 320, L_TB = 0, TB_W = 26 * TBP;
constexpr int L_RS = 8 * TB_W;
constexpr int L_TAB = L_RS + 256;
constexpr int L_K = 98304;
constexpr int L_U = L_K + 24576, U_CH = (240 + 2048) * 2;
constexpr int L_R3 = 98304;
constexpr int L_ST = 155648;
constexpr int L_CS = L_ST + 4096;
constexpr int L_TH = L_CS + 1024;
constexpr int L_SZ = 163840;
constexpr int SPIN_MAX = 1 << 16;
#define X_DELAY 4

DEVI float bflo(u32 v) { return __uint_as_float(v << 16); }
DEVI float bfhi(u32 v) { return __uint_as_float(v & 0xffff0000u); }
DEVI float b2f(u16 v) { return __uint_as_float(((u32)v) << 16); }
DEVI u32 pkbf(float a, float b) { bf16x2 r = __builtin_convertvector((f32x2){a, b}, bf16x2); return __builtin_bit_cast(u32, r); }
DEVI u32 pkf16(float a, float b) { f16x2 r = __builtin_convertvector((f32x2){a, b}, f16x2); return __builtin_bit_cast(u32, r); }
DEVI float rbf(float a) { return bflo(pkbf(a, 0.f)); }
template <int C> DEVI float dppf(float v) { return __uint_as_float(__builtin_amdgcn_update_dpp(0u, __float_as_uint(v), C, 0xf, 0xf, true)); }
template <int C> DEVI u32 dppu(u32 v) { return __builtin_amdgcn_update_dpp(0u, v, C, 0xf, 0xf, true); }
DEVI float sw16(float v) { auto r = __builtin_amdgcn_permlane16_swap(__float_as_uint(v), __float_as_uint(v), false, false); return __uint_as_float(r[0]) + __uint_as_float(r[1]); }
DEVI float sw32(float a, float b) { auto r = __builtin_amdgcn_permlane32_swap(__float_as_uint(a), __float_as_uint(b), false, false); return __uint_as_float(r[0]) + __uint_as_float(r[1]); }
DEVI float wsum(float v) { v += dppf<0xB1>(v); v += dppf<0x4E>(v); v += dppf<0x141>(v); v += dppf<0x140>(v); v = sw16(v); return sw32(v, v); }
DEVI float wmax(float v) {
  v = fmaxf(v, dppf<0xB1>(v)); v = fmaxf(v, dppf<0x4E>(v)); v = fmaxf(v, dppf<0x141>(v)); v = fmaxf(v, dppf<0x140>(v));
  auto r = __builtin_amdgcn_permlane16_swap(__float_as_uint(v), __float_as_uint(v), false, false);
  v = fmaxf(__uint_as_float(r[0]), __uint_as_float(r[1]));
  r = __builtin_amdgcn_permlane32_swap(__float_as_uint(v), __float_as_uint(v), false, false);
  return fmaxf(__uint_as_float(r[0]), __uint_as_float(r[1]));
}
DEVI float dot2(u32 a, u32 b, float c) { return __builtin_amdgcn_fdot2_f32_bf16(__builtin_bit_cast(bf16x2, a), __builtin_bit_cast(bf16x2, b), c, false); }

#define WAITV(n) asm volatile("s_waitcnt vmcnt(" #n ")" ::: "memory")
#define WAITL0() asm volatile("s_waitcnt lgkmcnt(0)" ::: "memory")
#define SCHED() __builtin_amdgcn_sched_barrier(0)
#define BAR() do { asm volatile("s_waitcnt lgkmcnt(0)\n\ts_barrier" ::: "memory"); } while (0)

#define STAMP(k, i)

DEVI u32x4 ld16(rsrc_t rs, u32 voff, u32 soff) { return __builtin_amdgcn_raw_buffer_load_b128(rs, voff, soff, 16); }
DEVI u32 ld4(rsrc_t rs, u32 voff, u32 soff) { return __builtin_amdgcn_raw_buffer_load_b32(rs, voff, soff, 16); }
DEVI u16 ld2(rsrc_t rs, u32 voff, u32 soff) { return __builtin_amdgcn_raw_buffer_load_b16(rs, voff, soff, 16); }
DEVI void st16(rsrc_t rs, u32 off, u32x4 v) { __builtin_amdgcn_raw_buffer_store_b128(v, rs, off, 0, 16); }
DEVI void st4(rsrc_t rs, u32 voff, u32 soff, u32 v) { __builtin_amdgcn_raw_buffer_store_b32(v, rs, voff, soff, 16); }
template <int K> DEVI void dma1(rsrc_t rs, u32 goff, u32 lds, u32 lane16) {
  const u32 g = goff + (K & ~3) * 1024;
  if constexpr ((K & 3) == 0)
    asm volatile("s_mov_b32 m0, %0\n\ts_nop 0\n\tbuffer_load_dwordx4 %1, %2, %3 offen sc1 lds"
                 :: "s"(lds + K * 1024), "v"(lane16), "s"(rs), "s"(g) : "memory");
  else
    asm volatile("buffer_load_dwordx4 %0, %1, %2 offen offset:%3 sc1 lds" :: "v"(lane16), "s"(rs), "s"(g), "i"((K & 3) * 1024) : "memory");
}
DEVI void dmak(int k, rsrc_t rs, u32 goff, u32 lds, u32 lane16) {
  switch (k) {
#define DK(n) case n: dma1<n>(rs, goff, lds, lane16); break;
    DK(0) DK(1) DK(2) DK(3) DK(4) DK(5) DK(6) DK(7) DK(8) DK(9) DK(10) DK(11) DK(12) DK(13)
#undef DK
  }
}
DEVI bool none(bool p) { return __builtin_amdgcn_ballot_w64(p) == 0; }
constexpr u32 OOB = 0x80000000u;
DEVI void poll(rsrc_t rs, u32 f, u32 voff, u32 soff, u32 tag) {
#pragma unroll 1
  for (int s = 0; s < SPIN_MAX; ++s) {
    if (none(f != tag)) break;
    asm volatile("" ::: "memory");
    f = ld4(rs, voff, soff);
  }
  asm volatile("" ::: "memory");
}
DEVI void poll(rsrc_t rs, u32 voff, u32 soff, u32 tag) {
  asm volatile("" ::: "memory");
  poll(rs, ld4(rs, voff, soff), voff, soff, tag);
}
constexpr u32 EMPTY = 0xffffffffu;
DEVI u32 umax4(u32 m, const u32x4& v) { return max(max(max(max(m, v[0]), v[1]), v[2]), v[3]); }
DEVI u32x4 notempty(u32x4 v) { return (u32x4){min(v[0], EMPTY - 1), min(v[1], EMPTY - 1), min(v[2], EMPTY - 1), min(v[3], EMPTY - 1)}; }
template <int N> DEVI void xload(rsrc_t rs, u32x4 (&v)[N], u32 voff, u32 soff, u32 stride) {
#pragma unroll
  for (int i = 0; i < N; ++i) v[i] = ld16(rs, voff, soff + i * stride);
}
template <int N> DEVI bool xfull(const u32x4 (&v)[N]) {
  u32 m = 0;
#pragma unroll
  for (int i = 0; i < N; ++i) m = umax4(m, v[i]);
  return none(m == EMPTY);
}
template <int N> DEVI void xpark(rsrc_t rs, u32x4 (&v)[N], u32 voff, u32 soff, u32 stride, u32 lds) {
  if (!xfull<N>(v)) {
#pragma unroll 1
    for (int s = 0; s < SPIN_MAX; ++s) {
      asm volatile("" ::: "memory");
      xload<N>(rs, v, voff, soff, stride);
      if (xfull<N>(v)) break;
    }
  }
#pragma unroll
  for (int i = 0; i < N; ++i) *(LDSP u32x4*)(size_t)(lds + i * stride) = v[i];
}
DEVI void delay(int n) { for (int i = 0; i < n; ++i) __builtin_amdgcn_s_sleep(2); }

DEVI f32x2 cexp(float are, float aim, float p) {
  const float e = __builtin_amdgcn_exp2f(are * p), ph = __builtin_amdgcn_fractf(aim * p);
  return (f32x2){__builtin_amdgcn_cosf(ph), __builtin_amdgcn_sinf(ph)} * (f32x2){e, e};
}
DEVI f32x2 cmul(f32x2 a, f32x2 b) {
  f32x2 r;
  asm("v_pk_mul_f32 %0, %1, %2 op_sel:[0,0] op_sel_hi:[0,1]\n\t"
      "v_pk_fma_f32 %0, %1, %2, %0 op_sel:[1,1,0] op_sel_hi:[1,0,1] neg_lo:[1,0,0]" : "=&v"(r) : "v"(a), "v"(b));
  return r;
}
DEVI f16x8 cmul4(f32x4 a0, f32x4 a1, f32x4 b0, f32x4 b1) {
  f32x2 r0, r1, r2, r3;
  asm("v_pk_mul_f32 %0, %4, %8 op_sel:[0,0] op_sel_hi:[0,1]\n\tv_pk_mul_f32 %1, %5, %9 op_sel:[0,0] op_sel_hi:[0,1]\n\t"
      "v_pk_mul_f32 %2, %6, %10 op_sel:[0,0] op_sel_hi:[0,1]\n\tv_pk_mul_f32 %3, %7, %11 op_sel:[0,0] op_sel_hi:[0,1]\n\t"
      "v_pk_fma_f32 %0, %4, %8, %0 op_sel:[1,1,0] op_sel_hi:[1,0,1] neg_lo:[1,0,0]\n\t"
      "v_pk_fma_f32 %1, %5, %9, %1 op_sel:[1,1,0] op_sel_hi:[1,0,1] neg_lo:[1,0,0]\n\t"
      "v_pk_fma_f32 %2, %6, %10, %2 op_sel:[1,1,0] op_sel_hi:[1,0,1] neg_lo:[1,0,0]\n\t"
      "v_pk_fma_f32 %3, %7, %11, %3 op_sel:[1,1,0] op_sel_hi:[1,0,1] neg_lo:[1,0,0]"
      : "=&v"(r0), "=&v"(r1), "=&v"(r2), "=&v"(r3)
      : "v"(a0.xy), "v"(a0.zw), "v"(a1.xy), "v"(a1.zw), "v"(b0.xy), "v"(b0.zw), "v"(b1.xy), "v"(b1.zw));
  return __builtin_bit_cast(f16x8, (u32x4){pkf16(r0[0], r0[1]), pkf16(r1[0], r1[1]), pkf16(r2[0], r2[1]), pkf16(r3[0], r3[1])});
}
DEVI s16x4 trread(u32 a) { return __builtin_amdgcn_ds_read_tr16_b64_v4i16((LDSP s16x4*)(size_t)a); }
DEVI bf16x8 trfrag(u32 a) {
  const s16x4 lo = trread(a), hi = trread(a + 256);
  return __builtin_bit_cast(bf16x8, __builtin_shufflevector(lo, hi, 0, 1, 2, 3, 4, 5, 6, 7));
}
template <class T> DEVI T lds_ld(u32 a) { return *(const LDSP T*)(size_t)a; }
template <class T> DEVI void lds_st(u32 a, T v) { *(LDSP T*)(size_t)a = v; }

constexpr int PD = 4;
constexpr int WKEEP = 24;
DEVI void g1_pre(bf16x8 (&fr)[PD], u32 xb) {
#pragma unroll
  for (int i = 0; i < PD; ++i) fr[i] = trfrag(xb + i * 1024);
}
template <bool NEXT, class F> DEVI void g1_chain(f32x16& acc, const bf16x8 (&W)[32], bf16x8 (&fr)[PD], u32 xb, F side) {
  asm volatile("" : "+v"(xb));
#pragma unroll
  for (int e = 0; e < 16; ++e) acc[e] = 0.f;
  __builtin_amdgcn_s_setprio(1);
  SCHED();
#pragma unroll
  for (int i = 0; i < 32; ++i) {
    acc = __builtin_amdgcn_mfma_f32_32x32x16_bf16(W[i], fr[i % PD], acc, 0, 0, 0);
    SCHED();
    if (i + PD < 32 || NEXT) fr[i % PD] = trfrag(xb + (i + PD) * 1024);
    if (!(i & 1)) side(i >> 1);
    SCHED();
  }
  __builtin_amdgcn_s_setprio(0);
}
template <bool V> struct Role { static constexpr bool A = V; };
template <bool PK> DEVI u32x4 g1_epi(const f32x16& acc, float nm, float v, u32 cssrc) {
  f32x4 cs[4];
#pragma unroll
  for (int q = 0; q < 4; ++q) cs[q] = lds_ld<f32x4>(cssrc + q * 16);
  const float rs = __builtin_amdgcn_rsqf(v);
  const float k0 = rs * -2.302208198f, k1 = rs * rs * rs * -0.102943239f;
  if constexpr (PK) {
    u32x4 o;
    f32x2 g[4], e[4];
#pragma unroll
    for (int p = 0; p < 4; ++p) {
      g[p] = __builtin_elementwise_fma((f32x2){cs[p >> 1][2 * (p & 1)], cs[p >> 1][2 * (p & 1) + 1]}, (f32x2){nm, nm}, (f32x2){acc[2 * p], acc[2 * p + 1]});
      const f32x2 t = g[p] * __builtin_elementwise_fma(g[p] * g[p], (f32x2){k1, k1}, (f32x2){k0, k0});
      e[p] = (f32x2){__builtin_amdgcn_exp2f(t[0]), __builtin_amdgcn_exp2f(t[1])};
    }
#pragma unroll
    for (int p = 0; p < 4; ++p) {
      const f32x2 d = __builtin_elementwise_fma(e[p], (f32x2){v, v}, (f32x2){v, v});
      const f32x2 uu = __builtin_elementwise_fma((f32x2){cs[2 + (p >> 1)][2 * (p & 1)], cs[2 + (p >> 1)][2 * (p & 1) + 1]}, (f32x2){nm, nm},
                                                 (f32x2){acc[8 + 2 * p], acc[9 + 2 * p]});
      const f32x2 r = g[p] * uu * (f32x2){__builtin_amdgcn_rcpf(d[0]), __builtin_amdgcn_rcpf(d[1])};
      o[p] = pkbf(r[0], r[1]);
    }
    return notempty(o);
  }
  u32x4 o;
#pragma unroll
  for (int q = 0; q < 2; ++q) {
    float g0 = cs[q][0], g1 = cs[q][1], g2 = cs[q][2], g3 = cs[q][3];
    float u0 = cs[2 + q][0], u1 = cs[2 + q][1], u2 = cs[2 + q][2], u3 = cs[2 + q][3];
    float t0, t1, t2, t3;
    asm("v_fma_f32 %0, %20, %0, %12\n\tv_fma_f32 %1, %20, %1, %13\n\tv_fma_f32 %2, %20, %2, %14\n\tv_fma_f32 %3, %20, %3, %15\n\t"
        "v_mul_f32 %8, %0, %0\n\tv_mul_f32 %9, %1, %1\n\tv_mul_f32 %10, %2, %2\n\tv_mul_f32 %11, %3, %3\n\t"
        "v_fma_f32 %8, %8, %22, %21\n\tv_fma_f32 %9, %9, %22, %21\n\tv_fma_f32 %10, %10, %22, %21\n\tv_fma_f32 %11, %11, %22, %21\n\t"
        "v_mul_f32 %8, %0, %8\n\tv_mul_f32 %9, %1, %9\n\tv_mul_f32 %10, %2, %10\n\tv_mul_f32 %11, %3, %11\n\t"
        "v_exp_f32 %8, %8\n\tv_exp_f32 %9, %9\n\tv_exp_f32 %10, %10\n\tv_exp_f32 %11, %11\n\t"
        "v_fma_f32 %4, %20, %4, %16\n\tv_fma_f32 %5, %20, %5, %17\n\tv_fma_f32 %6, %20, %6, %18\n\tv_fma_f32 %7, %20, %7, %19\n\t"
        "v_fma_f32 %8, %8, %23, %23\n\tv_fma_f32 %9, %9, %23, %23\n\tv_fma_f32 %10, %10, %23, %23\n\tv_fma_f32 %11, %11, %23, %23\n\t"
        "v_rcp_f32 %8, %8\n\tv_rcp_f32 %9, %9\n\tv_rcp_f32 %10, %10\n\tv_rcp_f32 %11, %11\n\t"
        "v_mul_f32 %0, %0, %4\n\tv_mul_f32 %1, %1, %5\n\tv_mul_f32 %2, %2, %6\n\tv_mul_f32 %3, %3, %7\n\t"
        "v_mul_f32 %0, %0, %8\n\tv_mul_f32 %1, %1, %9\n\tv_mul_f32 %2, %2, %10\n\tv_mul_f32 %3, %3, %11"
        : "+v"(g0), "+v"(g1), "+v"(g2), "+v"(g3), "+v"(u0), "+v"(u1), "+v"(u2), "+v"(u3), "=&v"(t0), "=&v"(t1), "=&v"(t2), "=&v"(t3)
        : "v"(acc[4 * q]), "v"(acc[4 * q + 1]), "v"(acc[4 * q + 2]), "v"(acc[4 * q + 3]),
          "v"(acc[8 + 4 * q]), "v"(acc[9 + 4 * q]), "v"(acc[10 + 4 * q]), "v"(acc[11 + 4 * q]), "v"(nm), "v"(k0), "v"(k1), "v"(v));
    o[2 * q] = pkbf(g0, g1);
    o[2 * q + 1] = pkbf(g2, g3);
  }
  return notempty(o);
}
#define MFMA_DRAIN(a) asm volatile("s_nop 15\n\ts_nop 3" : "+v"(a))

__global__ __launch_bounds__(512, 2) void k_all(const u16* __restrict__ u, const u16* __restrict__ logA,
                                                const u16* __restrict__ Aim, const u16* __restrict__ Cre,
                                                const u16* __restrict__ Cim, const u16* __restrict__ logdt,
                                                const u16* __restrict__ dsk, const u16* __restrict__ wg,
                                                const u16* __restrict__ wu, const u16* __restrict__ wd,
                                                char* ws, u16* __restrict__ out) {
  __shared__ __attribute__((aligned(16))) char sm[L_SZ];
  const int tid0 = threadIdx.x, w0 = __builtin_amdgcn_readfirstlane(tid0 >> 6), b = blockIdx.x;
  const int n0 = (b & 7) * 32 + (b >> 3);
  asm volatile("" :: "v"((LDSP char*)sm) : "memory");
  STAMP(0, 0);
  const rsrc_t rs = __builtin_amdgcn_make_buffer_rsrc(ws, 0, WS_END, 0x00020000);
  int ntile0, Ta0, Tc0, ftw0, pub0;
#define PHASE_IDS()                                                                               \
  int tid = tid0, w = w0, n = n0, ntile = ntile0, Ta = Ta0, Tc = Tc0, ftw = ftw0;                 \
  u32 tag = tag0;                                                                                 \
  asm volatile("" : "+v"(tid));                                                                   \
  asm volatile("" : "+s"(w), "+s"(n), "+s"(ntile), "+s"(Ta), "+s"(Tc), "+s"(ftw), "+s"(tag));     \
  const int lane = tid & 63, r32 = lane & 31, hh = lane >> 5, ch = w >> 2;                        \
  const u32 lane16 = lane * 16;                                                                   \
  const int Tb = 23 + Ta, ftc = ftw < FT ? ftw : FT - 1;                                          \
  const int nt = n & 7, tt = w >> 2, kq = w & 3, T2 = (n >> 3) + 32 * tt;                         \
  (void)r32; (void)hh; (void)ch; (void)lane16; (void)ftc; (void)nt; (void)kq; (void)T2; (void)Tb

  u32 tag0, dsk2;
  float ks0, ks1;
  float base[2][4];
  auto normalize = [&](int tid, const u32x4& xr, const float (&mean)[4], const float (&rstd)[4]) {
    const f32x2 d = {bflo(dsk2), bfhi(dsk2)};
    f32x2 un[4];
#pragma unroll
    for (int i = 0; i < 4; ++i) {
      const f32x2 x = {bflo(xr[i]), bfhi(xr[i])};
      un[i] = (x - mean[i]) * rstd[i];
      const f32x2 t = __builtin_elementwise_fma(un[i], d, x);
      base[0][i] = t[0]; base[1][i] = t[1];
    }
    lds_st<u32x2>(L_U + 480 + tid * 8, (u32x2){pkf16(un[0][0], un[1][0]), pkf16(un[2][0], un[3][0])});
    lds_st<u32x2>(L_U + U_CH + 480 + tid * 8, (u32x2){pkf16(un[0][1], un[1][1]), pkf16(un[2][1], un[3][1])});
  };
  auto zero_pad = [&](int tid) {
    u32 z = 0;
    asm volatile("" : "+v"(z));
    if (tid < 76)
      lds_st<u32x4>(tid < 60 ? L_U + (tid >= 30 ? U_CH + (tid - 30) * 16 : tid * 16) : L_K + ((tid - 60) >> 2) * KCOPY * 2 + 4096 + (tid & 3) * 16,
                    (u32x4){z, z, z, z});
  };
  auto tap_index = [&](int tid, int ch, u32 (&kix)[9]) {
#pragma unroll
    for (int j = 0; j < 9; ++j) {
      const int l = 2047 - ((tid & 255) * 8 + j), lc = l < 0 ? 0 : l;
      kix[j] = L_TH + ch * 1312 + 2 * (lc < 128 ? lc : (int)(pkbf((float)lc, 0.f) & 0xffffu) - 0x4300 + 128);
      asm volatile("" : "+v"(kix[j]));
    }
  };
  auto expand = [&](int tid, int ch, const u32 (&kix)[9]) {
    const int i = tid & 255;
    u32 kv[9];
#pragma unroll
    for (int j = 0; j < 9; ++j) kv[j] = lds_ld<u16>(kix[j]);
    if (i == 255) kv[8] = 0;
    lds_st<u32x4>(L_K + ch * KCH_B + i * 16,
                  (u32x4){kv[0] | (kv[1] << 16), kv[2] | (kv[3] << 16), kv[4] | (kv[5] << 16), kv[6] | (kv[7] << 16)});
    lds_st<u32x4>(L_K + ch * KCH_B + i * 16 + KCOPY * 2,
                  (u32x4){kv[1] | (kv[2] << 16), kv[3] | (kv[4] << 16), kv[5] | (kv[6] << 16), kv[7] | (kv[8] << 16)});
  };
  {
    const int tid = tid0, w = w0, n = n0, lane = tid & 63, r32 = lane & 31, hh = lane >> 5, ch = w >> 2, kq = w & 3;
    const u32 lane16 = lane * 16;
    const u32x4 til = *(const u32x4*)(u + (size_t)((b >> 2) * 32 + r32) * SD + (b & 3) * 128 + (w * 2 + hh) * 8);
    const u32x4 row = *(const u32x4*)(u + (size_t)(4 * (8 * (b >> 2) + w) + (b & 3)) * SD + lane * 8);
    const u32 ep = ld4(rs, O_EP, 0);
    const int pidx = n * 2 * NM + w * 32 + r32;
    const u16 pla = logA[pidx], pai = Aim[pidx], pcr = Cre[pidx], pci = Cim[pidx];
    const u32 ldt2 = *(const u32*)(logdt + 2 * n);
    dsk2 = *(const u32*)(dsk + 2 * n);
    SCHED();
    __builtin_amdgcn_s_barrier();
    SCHED();
    st16(rs, O_XT + n * 128 + (tid >> 3) * 32768 + (tid & 7) * 16, (u32x4){EMPTY, EMPTY, EMPTY, EMPTY});
    asm volatile("" ::: "memory");
    u32 wt[2][8], wo[2], wo2[2];
#pragma unroll
    for (int k = 0; k < 2; ++k) {
      const int task = b * 8 + w + 2048 * k;
      const bool isg = k == 0 && task < 1376;
      const int mk = task / 43, fp = task - mk * 43, mat = mk >> 4, kp = mk & 15;
      const int t2 = task - 1376, ft = t2 >> 3, nb = t2 & 7;
      const int fi = ft >> 2, pos = fi < 18 ? (fi & 1) * 9 + (fi >> 1) : 9 + (fi & 1) * 2 + (fi >> 1);
      const rsrc_t wr = __builtin_amdgcn_make_buffer_rsrc((void*)(isg ? (mat ? wu : wg) : wd), 0, SD * DF * 2, 0x00020000);
      const u32 so = isg ? (kp * 32 * DF + fp * 32) * 2 : ((32 * (ft >> 1) + (ft & 1)) * SD + nb * 64) * 2;
      const u32 s1 = isg ? DF * 2 : 2 * SD * 2, s4 = isg ? 4 * DF * 2 : 16 * SD * 2;
      const u32 vo = isg ? (lane >> 4) * (8 * DF * 2) + (lane & 15) * 4 : hh * (8 * SD * 2) + r32 * 4;
#pragma unroll
      for (int j = 0; j < 8; ++j) wt[k][j] = __builtin_amdgcn_raw_buffer_load_b32(wr, vo, so + (j & 3) * s1 + (j >> 2) * s4, 0);
      const u32 sb = task >= 2064 ? OOB : isg ? O_W1F + (fp * 64 + kp * 2) * 1024 + mat * 256 : O_W2F + (nb * FT + 4 * pos + (ft & 3)) * 2048;
      wo[k] = sb + (isg ? (lane >> 4) * 512 + (lane & 15) * 16 : lane16);
      wo2[k] = wo[k] + (isg ? 32768 : 1024);
    }
    {
      int nn = n0;
      asm volatile("" : "+s"(nn));
      const int pe = (nn >> 6) * 32 + (nn & 31), pp = (nn >> 5) & 1, pq = pe / 11, pr = pe - pq * 11;
      const int mt = pr + pp >= 6 ? 2 * pq + 1 : 2 * pq, sl = pp ? (pr < 5 ? 6 + pr : pr) : (pr < 6 ? pr : pr - 6), si = (mt / 5) * 11 + sl;
      ntile0 = mt < 23 ? (mt % 5 == 2 ? 2 : 3) : 0;
      Ta0 = min(mt, 22);
      Tc0 = ntile0 == 3 ? 46 + mt - (mt + 2) / 5 : min(23 + si, 63);
      ftw0 = sl * 8 + w0;
      pub0 = ntile0 == 2 ? si : 99;
      asm volatile("" : "+s"(ntile0), "+s"(Ta0), "+s"(Tc0), "+s"(ftw0), "+s"(pub0));
    }
    u32 kix[9];
    tap_index(tid, ch, kix);
    const bool odd = lane & 1;
    u32 xgo = O_XG + ((b & 3) * 64 + (w * 2 + hh) * 4 + (odd ? 2 : 0)) * 16384 + ((r32 >> 1) & 1) * 8192 + ((b >> 2) * 8 + (r32 >> 2)) * 16;
    const u32 tb0 = L_TB + w * TB_W + r32 * 8;
    u32 tb = tb0 + hh * 13 * TBP, tbe = tb0 + (hh ? 18 : 12) * TBP, tbf = tb0 + (hh ? 19 : 5) * TBP, tbg = tb0 + (hh ? 20 : 25) * TBP,
        tb1 = tb0 + (hh ? 17 : 0) * TBP;
    float pa = hh ? 4.f : 1.f, pb = hh ? 64.f : 256.f, pe = hh ? 16.f : 2044.f, sg = hh ? -1.f : 1.f, sc = hh ? 16384.f : 1.f;
    asm volatile("" : "+v"(xgo), "+v"(tb), "+v"(tbe), "+v"(tbf), "+v"(tbg), "+v"(tb1), "+v"(pa), "+v"(pb), "+v"(pe), "+v"(sg), "+v"(sc));
    lds_st<f32x2>(tb0 + 13 * TBP, (f32x2){16384.f, 0.f});
    zero_pad(tid);
    const float dt0 = rbf(__expf(bflo(ldt2))), dt1 = rbf(__expf(bfhi(ldt2)));
    SCHED();
    const u32 tag = tag0 = __builtin_amdgcn_readfirstlane(ep) + 1u;
    STAMP(0, 1);
    {
      const u32 r0 = dppu<0xB1>(odd ? til[0] : til[2]), r1 = dppu<0xB1>(odd ? til[1] : til[3]);
      st16(rs, xgo, (u32x4){odd ? r0 : til[0], tag, odd ? til[2] : r0, tag});
      st16(rs, xgo + 16384, (u32x4){odd ? r1 : til[1], tag, odd ? til[3] : r1, tag});
      float s1 = 0.f, s2 = 0.f;
#pragma unroll
      for (int c = 0; c < 4; ++c) { s1 = dot2(row[c], 0x3f803f80u, s1); s2 = dot2(row[c], row[c], s2); }
      float s = sw32(s1, s2);
      s += dppf<0xB1>(s); s += dppf<0x4E>(s); s += dppf<0x141>(s); s += dppf<0x140>(s);
      s = sw16(s);
      if (r32 == 0) lds_st<float>(L_RS + w * 8 + hh * 4, s);
    }
    STAMP(0, 2);
    {
      const float dt = ch ? dt1 : dt0;
      const float ar = -rbf(__expf(b2f(pla))) * dt * 1.44269504088896f, ai = (b2f(pai) * dt) * 0.159154943091895f, aj = ai * sg;
      const float cr = b2f(pcr), ci = b2f(pci);
      float mx = fmaxf(fabsf(cr), fabsf(ci));
      mx = fmaxf(mx, dppf<0xB1>(mx)); mx = fmaxf(mx, dppf<0x4E>(mx)); mx = fmaxf(mx, dppf<0x141>(mx)); mx = fmaxf(mx, dppf<0x140>(mx));
      const auto mr = __builtin_amdgcn_permlane16_swap(__float_as_uint(mx), __float_as_uint(mx), false, false);
      const int ce = __builtin_amdgcn_frexp_expf(fmaxf(__uint_as_float(mr[0]), __uint_as_float(mr[1])));
      if (lane == 0) lds_st<int>(L_RS + 64 + w * 4, ce);
      const f32x2 a = cexp(ar, ai, pa), bb = cexp(ar, aj, pb), e = cexp(ar, aj, pe), e1k = cexp(ar, ai, 1024.f);
      const f32x2 d = hh ? (f32x2){ldexpf(cr, 14 - ce), ldexpf(-ci, 14 - ce)} : e1k;
      const f32x2 as = a * sc, a2 = cmul(as, a), b2 = cmul(bb, bb), b3 = cmul(b2, bb);
      const f32x2 f = cmul(hh ? e : bb, hh ? e : a2);
      lds_st<f32x2>(tb + 1 * TBP, as);
      lds_st<f32x2>(tb + 2 * TBP, a2);
      lds_st<f32x2>(tb + 3 * TBP, cmul(a2, a));
      lds_st<f32x2>(tb + 4 * TBP, bb);
      lds_st<f32x2>(tb + 6 * TBP, b2);
      lds_st<f32x2>(tb + 7 * TBP, b3);
      lds_st<f32x2>(tb + 8 * TBP, d);
      lds_st<f32x2>(tb + 9 * TBP, cmul(d, bb));
      lds_st<f32x2>(tb + 10 * TBP, cmul(d, b2));
      lds_st<f32x2>(tb + 11 * TBP, cmul(d, b3));
      asm volatile("" ::: "memory");
      lds_st<f32x2>(tbe, e);
      lds_st<f32x2>(tbf, f);
      lds_st<f32x2>(tbg, cmul(f, e));
      lds_st<f32x2>(tb1, (f32x2){1.f, 0.f});
    }
#pragma unroll
    for (int k = 0; k < 2; ++k) {
      u32x4 lo, hi;
#pragma unroll
      for (int j = 0; j < 4; ++j) {
        lo[j] = __builtin_amdgcn_perm(wt[k][2 * j + 1], wt[k][2 * j], 0x05040100u);
        hi[j] = __builtin_amdgcn_perm(wt[k][2 * j + 1], wt[k][2 * j], 0x07060302u);
      }
      st16(rs, wo[k], lo);
      st16(rs, wo2[k], hi);
    }
    WAITV(6);
    BAR();
    STAMP(0, 3);
    if (w == 7) {
      const f32x2 v = lds_ld<f32x2>(L_RS + (lane & 7) * 8) * (1.f / SD);
      const float r = __builtin_amdgcn_rsqf(fmaxf(fmaf(-v[0], v[0], v[1]), 0.f) + 1e-5f);
      st16(rs, lane < 8 ? O_G0 + (b & 3) * 8192 + ((b >> 2) * 8 + lane) * 16 : OOB, (u32x4){__float_as_uint(v[0]), tag, __float_as_uint(r), tag});
      st16(rs, lane < 8 && b < 128 ? O_S2 + b * 128 + lane16 : OOB, (u32x4){0, 0, 0, 0});
    }

    u32x4 g[6];
    auto gran_loads = [&]() {
#pragma unroll
      for (int i = 0; i < 2; ++i) g[4 + i] = ld16(rs, tid * 16, O_XG + n * 16384 + i * 8192);
#pragma unroll
      for (int i = 0; i < 4; ++i) g[i] = ld16(rs, tid * 16, O_G0 + i * 8192);
    };
    {
      const int r = lane & 15, h = lane >> 4;
      const u32 t = L_TB + w * TB_W + h * 32;
      const u32 aL = t + (r & 3) * TBP, aM = t + (r >> 2) * TBP, a2x = r == 4 ? t + 4 * TBP : aL;
      f32x4 a0 = {0, 0, 0, 0}, a1 = {0, 0, 0, 0}, a2 = {0, 0, 0, 0};
#pragma unroll
      for (int s = 0; s < 2; ++s) {
        auto L = [&](u32 a) { return lds_ld<f32x4>(a + s * 128); };
        const f32x4 m0 = L(aM + 13 * TBP), m1 = L(aM + 13 * TBP + 16);
        const f16x8 bb = cmul4(L(aL + 17 * TBP), L(aL + 17 * TBP + 16), L(aM + 21 * TBP), L(aM + 21 * TBP + 16));
        a0 = __builtin_amdgcn_mfma_f32_16x16x32_f16(cmul4(L(aL), L(aL + 16), m0, m1), bb, a0, 0, 0, 0);
        a1 = __builtin_amdgcn_mfma_f32_16x16x32_f16(cmul4(L(aL + 4 * TBP), L(aL + 4 * TBP + 16), m0, m1), bb, a1, 0, 0, 0);
        a2 = __builtin_amdgcn_mfma_f32_16x16x32_f16(cmul4(L(a2x + 8 * TBP), L(a2x + 8 * TBP + 16), m0, m1), bb, a2, 0, 0, 0);
      }
      const u32 T = L_TAB + w * 3072 + lane16;
      lds_st<f32x4>(T, a0);
      lds_st<f32x4>(T + 1024, a1);
      lds_st<f32x4>(T + 2048, a2);
    }
    SCHED();
    WAITV(0);
    st4(rs, lane == 0 ? O_FW + (b * 8 + w) * 128 : OOB, 0, tag);
    asm volatile("" ::: "memory");
    gran_loads();
    STAMP(0, 4);
    BAR();
    {
      typedef __attribute__((ext_vector_type(4))) int i32x4;
      const i32x4 ca = lds_ld<i32x4>(L_RS + 64), cb = lds_ld<i32x4>(L_RS + 80);
      const int c0 = max(max(ca[0], ca[1]), max(ca[2], ca[3])), c1 = max(max(cb[0], cb[1]), max(cb[2], cb[3]));
      ks0 = __uint_as_float(__builtin_amdgcn_readfirstlane(__float_as_uint(ldexpf(dt0, c0 - 7))));
      ks1 = __uint_as_float(__builtin_amdgcn_readfirstlane(__float_as_uint(ldexpf(dt1, c1 - 7))));
      if (kq < 3) {
        const i32x4 ce = (ch ? cb - c1 : ca - c0) - 20;
        const u32 T = L_TAB + ch * 4 * 3072 + kq * 1024 + lane16;
        const f32x4 v = (lds_ld<f32x4>(T) * ldexpf(1.f, ce[0]) + lds_ld<f32x4>(T + 3072) * ldexpf(1.f, ce[1])) +
                        (lds_ld<f32x4>(T + 2 * 3072) * ldexpf(1.f, ce[2]) + lds_ld<f32x4>(T + 3 * 3072) * ldexpf(1.f, ce[3]));
        const u32 p0 = pkf16(v[0], v[1]), p1 = pkf16(v[2], v[3]);
        const int j = lane & 15, h = lane >> 4;
        const u32 H = L_TH + ch * 1312;
        if (kq == 0) {
          lds_st<u32x2>(H + 2 * (16 * j + 4 * h), (u32x2){p0, p1});
        } else if (kq == 1) {
          lds_st<u32>(H + 2 * (256 + 8 * j + 2 * h), p0);
          lds_st<u16>(H + 2 * (384 + 4 * j + h), (u16)p1);
          lds_st<u16>(H + 2 * (448 + 4 * j + h), (u16)(p1 >> 16));
        } else if (!(h & 1)) {
          const u32 a = H + 2 * (512 + 2 * j + (h >> 1));
          lds_st<u16>(a, (u16)p0);
          lds_st<u16>(a + 64, (u16)(p0 >> 16));
          lds_st<u16>(a + 128, (u16)p1);
          lds_st<u16>(a + 192, (u16)(p1 >> 16));
        } else if (lane == 16) {
          lds_st<u16>(H + 2 * 640, (u16)p0);
        }
      }
    }
    BAR();
    STAMP(0, 5);
    expand(tid, ch, kix);
    STAMP(0, 6);
#pragma unroll 1
    for (int s = 0; s < SPIN_MAX; ++s) {
      u32 bad = 0;
#pragma unroll
      for (int k = 0; k < 6; ++k) bad |= (g[k][1] ^ tag) | (g[k][3] ^ tag);
      if (none(bad != 0)) break;
      asm volatile("" ::: "memory");
      gran_loads();
    }
    {
      float mean[4], rstd[4];
#pragma unroll
      for (int k = 0; k < 4; ++k) { mean[k] = __uint_as_float(g[k][0]); rstd[k] = __uint_as_float(g[k][2]); }
      normalize(tid, (u32x4){g[4][0], g[4][2], g[5][0], g[5][2]}, mean, rstd);
    }
    STAMP(0, 7);
  }

  bf16x8 Wk[WKEEP];
#pragma unroll 1
  for (int layer = 0; layer < 2; ++layer) {
    const u32 xt_off = O_XT + layer * (2u << 20), h_off = O_H + layer * H_SZ;
    {
      PHASE_IDS();
      const int r = lane & 15, h = lane >> 4, rho0 = kq * 2;
      f32x4 acc[8];
#pragma unroll
      for (int g = 0; g < 8; ++g) acc[g] = (f32x4){0, 0, 0, 0};
      u32 kb[4];
#pragma unroll
      for (int p = 0; p < 4; ++p) {
        kb[p] = L_K + ch * KCH_B + 4 * (((2031 - r + 8 * h) >> 1) + ((r & 1) ? 0 : KCOPY / 2)) - 64 * rho0 - 64 * (16 * p + 9);
        asm volatile("" : "+v"(kb[p]));
      }
      u32 ub = L_U + ch * U_CH + 2 * (224 + 16 * r + 8 * h) - 64 * rho0 - 64;
      asm volatile("" : "+v"(ub));
      auto lag = [](int st) { return (st & 1) ? 7 - ((st & 7) >> 1) : (st & 7) >> 1; };
      auto ldA = [&](int st) {
        const int a = lag(st);
        const u32 p = kb[a >> 1] + 64 * (9 - (8 * (a & 1) + (st >> 3)));
        return __builtin_bit_cast(f16x8, (u32x4){lds_ld<u32>(p), lds_ld<u32>(p + 4), lds_ld<u32>(p + 8), lds_ld<u32>(p + 12)});
      };
      auto ldB = [&](int rr, int q) { return lds_ld<f16x8>(ub + 64 * (8 * q + 1 - rr)); };
      const u32 fwo = O_FW + (r32 * 43 + (ftc >> 1)) * 128;
      auto ldW1 = [&]() {
        poll(rs, fwo, 0, tag);
#pragma unroll
        for (int i = 0; i < WKEEP; ++i) Wk[i] = __builtin_bit_cast(bf16x8, ld16(rs, lane16, O_W1F + (ftc * 32 + i) * 1024));
      };
      if (layer == 0) {
        u32 e = EMPTY;
        asm volatile("" : "+v"(e));
        const u32 ho = ftw < FT && ntile > 0 ? O_H + ftc * 1024 + lane16 : OOB;
        st16(rs, ho + Ta * (FT * 1024), (u32x4){e, e, e, e});
        st16(rs, ho + Tb * (FT * 1024), (u32x4){e, e, e, e});
        st16(rs, (ntile == 3 ? ho : OOB) + Tc * (FT * 1024), (u32x4){e, e, e, e});
      }
      f16x8 A[4], B[8];
      BAR();
      STAMP(1 + 3 * layer, 0);
      if (layer == 0 && w >= 4) ldW1();
      A[0] = ldA(0);
#pragma unroll
      for (int q = 7; q >= 0; --q) B[q] = ldB(0, q);
      A[1] = ldA(1);
      A[2] = ldA(2);
      A[3] = ldA(3);
      SCHED();
#pragma unroll
      for (int st = 0; st < 16; ++st) {
        const int a = lag(st);
#pragma unroll
        for (int q = 7 - a; q >= 0; --q) {
          acc[a + q] = __builtin_amdgcn_mfma_f32_16x16x32_f16(A[st & 3], B[q], acc[a + q], 0, 0, 0);
          if (st == 7) { SCHED(); B[q] = ldB(1, q); SCHED(); }
        }
        SCHED();
        if (st + 4 < 16) A[st & 3] = ldA(st + 4);
        if (st < 7 && !(st & 1)) B[7 - a] = ldB(1, 7 - a);
        SCHED();
      }
      STAMP(1 + 3 * layer, 1);
#pragma unroll
      for (int g = 0; g < 8; ++g) lds_st<f32x4>(L_Y + w * 8192 + (64 * g + 16 * h + (r ^ (4 * h))) * 16, acc[g]);
      if (layer == 0 && w < 4) ldW1();
    }
    {
      PHASE_IDS();
      const int c = lane >> 2, h = lane & 3;
      u32 y = L_Y + (64 * w + 16 * h + (c ^ (4 * h))) * 16;
      u32 xo = xt_off + n * 128 + (tid >> 3) * 32768 + (tid & 1) * 64 + ((tid >> 1) & 3) * 16;
      asm volatile("" : "+v"(y), "+v"(xo));
      BAR();
      STAMP(1 + 3 * layer, 2);
      const f32x4 y0 = (lds_ld<f32x4>(y) + lds_ld<f32x4>(y + 8192)) + (lds_ld<f32x4>(y + 16384) + lds_ld<f32x4>(y + 24576));
      const f32x4 y1 = (lds_ld<f32x4>(y + 32768) + lds_ld<f32x4>(y + 40960)) + (lds_ld<f32x4>(y + 49152) + lds_ld<f32x4>(y + 57344));
      const u32 a0 = pkbf(fmaf(y0[0], ks0, base[0][0]), fmaf(y0[1], ks0, base[0][1]));
      const u32 a1 = pkbf(fmaf(y0[2], ks0, base[0][2]), fmaf(y0[3], ks0, base[0][3]));
      const u32 c0 = pkbf(fmaf(y1[0], ks1, base[1][0]), fmaf(y1[1], ks1, base[1][1]));
      const u32 c1 = pkbf(fmaf(y1[2], ks1, base[1][2]), fmaf(y1[3], ks1, base[1][3]));
      const bool odd = lane & 1;
      const u32 r0 = dppu<0xB1>(odd ? a0 : c0), r1 = dppu<0xB1>(odd ? a1 : c1);
      st16(rs, xo, notempty((u32x4){odd ? r0 : a0, odd ? r1 : a1, odd ? c0 : r0, odd ? c1 : r1}));
      STAMP(1 + 3 * layer, 3);
    }
    f32x4 e4, o4;
    auto ffn = [&](auto role) {
      constexpr bool A = decltype(role)::A;
      PHASE_IDS();
      auto hof = [&](int p) {
        const int i = p < 9 ? 2 * p : p < 18 ? 2 * p - 17 : 18 + 2 * (p & 1) + ((p - 18) >> 1);
        return h_off + (T2 * FT + min(kq + 4 * i, FT - 1)) * 1024;
      };
      const u32 w2s = O_W2F + (nt * FT + 12) * 2048;
      bf16x8 W[32];
#pragma unroll
      for (int i = 0; i < 32; ++i)
        W[i] = i < WKEEP ? Wk[i < WKEEP ? i : 0] : __builtin_bit_cast(bf16x8, ld16(rs, lane16, O_W1F + (ftc * 32 + i) * 1024));
      const u32 xs0 = xt_off + (ntile == 3 ? Ta : Tc) * 32768;
      const u32 xs1 = xt_off + (A ? (ntile == 3 ? Tc : Tb) : (ntile == 3 ? Tb : Ta)) * 32768;
      const u32 t16 = tid * 16, q16 = (tid & 255) * 16;
      u32 l0 = (ntile == 3 ? 0 : 65536) + t16, l1 = (ntile == 3 ? 32768 : 0) + (A ? 32768 : 0) + q16;
      u32 xb = hh * 512 + ((lane & 15) >> 2) * 64 + ((lane >> 4) & 1) * 32 + (lane & 3) * 8;
      asm volatile("" : "+v"(l0), "+v"(l1), "+v"(xb));
      u32x4 xa[4], xc[8];
      BAR();
      STAMP(1 + 3 * layer, 4);
      xload<4>(rs, xa, t16, xs0, 8192);
      __builtin_amdgcn_s_barrier();
      xload<8>(rs, xc, q16, xs1, 4096);
      if (layer == 0) {
        float c0 = 0.f, c1 = 0.f;
#pragma unroll
        for (int i = 0; i < 32; ++i) {
          const u32x4 v = __builtin_bit_cast(u32x4, W[i]);
          c0 = dot2(v[0], 0x3f803f80u, c0); c1 = dot2(v[1], 0x3f803f80u, c1);
          c0 = dot2(v[2], 0x3f803f80u, c0); c1 = dot2(v[3], 0x3f803f80u, c1);
        }
        c0 += c1;
        lds_st<float>(L_CS + w * 128 + ((r32 >> 2) & 1) * 64 + (r32 >> 3) * 16 + (r32 & 3) * 4, sw32(c0, c0));
      }
      xpark<4>(rs, xa, t16, xs0, 8192, l0);
      STAMP(1 + 3 * layer, 5);
      BAR();
      const u32 g1_off = O_G1 + layer * 32768;
      if (ntile != 3) {
        float s1 = 0.f, s2 = 0.f;
#pragma unroll
        for (int i = 0; i < 4; ++i) {
          const u32x4 v = __builtin_bit_cast(u32x4, trfrag(xb + 65536 + w * 4096 + i * 1024));
#pragma unroll
          for (int j = 0; j < 4; ++j) { s1 = dot2(v[j], 0x3f803f80u, s1); s2 = dot2(v[j], v[j], s2); }
        }
        lds_st<float>(L_ST + lane * 32 + w * 4, sw32(s1, s2));
        if constexpr (!A) xpark<8>(rs, xc, q16, xs1, 4096, l1);
        BAR();
        if (!A && w == 7) {
          const f32x4 p = lds_ld<f32x4>(L_ST + lane * 32) + lds_ld<f32x4>(L_ST + lane * 32 + 16);
          const float s = ((p[0] + p[1]) + (p[2] + p[3])) * (1.f / SD);
          const auto sw = __builtin_amdgcn_permlane32_swap(__float_as_uint(s), __float_as_uint(s), false, false);
          const float m = __uint_as_float(sw[0]);
          const float v = fmaxf(fmaf(-m, m, __uint_as_float(sw[1])), 0.f) + 1e-5f;
          st16(rs, lane < 32 && pub0 < 41 ? g1_off + (Tc * 32 + lane) * 16 : OOB,
               (u32x4){__float_as_uint(-m), tag, __float_as_uint(v), tag});
        }
      }
      STAMP(1 + 3 * layer, 6);
      const u32 csr = L_CS + w * 128 + hh * 64;
      const u32 hso = (ftw < FT && ntile > 0 ? h_off + ftc * 1024 : OOB) + lane16;
      u32 rot16 = (((r32 + 8 * kq) & 31) + 32 * hh) * 16, r16 = r32 * 16;
      asm volatile("" : "+v"(rot16), "+v"(r16));
      bf16x8 Wr[3][2];
      constexpr int NH = 20;
      u32x4 fh[NH];
      auto hcheck = [&](int p0, int p1) {
        u32 m = 0;
#pragma unroll
        for (int p = p0; p < p1; ++p) m = umax4(m, fh[p % NH]);
        if (__builtin_expect(!none(m == EMPTY), 0)) {
#pragma unroll 1
          for (int s = 0; s < SPIN_MAX; ++s) {
            asm volatile("" ::: "memory");
            m = 0;
#pragma unroll
            for (int p = p0; p < p1; ++p) fh[p % NH] = ld16(rs, lane16, hof(p));
#pragma unroll
            for (int p = p0; p < p1; ++p) m = umax4(m, fh[p % NH]);
            if (none(m == EMPTY)) break;
          }
        }
      };
      {
        f32x16 acc;
        bf16x8 fr[PD];
        u32x4 gb, gc;
        auto stat_wait = [&]() {
#pragma unroll 1
          for (int s = 0; s < SPIN_MAX; ++s) {
            if (none(((gb[1] ^ tag) | (gb[3] ^ tag) | (gc[1] ^ tag) | (gc[3] ^ tag)) != 0)) break;
            asm volatile("" ::: "memory");
            gb = ld16(rs, r16, g1_off + Tb * 512);
            gc = ld16(rs, r16, g1_off + Tc * 512);
          }
          __builtin_amdgcn_s_waitcnt(0x0F70);
        };
        auto epi0 = [&]() {
          const f32x4 p = lds_ld<f32x4>(L_ST + 2048 + lane16);
          float s = p[0] + p[1], t = p[2] + p[3];
          asm volatile("" : "+v"(s), "+v"(t));
          s = (s + t) * (1.f / SD);
          const auto sw = __builtin_amdgcn_permlane32_swap(__float_as_uint(s), __float_as_uint(s), false, false);
          const float m = __uint_as_float(sw[0]);
          st16(rs, hso + Ta * (FT * 1024), g1_epi<false>(acc, -m, fmaxf(fmaf(-m, m, __uint_as_float(sw[1])), 0.f) + 1e-5f, csr));
        };
        auto ldWr = [&](int k) {
          Wr[k >> 1][k & 1] = __builtin_bit_cast(bf16x8, ld16(rs, rot16 + (k & 1) * 1024, O_W2F + (nt * FT + kq + 4 * (k >> 1)) * 2048));
        };
        auto ldst = [&](int k) {
          if (k == 0) gb = ld16(rs, r16, g1_off + Tb * 512);
          if (k == 1) gc = ld16(rs, r16, g1_off + Tc * 512);
        };
        auto last_chain = [&](auto side) {
          if (ntile != 3) {
#pragma unroll
            for (int k = 0; k < 16; ++k) side(k);
          } else {
            g1_chain<false>(acc, W, fr, xb + 65536, side);
          }
        };
        if constexpr (A) {
          g1_pre(fr, xb);
          u32 xm = 0;
          g1_chain<false>(acc, W, fr, xb, [&](int k) {
            if (k >= 8) { xm = umax4(xm, xc[k - 8]); lds_st<u32x4>(l1 + (k - 8) * 4096, xc[k - 8]); }
          });
          if (!none(xm == EMPTY)) xpark<8>(rs, xc, q16, xs1, 4096, l1);
          BAR();
          STAMP(2 + 3 * layer, 0);
          epi0();
          g1_pre(fr, xb + 32768);
          BAR();
          STAMP(2 + 3 * layer, 1);
          g1_chain<true>(acc, W, fr, xb + 32768, [&](int k) {
            if (k < 8) dmak(k, rs, w2s + 57344 + kq * 8192, kq * 8192, lane16);
            else if (k < 10) ldst(k - 8);
          });
          BAR();
          STAMP(2 + 3 * layer, 2);
          stat_wait();
          st16(rs, hso + Tb * (FT * 1024), g1_epi<false>(acc, __uint_as_float(gb[0]), __uint_as_float(gb[2]), csr));
          BAR();
          STAMP(2 + 3 * layer, 3);
          asm volatile("" : "=v"(Wr[0][0]), "=v"(Wr[0][1]), "=v"(Wr[1][0]), "=v"(Wr[1][1]), "=v"(Wr[2][0]), "=v"(Wr[2][1]));
          asm volatile("" : "=v"(fh[0]), "=v"(fh[1]), "=v"(fh[2]), "=v"(fh[3]), "=v"(fh[4]), "=v"(fh[5]), "=v"(fh[6]), "=v"(fh[7]),
                            "=v"(fh[8]), "=v"(fh[18]));
          last_chain([&](int k) {
            if (k < 6) ldWr(k);
            else fh[k < 15 ? k - 6 : 18] = ld16(rs, lane16, hof(k < 15 ? k - 6 : 18));
          });
          fh[19] = ld16(rs, lane16, hof(19));
          BAR();
          STAMP(2 + 3 * layer, 4);
          if (ntile == 3) st16(rs, hso + Tc * (FT * 1024), g1_epi<false>(acc, __uint_as_float(gc[0]), __uint_as_float(gc[2]), csr));
          hcheck(0, 9);
#pragma unroll
          for (int p = 9; p < 18; ++p) fh[p] = ld16(rs, lane16, hof(p));
          STAMP(2 + 3 * layer, 5);
          BAR();
        } else {
          {
            float s1 = 0.f, s2 = 0.f;
            u32 a = xb + kq * 8192;
            asm volatile("" : "+v"(a));
            __builtin_amdgcn_s_setprio(2);
#pragma unroll
            for (int i = 0; i < 8; ++i) {
              const u32x4 v = __builtin_bit_cast(u32x4, trfrag(a + i * 1024));
#pragma unroll
              for (int j = 0; j < 4; ++j) { s1 = dot2(v[j], 0x3f803f80u, s1); s2 = dot2(v[j], v[j], s2); }
            }
            __builtin_amdgcn_s_setprio(0);
            lds_st<float>(L_ST + 2048 + lane16 + kq * 4, sw32(s1, s2));
          }
          if (ntile == 3) xpark<8>(rs, xc, q16, xs1, 4096, l1);
          __builtin_amdgcn_s_waitcnt(0x0F71);
          g1_pre(fr, xb);
          BAR();
          STAMP(2 + 3 * layer, 0);
          g1_chain<true>(acc, W, fr, xb, [&](int k) {
            if (k < 14) dmak(k, rs, w2s + kq * 14336, L_R3 + kq * 14336, lane16);
          });
          BAR();
          STAMP(2 + 3 * layer, 1);
          epi0();
          if (layer == 0 && b == 0) st4(rs, tid == 256 ? O_EP : OOB, 0, tag);
          BAR();
          STAMP(2 + 3 * layer, 2);
          g1_chain<true>(acc, W, fr, xb + 32768, [&](int k) {
            if (k < 6) ldWr(k);
            else if (k < 8) ldst(k - 6);
          });
          BAR();
          STAMP(2 + 3 * layer, 3);
          stat_wait();
          st16(rs, hso + Tb * (FT * 1024), g1_epi<false>(acc, __uint_as_float(gb[0]), __uint_as_float(gb[2]), csr));
          BAR();
          STAMP(2 + 3 * layer, 4);
          last_chain([&](int k) {
            if (k < 8) dmak(k, rs, w2s + 90112 + kq * 8192, 32768 + kq * 8192, lane16);
          });
          WAITV(0);
          STAMP(2 + 3 * layer, 5);
          BAR();
          if (ntile == 3) st16(rs, hso + Tc * (FT * 1024), g1_epi<false>(acc, __uint_as_float(gc[0]), __uint_as_float(gc[2]), csr));
#pragma unroll
          for (int p = 0; p < 11; ++p) fh[p < 9 ? p : p + 9] = ld16(rs, lane16, hof(p < 9 ? p : p + 9));
#pragma unroll
          for (int p = 9; p < 18; ++p) fh[p] = ld16(rs, lane16, hof(p));
          BAR();
        }
      }
      STAMP(2 + 3 * layer, 6);
      f32x16 acc[2];
#pragma unroll
      for (int c = 0; c < 2; ++c)
#pragma unroll
        for (int e = 0; e < 16; ++e) acc[c][e] = 0.f;
      u32 wb0 = kq * 2048 + rot16, wb1 = wb0 + 65536, wb3 = wb0 + L_R3;
      asm volatile("" : "+v"(wb0), "+v"(wb1), "+v"(wb3));
      auto ldW = [&](int p, int c) {
        return lds_ld<bf16x8>((p < 10 ? wb3 + (p - 3) * 8192 : p < 18 ? wb0 + (p - 10) * 8192 : wb1 + (p - 18) * 8192) + c * 1024);
      };
      u16 rsd[8];
      SCHED();
#pragma unroll
      for (int p = 0; p < 22; ++p) {
        if (p % 3 == 0 && p < 21 && (!A || p >= 9)) hcheck(p, p == 18 ? 22 : p + 3);
        if (p < 21 || kq < 2) {
          acc[0] = __builtin_amdgcn_mfma_f32_32x32x16_bf16(Wr[p % 3][0], __builtin_bit_cast(bf16x8, fh[p % NH]), acc[0], 0, 0, 0);
          acc[1] = __builtin_amdgcn_mfma_f32_32x32x16_bf16(Wr[p % 3][1], __builtin_bit_cast(bf16x8, fh[p % NH]), acc[1], 0, 0, 0);
        }
        SCHED();
        if (A && p < 7) dmak(p, rs, w2s + 122880 + kq * 7168, 65536 + kq * 7168, lane16);
        if (p == (A ? 7 : 0) || p == (A ? 8 : 1)) fh[(p & 1) ^ (A ? 1 : 0)] = ld16(rs, lane16, hof(20 + ((p & 1) ^ (A ? 1 : 0))));
        if (p == 8) {
          STAMP(3 + 3 * layer, 2);
          if constexpr (!A) BAR();
        }
        if (p == (A ? 15 : 3)) {
          if constexpr (A) { WAITV(2); BAR(); }
#pragma unroll
          for (int j = 0; j < 8; ++j) rsd[j] = ld2(rs, (8 * hh + j) * 64 + r32 * 2, xt_off + T2 * 32768 + (nt * 64 + kq * 16) * 64);
        }
        if (p + 3 < 22) { Wr[p % 3][0] = ldW(p + 3, 0); Wr[p % 3][1] = ldW(p + 3, 1); }
        SCHED();
      }
      STAMP(3 + 3 * layer, 3);
      if constexpr (A) BAR();
      const u32 pw = L_R3 + kq * 2048 + tt * 24576 + lane16;
#pragma unroll
      for (int q = 1; q < 4; ++q) {
        lds_st<f32x4>(pw + (q - 1) * 8192, (f32x4){acc[0][4 * q], acc[0][4 * q + 1], acc[0][4 * q + 2], acc[0][4 * q + 3]});
        lds_st<f32x4>(pw + (q - 1) * 8192 + 1024, (f32x4){acc[1][4 * q], acc[1][4 * q + 1], acc[1][4 * q + 2], acc[1][4 * q + 3]});
      }
#pragma unroll
      for (int e = 0; e < 4; ++e) { e4[e] = acc[0][e] + b2f(rsd[2 * e]); o4[e] = acc[1][e] + b2f(rsd[2 * e + 1]); }
      asm volatile("" : "+v"(e4), "+v"(o4));
      if (A && layer == 0) {
        u32 e = EMPTY;
        asm volatile("" : "+v"(e));
#pragma unroll
        for (int k = 0; k < 2; ++k) {
          const u32 ho = (ftw + 4 * k < FT && ntile > 0 ? O_H + H_SZ + (ftw + 4 * k) * 1024 : OOB) + lane16;
          st16(rs, ho + Ta * (FT * 1024), (u32x4){e, e, e, e});
          st16(rs, ho + Tb * (FT * 1024), (u32x4){e, e, e, e});
          st16(rs, (ntile == 3 ? ho : OOB) + Tc * (FT * 1024), (u32x4){e, e, e, e});
          st16(rs, O_XT + (2u << 20) + n * 128 + ((tid >> 3) + 32 * k) * 32768 + (tid & 7) * 16, (u32x4){e, e, e, e});
        }
      }
    };
    asm volatile("" : "=v"(e4), "=v"(o4));
    if (w0 < 4) ffn(Role<true>{}); else ffn(Role<false>{});
    PHASE_IDS();
    {
      BAR();
      STAMP(3 + 3 * layer, 4);
#pragma unroll
      for (int q = 1; q < 4; ++q) {
        const u32 a = L_R3 + ((kq - q) & 3) * 2048 + tt * 24576 + lane16 + (q - 1) * 8192;
        e4 += lds_ld<f32x4>(a);
        o4 += lds_ld<f32x4>(a + 1024);
      }
      u32x4 o = {pkbf(e4[0], o4[0]), pkbf(e4[1], o4[1]), pkbf(e4[2], o4[2]), pkbf(e4[3], o4[3])};
      int last = layer;
      asm volatile("" : "+s"(last));
      asm volatile("" : "=v"(base[0][0]), "=v"(base[0][1]), "=v"(base[0][2]), "=v"(base[0][3]),
                        "=v"(base[1][0]), "=v"(base[1][1]), "=v"(base[1][2]), "=v"(base[1][3]));
      if (last == 1) {
        *(u32x4*)(out + (size_t)(T2 * 32 + r32) * SD + nt * 64 + kq * 16 + hh * 8) = o;
        continue;
      }
      float s1 = 0.f, s2 = 0.f;
#pragma unroll
      for (int k = 0; k < 4; ++k) { s1 = dot2(o[k], 0x3f803f80u, s1); s2 = dot2(o[k], o[k], s2); }
      lds_st<float>(L_ST + tt * 1024 + lane16 + kq * 4, sw32(s1, s2));
      {
        const bool b0 = lane & 1, b1 = lane & 2;
        const u32 s0 = dppu<0xB1>(b0 ? o[0] : o[1]), s1_ = dppu<0xB1>(b0 ? o[2] : o[3]);
        const u32 p0 = b0 ? s0 : o[0], p1 = b0 ? o[1] : s0, p2 = b0 ? s1_ : o[2], p3 = b0 ? o[3] : s1_;
        const u32 t0 = dppu<0x4E>(b1 ? p0 : p2), t1 = dppu<0x4E>(b1 ? p1 : p3);
        o = (u32x4){b1 ? t0 : p0, b1 ? t1 : p1, b1 ? p2 : t0, b1 ? p3 : t1};
      }
      st16(rs, O_XP + (nt * 32 + kq * 8 + hh * 4 + (lane & 3)) * 8192 + (T2 * 32 + (r32 & ~3)) * 4, o);
      BAR();
      if (kq == 0) {
        const f32x4 p = lds_ld<f32x4>(L_ST + tt * 1024 + lane16);
        __hip_atomic_fetch_add((GLBP float*)(ws + O_S2) + hh * 2048 + T2 * 32 + r32, (p[0] + p[1]) + (p[2] + p[3]), __ATOMIC_RELAXED,
                               __HIP_MEMORY_SCOPE_AGENT);
      }
    }
    STAMP(3, 5);
    u32 kix[9];
    tap_index(tid, ch, kix);
    WAITV(0);
    BAR();
    if (w == 7) st4(rs, lane == 0 ? O_F3 + b * 128 : OOB, 0, tag);
    STAMP(3, 6);
    expand(tid, ch, kix);
    zero_pad(tid);
    delay(3);
    if (w < 4) poll(rs, O_F3 + tid * 128, 0, tag);
    BAR();
    STAMP(3, 7);
    {
      const u32x4 sa = ld16(rs, tid * 16, O_S2), sb = ld16(rs, tid * 16, O_S2 + 8192);
      const u32x4 xr = ld16(rs, tid * 16, O_XP + n * 8192);
      float mean[4], rstd[4];
#pragma unroll
      for (int i = 0; i < 4; ++i) {
        mean[i] = __uint_as_float(sa[i]) * (1.f / SD);
        rstd[i] = __builtin_amdgcn_rsqf(fmaxf(fmaf(__uint_as_float(sb[i]), 1.f / SD, -mean[i] * mean[i]), 0.f) + 1e-5f);
      }
      normalize(tid, xr, mean, rstd);
    }
  }
}

extern "C" void kernel_launch(void* const* d_in, const int* in_sizes, int n_in, void* d_out, int out_size,
                              void* d_ws, size_t ws_size, hipStream_t stream) {
  k_all<<<256, 512, 0, stream>>>((const u16*)d_in[0], (const u16*)d_in[1], (const u16*)d_in[2], (const u16*)d_in[3],
                                 (const u16*)d_in[4], (const u16*)d_in[5], (const u16*)d_in[6], (const u16*)d_in[8],
                                 (const u16*)d_in[9], (const u16*)d_in[10], (char*)d_ws + ((ws_size - WS_END) & ~(size_t)0x1fffff), (u16*)d_out);
}
```

```cpp
#include <hip/hip_runtime.h>
#include <stdint.h>

typedef unsigned short u16;
typedef uint32_t u32;
typedef unsigned long long u64;
typedef __attribute__((ext_vector_type(2))) float f32x2;
typedef __attribute__((ext_vector_type(4))) float f32x4;
typedef __attribute__((ext_vector_type(16))) float f32x16;
typedef __attribute__((ext_vector_type(2))) _Float16 f16x2;
typedef __attribute__((ext_vector_type(8))) _Float16 f16x8;
typedef __attribute__((ext_vector_type(2))) __bf16 bf16x2;
typedef __attribute__((ext_vector_type(8))) __bf16 bf16x8;
typedef __attribute__((ext_vector_type(4))) short s16x4;
typedef __attribute__((ext_vector_type(2))) u32 u32x2;
typedef __attribute__((ext_vector_type(4))) u32 u32x4;
typedef __amdgpu_buffer_rsrc_t rsrc_t;
#define LDSP __attribute__((address_space(3)))
#define GLBP __attribute__((address_space(1)))
#define DEVI __device__ __forceinline__

constexpr int SL = 2048, SD = 512, NM = 128, DF = 1376, FT = 86;
constexpr int KCOPY = 2080;
constexpr int KCH_B = 2 * KCOPY * 2;
constexpr u32 O_EP = 0;
constexpr u32 O_F3 = 4096;
constexpr u32 O_G0 = O_F3 + 32768;
constexpr u32 O_G1 = O_G0 + 32768;
constexpr u32 O_S2 = O_G1 + 65536;
constexpr u32 O_W2F = O_S2 + 32768;
constexpr u32 H_SZ = 64 * FT * 1024;
constexpr u32 O_XT = O_W2F + 8 * FT * 2048 + 4096;
constexpr u32 O_H = O_XT + (2u << 20);
constexpr u32 O_XP = O_H + H_SZ;
constexpr u32 LAY_SZ = (4u << 20) + H_SZ;
constexpr u32 O_XG = O_H + LAY_SZ + (6u << 20);
constexpr u32 WS_END = O_XG + (4u << 20);
constexpr int L_Y = 0;
constexpr int TBP = 320, L_TB = 0, TB_W = 26 * TBP;
constexpr int L_K = 65536;
constexpr int L_U = L_K + 2 * KCH_B, U_CH = (240 + 2048) * 2;
constexpr int L_F = 91392;
constexpr int L_R3 = 98304;
constexpr int L_ST = 156928;
constexpr int L_CS = L_ST + 3072;
constexpr int L_TH = L_CS + 1024;
constexpr int L_RS = L_TH + 2640;
constexpr int L_SZ = 163840;
constexpr int SPIN_MAX = 1 << 16;
#define X_DELAY 2
#define CHAIN_PRIO 0
#define RAW0 8
#define RAW1 12
#define RAW2 16
#define S3_GATE 0
#define B_PRIO 1
#define B_AHEAD 4
#define T2_DELAY 6
#define B_MID 0
#define X_ROUND 2
#define GRAN_AT 1
#define HCK 0
#define ODD_DELAY 0
#define ST_A 4
#define S3_DELAY 8

DEVI float bflo(u32 v) { return __uint_as_float(v << 16); }
DEVI float bfhi(u32 v) { return __uint_as_float(v & 0xffff0000u); }
DEVI float b2f(u16 v) { return __uint_as_float(((u32)v) << 16); }
DEVI u32 pkbf(float a, float b) { bf16x2 r = __builtin_convertvector((f32x2){a, b}, bf16x2); return __builtin_bit_cast(u32, r); }
DEVI u32 pkf16(float a, float b) { f16x2 r = __builtin_convertvector((f32x2){a, b}, f16x2); return __builtin_bit_cast(u32, r); }
DEVI float rbf(float a) { return bflo(pkbf(a, 0.f)); }
template <int C> DEVI float dppf(float v) { return __uint_as_float(__builtin_amdgcn_update_dpp(0u, __float_as_uint(v), C, 0xf, 0xf, true)); }
template <int C> DEVI u32 dppu(u32 v) { return __builtin_amdgcn_update_dpp(0u, v, C, 0xf, 0xf, true); }
DEVI float sw16(float v) { auto r = __builtin_amdgcn_permlane16_swap(__float_as_uint(v), __float_as_uint(v), false, false); return __uint_as_float(r[0]) + __uint_as_float(r[1]); }
DEVI float sw32(float a, float b) { auto r = __builtin_amdgcn_permlane32_swap(__float_as_uint(a), __float_as_uint(b), false, false); return __uint_as_float(r[0]) + __uint_as_float(r[1]); }
DEVI float wsum(float v) { v += dppf<0xB1>(v); v += dppf<0x4E>(v); v += dppf<0x141>(v); v += dppf<0x140>(v); v = sw16(v); return sw32(v, v); }
DEVI float wmax(float v) {
  v = fmaxf(v, dppf<0xB1>(v)); v = fmaxf(v, dppf<0x4E>(v)); v = fmaxf(v, dppf<0x141>(v)); v = fmaxf(v, dppf<0x140>(v));
  auto r = __builtin_amdgcn_permlane16_swap(__float_as_uint(v), __float_as_uint(v), false, false);
  v = fmaxf(__uint_as_float(r[0]), __uint_as_float(r[1]));
  r = __builtin_amdgcn_permlane32_swap(__float_as_uint(v), __float_as_uint(v), false, false);
  return fmaxf(__uint_as_float(r[0]), __uint_as_float(r[1]));
}
DEVI float dot2(u32 a, u32 b, float c) { return __builtin_amdgcn_fdot2_f32_bf16(__builtin_bit_cast(bf16x2, a), __builtin_bit_cast(bf16x2, b), c, false); }

#define WAITV(n) asm volatile("s_waitcnt vmcnt(" #n ")" ::: "memory")
#define WAITL0() asm volatile("s_waitcnt lgkmcnt(0)" ::: "memory")
#define SCHED() __builtin_amdgcn_sched_barrier(0)
#define BAR() do { asm volatile("s_waitcnt lgkmcnt(0)\n\ts_barrier" ::: "memory"); } while (0)

#define STAMP(k, i)
#define RETRY(p)

DEVI u32x4 ld16(rsrc_t rs, u32 voff, u32 soff) { return __builtin_amdgcn_raw_buffer_load_b128(rs, voff, soff, 16); }
DEVI u32 ld4(rsrc_t rs, u32 voff, u32 soff) { return __builtin_amdgcn_raw_buffer_load_b32(rs, voff, soff, 16); }
DEVI u16 ld2(rsrc_t rs, u32 voff, u32 soff) { return __builtin_amdgcn_raw_buffer_load_b16(rs, voff, soff, 16); }
DEVI void st16(rsrc_t rs, u32 off, u32x4 v) { __builtin_amdgcn_raw_buffer_store_b128(v, rs, off, 0, 16); }
DEVI void st4(rsrc_t rs, u32 voff, u32 soff, u32 v) { __builtin_amdgcn_raw_buffer_store_b32(v, rs, voff, soff, 16); }
template <int K> DEVI void dma1(rsrc_t rs, u32 goff, u32 lds, u32 lane16) {
  const u32 g = goff + (K & ~3) * 1024;
  if constexpr ((K & 3) == 0)
    asm volatile("s_mov_b32 m0, %0\n\ts_nop 0\n\tbuffer_load_dwordx4 %1, %2, %3 offen sc1 lds"
                 :: "s"(lds + K * 1024), "v"(lane16), "s"(rs), "s"(g) : "memory");
  else
    asm volatile("buffer_load_dwordx4 %0, %1, %2 offen offset:%3 sc1 lds" :: "v"(lane16), "s"(rs), "s"(g), "i"((K & 3) * 1024) : "memory");
}
DEVI void dmak(int k, rsrc_t rs, u32 goff, u32 lds, u32 lane16) {
  switch (k) {
#define DK(n) case n: dma1<n>(rs, goff, lds, lane16); break;
    DK(0) DK(1) DK(2) DK(3) DK(4) DK(5) DK(6) DK(7) DK(8) DK(9) DK(10) DK(11) DK(12) DK(13)
#undef DK
  }
}
DEVI bool none(bool p) { return __builtin_amdgcn_ballot_w64(p) == 0; }
constexpr u32 OOB = 0x80000000u;
DEVI void poll(rsrc_t rs, u32 f, u32 voff, u32 soff, u32 tag) {
#pragma unroll 1
  for (int s = 0; s < SPIN_MAX; ++s) {
    if (none(f != tag)) break;
    asm volatile("" ::: "memory");
    f = ld4(rs, voff, soff);
  }
  asm volatile("" ::: "memory");
}
DEVI void poll(rsrc_t rs, u32 voff, u32 soff, u32 tag) {
  asm volatile("" ::: "memory");
  poll(rs, ld4(rs, voff, soff), voff, soff, tag);
}
constexpr u32 EMPTY = 0xffffffffu;
DEVI u32 umax4(u32 m, const u32x4& v) { return max(max(max(max(m, v[0]), v[1]), v[2]), v[3]); }
DEVI u32x4 notempty(u32x4 v) { return (u32x4){min(v[0], EMPTY - 1), min(v[1], EMPTY - 1), min(v[2], EMPTY - 1), min(v[3], EMPTY - 1)}; }
template <int N> DEVI void xload(rsrc_t rs, u32x4 (&v)[N], u32 voff, u32 soff, u32 stride) {
#pragma unroll
  for (int i = 0; i < N; ++i) v[i] = ld16(rs, voff, soff + i * stride);
}
template <int N> DEVI bool xfull(const u32x4 (&v)[N]) {
  u32 m = 0;
#pragma unroll
  for (int i = 0; i < N; ++i) m = umax4(m, v[i]);
  return none(m == EMPTY);
}
template <int N> DEVI void xretry(rsrc_t rs, u32x4 (&v)[N], u32 voff, u32 soff, u32 stride) {
#pragma unroll 1
  for (int s = 0; s < SPIN_MAX; ++s) {
    asm volatile("" ::: "memory");
    u32x4 t[N];
#pragma unroll
    for (int i = 0; i < N; ++i) t[i] = ld16(rs, umax4(0, v[i]) == EMPTY ? voff : OOB, soff + i * stride);
#pragma unroll
    for (int i = 0; i < N; ++i) v[i] = umax4(0, v[i]) == EMPTY ? t[i] : v[i];
    if (xfull<N>(v)) break;
    __builtin_amdgcn_s_sleep(4);
  }
}
template <int N> DEVI void xpark(rsrc_t rs, u32x4 (&v)[N], u32 voff, u32 soff, u32 stride, u32 lds) {
  if (!xfull<N>(v)) xretry<N>(rs, v, voff, soff, stride);
#pragma unroll
  for (int i = 0; i < N; ++i) *(LDSP u32x4*)(size_t)(lds + i * stride) = v[i];
}
DEVI void delay(int n) { for (int i = 0; i < n; ++i) __builtin_amdgcn_s_sleep(2); }

DEVI f32x2 cexp(float are, float aim, float p) {
  const float e = __builtin_amdgcn_exp2f(are * p), ph = __builtin_amdgcn_fractf(aim * p);
  return (f32x2){__builtin_amdgcn_cosf(ph), __builtin_amdgcn_sinf(ph)} * (f32x2){e, e};
}
DEVI f32x2 cmul(f32x2 a, f32x2 b) {
  f32x2 r;
  asm("v_pk_mul_f32 %0, %1, %2 op_sel:[0,0] op_sel_hi:[0,1]\n\t"
      "v_pk_fma_f32 %0, %1, %2, %0 op_sel:[1,1,0] op_sel_hi:[1,0,1] neg_lo:[1,0,0]" : "=&v"(r) : "v"(a), "v"(b));
  return r;
}
DEVI f16x8 cmul4(f32x4 a0, f32x4 a1, f32x4 b0, f32x4 b1) {
  f32x2 r0, r1, r2, r3;
  asm("v_pk_mul_f32 %0, %4, %8 op_sel:[0,0] op_sel_hi:[0,1]\n\tv_pk_mul_f32 %1, %5, %9 op_sel:[0,0] op_sel_hi:[0,1]\n\t"
      "v_pk_mul_f32 %2, %6, %10 op_sel:[0,0] op_sel_hi:[0,1]\n\tv_pk_mul_f32 %3, %7, %11 op_sel:[0,0] op_sel_hi:[0,1]\n\t"
      "v_pk_fma_f32 %0, %4, %8, %0 op_sel:[1,1,0] op_sel_hi:[1,0,1] neg_lo:[1,0,0]\n\t"
      "v_pk_fma_f32 %1, %5, %9, %1 op_sel:[1,1,0] op_sel_hi:[1,0,1] neg_lo:[1,0,0]\n\t"
      "v_pk_fma_f32 %2, %6, %10, %2 op_sel:[1,1,0] op_sel_hi:[1,0,1] neg_lo:[1,0,0]\n\t"
      "v_pk_fma_f32 %3, %7, %11, %3 op_sel:[1,1,0] op_sel_hi:[1,0,1] neg_lo:[1,0,0]"
      : "=&v"(r0), "=&v"(r1), "=&v"(r2), "=&v"(r3)
      : "v"(a0.xy), "v"(a0.zw), "v"(a1.xy), "v"(a1.zw), "v"(b0.xy), "v"(b0.zw), "v"(b1.xy), "v"(b1.zw));
  return __builtin_bit_cast(f16x8, (u32x4){pkf16(r0[0], r0[1]), pkf16(r1[0], r1[1]), pkf16(r2[0], r2[1]), pkf16(r3[0], r3[1])});
}
DEVI s16x4 trread(u32 a) { return __builtin_amdgcn_ds_read_tr16_b64_v4i16((LDSP s16x4*)(size_t)a); }
DEVI bf16x8 trfrag(u32 a) {
  const s16x4 lo = trread(a), hi = trread(a + 256);
  return __builtin_bit_cast(bf16x8, __builtin_shufflevector(lo, hi, 0, 1, 2, 3, 4, 5, 6, 7));
}
template <class T> DEVI T lds_ld(u32 a) { return *(const LDSP T*)(size_t)a; }
template <class T> DEVI void lds_st(u32 a, T v) { *(LDSP T*)(size_t)a = v; }

constexpr int PD = 4;
DEVI void g1_pre(bf16x8 (&fr)[PD], const u32 (&xq)[4], int toff) {
#pragma unroll
  for (int i = 0; i < PD; ++i) fr[i] = trfrag(xq[0] + toff + i * 1024);
}
template <int ST, class F>
DEVI float g1_chain(f32x16& acc, const bf16x8 (&W)[32], bf16x8 (&fr)[PD], const u32 (&xq)[4], int toff, u32 stdst, F side) {
#pragma unroll
  for (int e = 0; e < 16; ++e) acc[e] = 0.f;
  f32x4 q1 = {0.f, 0.f, 0.f, 0.f}, q2 = {0.f, 0.f, 0.f, 0.f};
  u32 one2 = 0x3f803f80u;
  if (ST) asm volatile("" : "+v"(one2));
  const s16x4 ones = __builtin_bit_cast(s16x4, (u32x2){one2, one2});
  auto fin = [&]() {
    const bool b0 = stdst & 16, b1 = stdst & 32;
    return sw32(q1[0], b1 ? (b0 ? q2[3] : q2[2]) : (b0 ? q2[1] : q2[0]));
  };
  if (CHAIN_PRIO) __builtin_amdgcn_s_setprio(1);
  SCHED();
#pragma unroll
  for (int i = 0; i < 32; ++i) {
    if (i < ST) {
      const u32x4 v = __builtin_bit_cast(u32x4, fr[i % PD]);
#pragma unroll
      for (int j = 0; j < 2; ++j) {
        const s16x4 x = __builtin_bit_cast(s16x4, (u32x2){v[2 * j], v[2 * j + 1]});
        q2 = __builtin_amdgcn_mfma_f32_4x4x4bf16_1k(x, x, q2, 0, 0, 0);
        q1 = __builtin_amdgcn_mfma_f32_4x4x4bf16_1k(ones, x, q1, 0, 0, 0);
      }
      SCHED();
    }
    acc = __builtin_amdgcn_mfma_f32_32x32x16_bf16(W[i], fr[i % PD], acc, 0, 0, 0);
    SCHED();
    if (ST > 0 && ST < 32 && i == ST + 1) lds_st<float>(stdst, fin());
    if (i + PD < 32) fr[i % PD] = trfrag(xq[(i + PD) >> 3] + toff + ((i + PD) & 7) * 1024);
    if (!(i & 1)) side(i >> 1);
    SCHED();
  }
  if (CHAIN_PRIO) __builtin_amdgcn_s_setprio(0);
  return ST == 32 ? fin() : 0.f;
}
template <bool V> struct Role { static constexpr bool A = V; };
template <int M> struct Mode { static constexpr int V = M; };
template <bool PK, class F> DEVI u32x4 g1_epi(const f32x16& acc, float nm, float v, u32 cssrc, F mid) {
  f32x4 cs[4];
#pragma unroll
  for (int q = 0; q < 4; ++q) cs[q] = lds_ld<f32x4>(cssrc + q * 16);
  const float rs = __builtin_amdgcn_rsqf(v);
  const float k0 = rs * -2.302208198f, k1 = rs * rs * rs * -0.102943239f;
  if constexpr (PK) {
    u32x4 o;
    f32x2 g[4], e[4];
#pragma unroll
    for (int p = 0; p < 4; ++p) {
      g[p] = __builtin_elementwise_fma((f32x2){cs[p >> 1][2 * (p & 1)], cs[p >> 1][2 * (p & 1) + 1]}, (f32x2){nm, nm}, (f32x2){acc[2 * p], acc[2 * p + 1]});
      const f32x2 t = g[p] * __builtin_elementwise_fma(g[p] * g[p], (f32x2){k1, k1}, (f32x2){k0, k0});
      e[p] = (f32x2){__builtin_amdgcn_exp2f(t[0]), __builtin_amdgcn_exp2f(t[1])};
    }
#pragma unroll
    for (int p = 0; p < 4; ++p) {
      const f32x2 d = __builtin_elementwise_fma(e[p], (f32x2){v, v}, (f32x2){v, v});
      const f32x2 uu = __builtin_elementwise_fma((f32x2){cs[2 + (p >> 1)][2 * (p & 1)], cs[2 + (p >> 1)][2 * (p & 1) + 1]}, (f32x2){nm, nm},
                                                 (f32x2){acc[8 + 2 * p], acc[9 + 2 * p]});
      const f32x2 r = g[p] * uu * (f32x2){__builtin_amdgcn_rcpf(d[0]), __builtin_amdgcn_rcpf(d[1])};
      o[p] = pkbf(r[0], r[1]);
    }
    return notempty(o);
  }
  u32x4 o;
#pragma unroll
  for (int q = 0; q < 2; ++q) {
    float g0 = cs[q][0], g1 = cs[q][1], g2 = cs[q][2], g3 = cs[q][3];
    float u0 = cs[2 + q][0], u1 = cs[2 + q][1], u2 = cs[2 + q][2], u3 = cs[2 + q][3];
    float t0, t1, t2, t3;
    asm("v_fma_f32 %0, %20, %0, %12\n\tv_fma_f32 %1, %20, %1, %13\n\tv_fma_f32 %2, %20, %2, %14\n\tv_fma_f32 %3, %20, %3, %15\n\t"
        "v_mul_f32 %8, %0, %0\n\tv_mul_f32 %9, %1, %1\n\tv_mul_f32 %10, %2, %2\n\tv_mul_f32 %11, %3, %3\n\t"
        "v_fma_f32 %8, %8, %22, %21\n\tv_fma_f32 %9, %9, %22, %21\n\tv_fma_f32 %10, %10, %22, %21\n\tv_fma_f32 %11, %11, %22, %21\n\t"
        "v_mul_f32 %8, %0, %8\n\tv_mul_f32 %9, %1, %9\n\tv_mul_f32 %10, %2, %10\n\tv_mul_f32 %11, %3, %11\n\t"
        "v_exp_f32 %8, %8\n\tv_exp_f32 %9, %9\n\tv_exp_f32 %10, %10\n\tv_exp_f32 %11, %11\n\t"
        "v_fma_f32 %4, %20, %4, %16\n\tv_fma_f32 %5, %20, %5, %17\n\tv_fma_f32 %6, %20, %6, %18\n\tv_fma_f32 %7, %20, %7, %19\n\t"
        "v_fma_f32 %8, %8, %23, %23\n\tv_fma_f32 %9, %9, %23, %23\n\tv_fma_f32 %10, %10, %23, %23\n\tv_fma_f32 %11, %11, %23, %23\n\t"
        "v_rcp_f32 %8, %8\n\tv_rcp_f32 %9, %9\n\tv_rcp_f32 %10, %10\n\tv_rcp_f32 %11, %11\n\t"
        "v_mul_f32 %0, %0, %4\n\tv_mul_f32 %1, %1, %5\n\tv_mul_f32 %2, %2, %6\n\tv_mul_f32 %3, %3, %7\n\t"
        "v_mul_f32 %0, %0, %8\n\tv_mul_f32 %1, %1, %9\n\tv_mul_f32 %2, %2, %10\n\tv_mul_f32 %3, %3, %11"
        : "+v"(g0), "+v"(g1), "+v"(g2), "+v"(g3), "+v"(u0), "+v"(u1), "+v"(u2), "+v"(u3), "=&v"(t0), "=&v"(t1), "=&v"(t2), "=&v"(t3)
        : "v"(acc[4 * q]), "v"(acc[4 * q + 1]), "v"(acc[4 * q + 2]), "v"(acc[4 * q + 3]),
          "v"(acc[8 + 4 * q]), "v"(acc[9 + 4 * q]), "v"(acc[10 + 4 * q]), "v"(acc[11 + 4 * q]), "v"(nm), "v"(k0), "v"(k1), "v"(v));
    o[2 * q] = pkbf(g0, g1);
    o[2 * q + 1] = pkbf(g2, g3);
    if (q == 0) mid();
  }
  return notempty(o);
}
#define MFMA_DRAIN(a) asm volatile("s_nop 15\n\ts_nop 3" : "+v"(a))

__device__ const u32 k_map[256] = {
    0x2e003, 0x62e003, 0x432053, 0xa32053, 0x3370b3, 0x9370b3, 0x23a116, 0x83f112, 0x12e003, 0x72e003, 0x33063, 0x633063, 0x4370b3, 0xa370b3, 0x33b116, 0x93f112,
    0x22e003, 0x82e003, 0x133063, 0x733063, 0x2d0c6, 0x6330c6, 0x43c116, 0xa3f112, 0x32e003, 0x92e003, 0x233063, 0x833063, 0x12e0c6, 0x7340c6, 0x3c123, 0x63c123,
    0x42e003, 0xa2e003, 0x333063, 0x933063, 0x22f0c6, 0x8350c6, 0x13c123, 0x73c123, 0x52e003, 0x52f013, 0x433063, 0xa33063, 0x3300c6, 0x9360c6, 0x23c123, 0x83c123,
    0x2f013, 0x62f013, 0x533063, 0x527076, 0x4310c6, 0xa370c6, 0x33c123, 0x93c123, 0x12f013, 0x72f013, 0x22076, 0x628076, 0x5320c6, 0x5380d3, 0x43c123, 0xa3c123,
    0x22f013, 0x82f013, 0x123076, 0x729076, 0x380d3, 0x6380d3, 0x53c123, 0x53d133, 0x32f013, 0x92f013, 0x224076, 0x82a076, 0x1380d3, 0x7380d3, 0x3d133, 0x63d133,
    0x42f013, 0xa2f013, 0x325076, 0x92b076, 0x2380d3, 0x8380d3, 0x13d133, 0x73d133, 0x17026, 0x61d026, 0x426076, 0xa2c076, 0x3380d3, 0x9380d3, 0x23d133, 0x83d133,
    0x118026, 0x71e026, 0x34083, 0x634083, 0x4380d3, 0xa380d3, 0x33d133, 0x93d133, 0x219026, 0x81f026, 0x134083, 0x734083, 0x390e3, 0x6390e3, 0x43d133, 0xa3d133,
    0x31a026, 0x920026, 0x234083, 0x834083, 0x1390e3, 0x7390e3, 0x3e143, 0x63e143, 0x41b026, 0xa21026, 0x334083, 0x934083, 0x2390e3, 0x8390e3, 0x13e143, 0x73e143,
    0x51c026, 0x530033, 0x434083, 0xa34083, 0x3390e3, 0x9390e3, 0x23e143, 0x83e143, 0x30033, 0x630033, 0x534083, 0x535093, 0x4390e3, 0xa390e3, 0x33e143, 0x93e143,
    0x130033, 0x730033, 0x35093, 0x635093, 0x5390e3, 0x53a0f3, 0x43e143, 0xa3e143, 0x230033, 0x830033, 0x135093, 0x735093, 0x3a0f3, 0x63a0f3, 0x53e143, 0x53f153,
    0x330033, 0x930033, 0x235093, 0x835093, 0x13a0f3, 0x73a0f3, 0x3f153, 0x63f153, 0x430033, 0xa30033, 0x335093, 0x935093, 0x23a0f3, 0x83a0f3, 0x13f153, 0x73f153,
    0x31043, 0x631043, 0x435093, 0xa35093, 0x33a0f3, 0x93a0f3, 0x23f153, 0x83f153, 0x131043, 0x731043, 0x360a3, 0x6360a3, 0x43a0f3, 0xa3a0f3, 0x33f153, 0x93f153,
    0x231043, 0x831043, 0x1360a3, 0x7360a3, 0x3b103, 0x63b103, 0x43f153, 0xa3f153, 0x331043, 0x931043, 0x2360a3, 0x8360a3, 0x13b103, 0x73b103, 0x3f162, 0x63f162,
    0x431043, 0xa31043, 0x3360a3, 0x9360a3, 0x23b103, 0x83b103, 0x13f162, 0x73f162, 0x531043, 0x532053, 0x4360a3, 0xa360a3, 0x33b103, 0x93b103, 0x23f162, 0x83f162,
    0x32053, 0x632053, 0x5360a3, 0x5370b3, 0x43b103, 0xa3b103, 0x33f162, 0x93f162, 0x132053, 0x732053, 0x370b3, 0x6370b3, 0x53b103, 0x53d116, 0x43f162, 0xa3f162,
    0x232053, 0x832053, 0x1370b3, 0x7370b3, 0x38116, 0x63e116, 0x53f162, 0x53f160, 0x332053, 0x932053, 0x2370b3, 0x8370b3, 0x139116, 0x73f116, 0x3f160, 0x63f160,
};

__global__ __launch_bounds__(512, 2) void k_all(const u16* __restrict__ u, const u16* __restrict__ logA,
                                                const u16* __restrict__ Aim, const u16* __restrict__ Cre,
                                                const u16* __restrict__ Cim, const u16* __restrict__ logdt,
                                                const u16* __restrict__ dsk, const u16* __restrict__ wg,
                                                const u16* __restrict__ wu, const u16* __restrict__ wd,
                                                char* ws, u16* __restrict__ out) {
  __shared__ __attribute__((aligned(16))) char sm[L_SZ];
  const int tid0 = threadIdx.x, w0 = __builtin_amdgcn_readfirstlane(tid0 >> 6), b = blockIdx.x;
  const int n0 = (b & 7) * 32 + (b >> 3);
  asm volatile("" :: "v"((LDSP char*)sm) : "memory");
  STAMP(0, 0);
  u32 pf;
  {
    u64 pc;
    u32 len;
    asm volatile("s_getpc_b64 %0\nKCODE_BEG:\n\ts_mov_b32 %1, KCODE_END-KCODE_BEG" : "=s"(pc), "=s"(len));
    const u32 o = min((u32)(((b >> 3) + 32 * (tid0 & 15)) * 128), len);
    asm volatile("global_load_dword %0, %1, %2" : "=v"(pf) : "v"(o), "s"(pc) : "memory");
  }
  asm volatile("" :: "s"(u), "s"(logA), "s"(Aim), "s"(Cre), "s"(Cim), "s"(logdt), "s"(dsk), "s"(wg), "s"(wu), "s"(wd), "s"(ws), "s"(out));
  const rsrc_t rs = __builtin_amdgcn_make_buffer_rsrc(ws, 0, WS_END, 0x00020000);
  u32 map0;
#define PHASE_IDS()                                                                               \
  int tid = tid0, w = w0, n = n0;                                                                 \
  u32 tag = tag0, map = map0;                                                                     \
  asm volatile("" : "+v"(tid));                                                                   \
  asm volatile("" : "+s"(w), "+s"(n), "+s"(map), "+s"(tag));                                      \
  const int lane = tid & 63, r32 = lane & 31, hh = lane >> 5, ch = w >> 2;                        \
  const u32 lane16 = lane * 16;                                                                   \
  const int ntile = map & 3, Ta = (map >> 4) & 255, Tb = 23 + Ta, Tc = (map >> 12) & 255;         \
  const int ftw = (map >> 20) * 8 + w, ftc = ftw < FT ? ftw : FT - 1;                             \
  const int nt = n & 7, tt = w >> 2, kq = w & 3, T2 = (n >> 3) + 32 * tt;                         \
  (void)r32; (void)hh; (void)ch; (void)lane16; (void)ftc; (void)nt; (void)kq; (void)T2; (void)Tb; (void)Tc; (void)ntile

  u32 tag0, dsk2;
  float ks0, ks1;
  float base[2][4];
  u32 kb, ub;
  auto normalize = [&](int tid, const u32x4& xr, const float (&mean)[4], const float (&rstd)[4]) {
    const f32x2 d = {bflo(dsk2), bfhi(dsk2)};
    f32x2 un[4];
#pragma unroll
    for (int i = 0; i < 4; ++i) {
      const f32x2 x = {bflo(xr[i]), bfhi(xr[i])};
      un[i] = (x - mean[i]) * rstd[i];
      const f32x2 t = __builtin_elementwise_fma(un[i], d, x);
      base[0][i] = t[0]; base[1][i] = t[1];
    }
    lds_st<u32x2>(L_U + 480 + tid * 8, (u32x2){pkf16(un[0][0], un[1][0]), pkf16(un[2][0], un[3][0])});
    lds_st<u32x2>(L_U + U_CH + 480 + tid * 8, (u32x2){pkf16(un[0][1], un[1][1]), pkf16(un[2][1], un[3][1])});
  };
  auto conv_prep = [&](int tid, int w, u32 (&kix)[9]) {
    u32 z = 0;
    asm volatile("" : "+v"(z));
    if (tid < 76)
      lds_st<u32x4>(tid < 60 ? L_U + (tid >= 30 ? U_CH + (tid - 30) * 16 : tid * 16) : L_K + ((tid - 60) >> 2) * KCOPY * 2 + 4096 + (tid & 3) * 16,
                    (u32x4){z, z, z, z});
    const int i = tid & 255, ch = w >> 2;
    const int sh = min(__builtin_clz((u32)(255 - i) | 16) - 24, 3);
    const float sc = __uint_as_float((124 + sh) << 23);
    float l0 = (float)(2047 - 8 * i);
    asm volatile("" : "+v"(l0));
    const float mg = 8388608.f + (float)(384 - 128 * sh);
#pragma unroll
    for (int j = 0; j < 9; ++j) kix[j] = (__float_as_uint(fmaf(l0 - (float)j, sc, mg)) << 1) + (u32)(L_TH + ch * 1312 - 0x96000000u);
    asm volatile("" : "+v"(kix[8]));
    kix[8] -= i == 223 ? 2 : 0;
    kix[8] = i == 255 ? L_TH + 2624 : kix[8];
#pragma unroll
    for (int j = 0; j < 9; ++j) asm volatile("" : "+v"(kix[j]));
    const int r = tid & 15, h = (tid >> 4) & 3;
    kb = L_K + ch * KCH_B + 4 * (((2031 - r + 8 * h) >> 1) + ((r & 1) ? 0 : KCOPY / 2)) - 128 * (w & 3) - 64 * 9;
    ub = L_U + ch * U_CH + 2 * (224 + 16 * r + 8 * h) - 128 * (w & 3) - 64;
    asm volatile("" : "+v"(kb), "+v"(ub));
  };
  auto expand = [&](int tid, int ch, const u32 (&kix)[9]) {
    const int i = tid & 255;
    u32 kv[9];
#pragma unroll
    for (int j = 0; j < 9; ++j) kv[j] = lds_ld<u16>(kix[j]);
    lds_st<u32x4>(L_K + ch * KCH_B + i * 16,
                  (u32x4){kv[0] | (kv[1] << 16), kv[2] | (kv[3] << 16), kv[4] | (kv[5] << 16), kv[6] | (kv[7] << 16)});
    lds_st<u32x4>(L_K + ch * KCH_B + i * 16 + KCOPY * 2,
                  (u32x4){kv[1] | (kv[2] << 16), kv[3] | (kv[4] << 16), kv[5] | (kv[6] << 16), kv[7] | (kv[8] << 16)});
  };
  bf16x8 W[32];
  rsrc_t wrs;
  u32 wvo, ra[4];
  u32x4 R[16];
  auto ldR = [&](int i) {
    SCHED();
    R[i] = __builtin_amdgcn_raw_buffer_load_b128(wrs, wvo, (((i & 7) >> 1) * 8 + 4 + 2 * (i >> 3) + (i & 1)) * (16 * DF * 2), 0);
    SCHED();
  };
  auto w_put = [&](int r, int j) {
    asm volatile("s_mov_b32 m0, %0\n\ts_nop 0\n\tbuffer_load_dwordx4 %1, %2, %3 offen lds"
                 :: "s"(L_F + j * 8192 + w0 * 1024), "v"(wvo), "s"(wrs), "s"(((j >> 1) * 8 + 2 * r + (j & 1)) * (16 * DF * 2)) : "memory");
  };
  auto w_get = [&](int r) {
#pragma unroll
    for (int j = 0; j < 8; ++j) {
      const u32 a = ra[j >> 1] + (j & 1) * 8192;
      const s16x4 lo = trread(a), hi = trread(a + 1024);
      W[(j >> 1) * 8 + 2 * r + (j & 1)] = __builtin_bit_cast(bf16x8, __builtin_shufflevector(lo, hi, 0, 1, 2, 3, 4, 5, 6, 7));
    }
  };
  {
    const int tid = tid0, w = w0, n = n0, lane = tid & 63, r32 = lane & 31, hh = lane >> 5, ch = w >> 2, kq = w & 3;
    const u32 lane16 = lane * 16;
    const int pidx = n * 2 * NM + w * 32 + r32;
    const u16 pla = logA[pidx], pai = Aim[pidx], pcr = Cre[pidx], pci = Cim[pidx];
    const u32 car = *(const u32*)(Cre + (n * 2 + ch) * NM + lane * 2), cai = *(const u32*)(Cim + (n * 2 + ch) * NM + lane * 2);
    const u32x4 til = *(const u32x4*)(u + (size_t)((b >> 2) * 32 + r32) * SD + (b & 3) * 128 + (w * 2 + hh) * 8);
    const u32x4 row = *(const u32x4*)(u + (size_t)(4 * (8 * (b >> 2) + w) + (b & 3)) * SD + lane * 8);
    const u32 ep = ld4(rs, O_EP, 0);
    int vz = 0;
    asm volatile("" : "+v"(vz));
    const u16 ldt = logdt[2 * n + ch + vz];
    const u32 dskv = ((const u32*)dsk)[n + vz], mapv = k_map[b + vz];
    SCHED();
    __builtin_amdgcn_s_barrier();
    SCHED();
    st16(rs, O_XT + b * 8192 + tid * 16, (u32x4){EMPTY, EMPTY, EMPTY, EMPTY});
    asm volatile("" ::: "memory");
    const u32 pf2 = __builtin_amdgcn_raw_buffer_load_b32(
        __builtin_amdgcn_make_buffer_rsrc((void*)(w < 2 ? wg : w < 4 ? wu : wd), 0, w < 6 ? SD * DF * 2 : 0, 0x00020000),
        lane < 43 ? lane * 64 : OOB, (b * 2 + (w & 1)) * 2752, 0);
    {
      const int pe = (n >> 6) * 32 + (n & 31), pq = pe / 11, pr = pe - pq * 11, up = (n >> 5) & 1;
      const int sl = up ? (pr < 5 ? 6 + pr : pr) : (pr < 6 ? pr : pr - 6);
      wrs = __builtin_amdgcn_make_buffer_rsrc((void*)(ch ? wu : wg), 0, SD * DF * 2, 0x00020000);
      const int q = lane >> 4;
      wvo = (kq * 4 + q) * (DF * 2) + sl * 256 + ((((lane & 15) >> 1) ^ (2 * q + ch)) * 32) + (lane & 1) * 16;
      const int mat = q & 1, rq = (lane & 15) >> 2;
#pragma unroll
      for (int g = 0; g < 4; ++g) {
        ra[g] = L_F + mat * 4096 + (8 * hh + rq) * 256 + ((w ^ (2 * rq + mat)) * 32) + (lane & 3) * 8 + ((g + kq) & 3) * 16384;
        asm volatile("" : "+v"(ra[g]));
      }
    }
    u32 kix[9];
    conv_prep(tid, w, kix);
    const bool odd = lane & 1;
    u32 xgo = O_XG + ((b & 3) * 64 + (w * 2 + hh) * 4 + (odd ? 2 : 0)) * 16384 + ((r32 >> 1) & 1) * 8192 + ((b >> 2) * 8 + (r32 >> 2)) * 16;
    const u32 tb0 = L_TB + w * TB_W + r32 * 8;
    u32 tb = tb0 + hh * 13 * TBP, tbe = tb0 + (hh ? 18 : 12) * TBP, tbf = tb0 + (hh ? 19 : 5) * TBP, tbg = tb0 + (hh ? 20 : 25) * TBP,
        tb1 = tb0 + (hh ? 17 : 0) * TBP;
    float pa = hh ? 4.f : 1.f, pb = hh ? 64.f : 256.f, pe = hh ? 16.f : 2044.f, sg = hh ? -1.f : 1.f, sc = hh ? 16384.f : 1.f;
    u32 aL = L_TB + w * TB_W + (lane >> 4) * 32 + (lane & 3) * TBP, aM = L_TB + w * TB_W + (lane >> 4) * 32 + ((lane & 15) >> 2) * TBP;
    u32 a2x = (lane & 15) == 4 ? aL + 4 * TBP : aL;
    asm volatile("" : "+v"(xgo), "+v"(tb), "+v"(tbe), "+v"(tbf), "+v"(tbg), "+v"(tb1), "+v"(pa), "+v"(pb), "+v"(pe), "+v"(sg), "+v"(sc));
    asm volatile("" : "+v"(aL), "+v"(aM), "+v"(a2x));
    lds_st<f32x2>(tb0 + 13 * TBP, (f32x2){16384.f, 0.f});
    if (tid == 0) lds_st<u32x2>(L_TH + 2624, (u32x2){0, 0});
    SCHED();
    const u32 tag = tag0 = __builtin_amdgcn_readfirstlane(ep) + 1u;
    STAMP(0, 1);
    {
      const u32 r0 = dppu<0xB1>(odd ? til[0] : til[2]), r1 = dppu<0xB1>(odd ? til[1] : til[3]);
      st16(rs, xgo, (u32x4){odd ? r0 : til[0], tag, odd ? til[2] : r0, tag});
      st16(rs, xgo + 16384, (u32x4){odd ? r1 : til[1], tag, odd ? til[3] : r1, tag});
      float s1 = 0.f, s2 = 0.f;
#pragma unroll
      for (int c = 0; c < 4; ++c) { s1 = dot2(row[c], 0x3f803f80u, s1); s2 = dot2(row[c], row[c], s2); }
      float s = sw32(s1, s2);
      s += dppf<0xB1>(s); s += dppf<0x4E>(s); s += dppf<0x141>(s); s += dppf<0x140>(s);
      s = sw16(s);
      if (r32 == 0) lds_st<float>(L_RS + w * 8 + hh * 4, s);
    }
    STAMP(0, 2);
    {
      const float dt = rbf(__expf(b2f(ldt)));
      const float ar = -rbf(__expf(b2f(pla))) * dt * 1.44269504088896f, ai = (b2f(pai) * dt) * 0.159154943091895f, aj = ai * sg;
      const int ce = __builtin_amdgcn_frexp_expf(wmax(fmaxf(fmaxf(fabsf(bflo(car)), fabsf(bfhi(car))), fmaxf(fabsf(bflo(cai)), fabsf(bfhi(cai))))));
      if (lane == 0) lds_st<float>(L_RS + 64 + ch * 4, ldexpf(dt, ce - 7));
      const f32x2 a = cexp(ar, ai, pa), bb = cexp(ar, aj, pb);
      const f32x2 e = cexp(ar, aj, pe), e1k = cexp(ar, ai, 1024.f);
      const f32x2 d = hh ? (f32x2){ldexpf(b2f(pcr), 14 - ce), ldexpf(-b2f(pci), 14 - ce)} : e1k;
      const f32x2 as = a * sc, a2 = cmul(as, a), b2 = cmul(bb, bb), b3 = cmul(b2, bb);
      const f32x2 f = cmul(hh ? e : bb, hh ? e : a2);
      lds_st<f32x2>(tb + 1 * TBP, as);
      lds_st<f32x2>(tb + 2 * TBP, a2);
      lds_st<f32x2>(tb + 3 * TBP, cmul(a2, a));
      lds_st<f32x2>(tb + 4 * TBP, bb);
      lds_st<f32x2>(tb + 6 * TBP, b2);
      lds_st<f32x2>(tb + 7 * TBP, b3);
      lds_st<f32x2>(tb + 8 * TBP, d);
      lds_st<f32x2>(tb + 9 * TBP, cmul(d, bb));
      lds_st<f32x2>(tb + 10 * TBP, cmul(d, b2));
      lds_st<f32x2>(tb + 11 * TBP, cmul(d, b3));
      asm volatile("" ::: "memory");
      lds_st<f32x2>(tbe, e);
      lds_st<f32x2>(tbf, f);
      lds_st<f32x2>(tbg, cmul(f, e));
      lds_st<f32x2>(tb1, (f32x2){1.f, 0.f});
    }
    asm volatile("" :: "v"(pf));
    dsk2 = __builtin_amdgcn_readfirstlane(dskv);
    map0 = __builtin_amdgcn_readfirstlane(mapv);
    asm volatile("" :: "v"(mapv));
    WAITV(3);
    BAR();
    if (w == 3) {
      const f32x2 v = lds_ld<f32x2>(L_RS + (lane & 7) * 8) * (1.f / SD);
      const float r = __builtin_amdgcn_rsqf(fmaxf(fmaf(-v[0], v[0], v[1]), 0.f) + 1e-5f);
      st16(rs, lane < 8 ? O_G0 + (b & 3) * 8192 + ((b >> 2) * 8 + lane) * 16 : OOB, (u32x4){__float_as_uint(v[0]), tag, __float_as_uint(r), tag});
      st16(rs, lane < 8 ? O_S2 + b * 128 + lane16 : OOB, (u32x4){0, 0, 0, 0});
    }
    STAMP(0, 3);
    u32x4 g[6];
    auto gran_loads = [&]() {
#pragma unroll
      for (int i = 0; i < 2; ++i) g[4 + i] = ld16(rs, tid * 16, O_XG + n * 16384 + i * 8192);
#pragma unroll
      for (int i = 0; i < 4; ++i) g[i] = ld16(rs, tid * 16, O_G0 + i * 8192);
    };
    {
      f32x4 a0 = {0, 0, 0, 0}, a1 = {0, 0, 0, 0}, a2 = {0, 0, 0, 0};
      f32x4 tv[2][12];
#pragma unroll
      for (int s = 0; s < 2; ++s) {
        auto L = [&](int i, u32 a) { tv[s][i] = lds_ld<f32x4>(a + s * 128); tv[s][i + 1] = lds_ld<f32x4>(a + s * 128 + 16); };
        L(0, aL + 17 * TBP); L(2, aM + 21 * TBP); L(4, aM + 13 * TBP); L(6, aL); L(8, aL + 4 * TBP); L(10, a2x + 8 * TBP);
      }
#pragma unroll
      for (int s = 0; s < 2; ++s) {
        const f32x4* t = tv[s];
        SCHED(); w_put(0, 4 * s + 0); SCHED();
        const f16x8 bb = cmul4(t[0], t[1], t[2], t[3]);
        SCHED(); w_put(0, 4 * s + 1); SCHED();
        a0 = __builtin_amdgcn_mfma_f32_16x16x32_f16(cmul4(t[6], t[7], t[4], t[5]), bb, a0, 0, 0, 0);
        SCHED(); w_put(0, 4 * s + 2); SCHED();
        a1 = __builtin_amdgcn_mfma_f32_16x16x32_f16(cmul4(t[8], t[9], t[4], t[5]), bb, a1, 0, 0, 0);
        SCHED(); w_put(0, 4 * s + 3); SCHED();
        a2 = __builtin_amdgcn_mfma_f32_16x16x32_f16(cmul4(t[10], t[11], t[4], t[5]), bb, a2, 0, 0, 0);
        if (s == GRAN_AT) { SCHED(); gran_loads(); SCHED(); }
      }
      const u32 T = L_TB + w * TB_W + lane16;
      lds_st<f32x4>(T, a0);
      lds_st<f32x4>(T + 1024, a1);
      lds_st<f32x4>(T + 2048, a2);
    }
    u32 wt[8], wo;
    {
      const int ft = b / 3, fi = ft >> 2, pos = fi < 18 ? (fi & 1) * 9 + (fi >> 1) : 9 + (fi & 1) * 2 + (fi >> 1);
      const rsrc_t wr = __builtin_amdgcn_make_buffer_rsrc((void*)wd, 0, b == ft * 3 ? SD * DF * 2 : 0, 0x00020000);
#pragma unroll
      for (int j = 0; j < 8; ++j)
        wt[j] = __builtin_amdgcn_raw_buffer_load_b32(wr, hh * (4 * SD * 2) + r32 * 4 + (j & 3) * (SD * 2), (ft * 16 + 8 * (j >> 2)) * (SD * 2) + w * 128, 0);
      wo = b == ft * 3 ? O_W2F + (w * FT + 4 * pos + (ft & 3)) * 2048 : OOB;
    }
    STAMP(0, 4);
    BAR();
    if (kq < 3) {
      const u32 T = L_TB + ch * 4 * TB_W + kq * 1024 + lane16;
      const f32x4 v = ((lds_ld<f32x4>(T) + lds_ld<f32x4>(T + TB_W)) + (lds_ld<f32x4>(T + 2 * TB_W) + lds_ld<f32x4>(T + 3 * TB_W))) * 9.5367431640625e-07f;
      const u32 p0 = pkf16(v[0], v[1]), p1 = pkf16(v[2], v[3]);
      const int j = lane & 15, h = lane >> 4;
      const u32 H = L_TH + ch * 1312;
      if (kq == 0) {
        lds_st<u32x2>(H + 2 * (16 * j + 4 * h), (u32x2){p0, p1});
      } else if (kq == 1) {
        lds_st<u32>(H + 2 * (256 + 8 * j + 2 * h), p0);
        lds_st<u16>(H + 2 * (384 + 4 * j + h), (u16)p1);
        lds_st<u16>(H + 2 * (448 + 4 * j + h), (u16)(p1 >> 16));
      } else if (!(h & 1)) {
        const u32 a = H + 2 * (512 + 2 * j + (h >> 1));
        lds_st<u16>(a, (u16)p0);
        lds_st<u16>(a + 64, (u16)(p0 >> 16));
        lds_st<u16>(a + 128, (u16)p1);
        lds_st<u16>(a + 192, (u16)(p1 >> 16));
      } else if (lane == 16) {
        lds_st<u16>(H + 2 * 640, (u16)p0);
      }
    }
    {
      const f32x2 k2 = lds_ld<f32x2>(L_RS + 64);
      ks0 = __uint_as_float(__builtin_amdgcn_readfirstlane(__float_as_uint(k2[0])));
      ks1 = __uint_as_float(__builtin_amdgcn_readfirstlane(__float_as_uint(k2[1])));
    }
    BAR();
    STAMP(0, 5);
    ldR(0); ldR(1);
    expand(tid, ch, kix);
    ldR(2); ldR(3);
    asm volatile("" : "+v"(g[0]), "+v"(g[1]), "+v"(g[2]), "+v"(g[3]), "+v"(g[4]), "+v"(g[5]) :: "memory");
    asm volatile("" :: "v"(wt[7]), "v"(pf2));
    if (wo != OOB) {
      u32x4 lo, hi;
#pragma unroll
      for (int j = 0; j < 4; ++j) {
        lo[j] = __builtin_amdgcn_perm(wt[2 * j + 1], wt[2 * j], 0x05040100u);
        hi[j] = __builtin_amdgcn_perm(wt[2 * j + 1], wt[2 * j], 0x07060302u);
      }
      st16(rs, wo + lane16, lo);
      st16(rs, wo + 1024 + lane16, hi);
    }
    STAMP(0, 6);
    auto gran_ok = [&]() {
      u32 bad = 0;
#pragma unroll
      for (int k = 0; k < 6; ++k) bad |= (g[k][1] ^ tag) | (g[k][3] ^ tag);
      return none(bad != 0);
    };
    if (!gran_ok()) {
#pragma unroll 1
      for (int s = 0; s < SPIN_MAX; ++s) {
        asm volatile("" ::: "memory");
        gran_loads();
        if (gran_ok()) break;
      }
    }
    ldR(4); ldR(5);
    {
      float mean[4], rstd[4];
#pragma unroll
      for (int k = 0; k < 4; ++k) { mean[k] = __uint_as_float(g[k][0]); rstd[k] = __uint_as_float(g[k][2]); }
      normalize(tid, (u32x4){g[4][0], g[4][2], g[5][0], g[5][2]}, mean, rstd);
    }
    ldR(6);
    ldR(7);
    STAMP(0, 7);
  }

  auto conv = [&](auto mode) {
    constexpr int layer = decltype(mode)::V;
    {
      BAR();
      if (layer == 0) {
        w_get(0);
        BAR();
      }
      STAMP(1 + 3 * layer, 0);
      PHASE_IDS();
      f32x4 acc[8];
#pragma unroll
      for (int g = 0; g < 8; ++g) acc[g] = (f32x4){0, 0, 0, 0};
      u32 kp[4];
#pragma unroll
      for (int p = 0; p < 4; ++p) {
        kp[p] = kb - 1024 * p;
        asm volatile("" : "+v"(kp[p]));
      }
      auto lag = [](int st) { return (st & 1) ? 7 - ((st & 7) >> 1) : (st & 7) >> 1; };
      auto ldA = [&](int st) {
        const int a = lag(st);
        const u32 p = kp[a >> 1] + 64 * (9 - (8 * (a & 1) + (st >> 3)));
        return __builtin_bit_cast(f16x8, (u32x4){lds_ld<u32>(p), lds_ld<u32>(p + 4), lds_ld<u32>(p + 8), lds_ld<u32>(p + 12)});
      };
      auto ldB = [&](int rr, int q) { return lds_ld<f16x8>(ub + 64 * (8 * q + 1 - rr)); };
      u32 ce = EMPTY, cof = (layer == 0 ? O_H + b * 40960 : O_H + LAY_SZ + b * 24576) + tid * 16;
      asm volatile("" : "+v"(ce), "+v"(cof));
      f16x8 A[3], B[8];
      A[0] = ldA(0);
#pragma unroll
      for (int q = 7; q >= 0; --q) B[q] = ldB(0, q);
      A[1] = ldA(1);
      A[2] = ldA(2);
      SCHED();
#pragma unroll
      for (int st = 0; st < 16; ++st) {
        const int a = lag(st);
#pragma unroll
        for (int q = 7 - a; q >= 0; --q) {
          acc[a + q] = __builtin_amdgcn_mfma_f32_16x16x32_f16(A[st % 3], B[q], acc[a + q], 0, 0, 0);
          if (st == 7) { SCHED(); B[q] = ldB(1, q); SCHED(); }
        }
        SCHED();
        if (st + 3 < 16) A[st % 3] = ldA(st + 3);
        if (st < 7 && !(st & 1)) B[7 - a] = ldB(1, 7 - a);
        if (st < (layer == 0 ? 5 : 3)) st16(rs, cof + st * 8192, (u32x4){ce, ce, ce, ce});
        if (layer == 0 && st < 8) w_put(1, st);
        if (layer == 0 && st >= 8) ldR(st);
        SCHED();
      }
      STAMP(1 + 3 * layer, 1);
      const int r = lane & 15, h = lane >> 4;
#pragma unroll
      for (int g = 0; g < 8; ++g) lds_st<f32x4>(L_Y + w * 8192 + (64 * g + 16 * h + (r ^ (4 * h))) * 16, acc[g]);
      if (layer == 0) WAITV(8); else WAITV(0);
    }
    {
      PHASE_IDS();
      const int c = lane >> 2, h = lane & 3;
      u32 y = L_Y + (64 * w + 16 * h + (c ^ (4 * h))) * 16;
      u32 xo = O_XT + layer * LAY_SZ + n * 128 + (tid >> 3) * 32768 + (tid & 1) * 64 + ((tid >> 1) & 3) * 16;
      asm volatile("" : "+v"(y), "+v"(xo));
      BAR();
      STAMP(1 + 3 * layer, 2);
      const f32x4 y0 = (lds_ld<f32x4>(y) + lds_ld<f32x4>(y + 8192)) + (lds_ld<f32x4>(y + 16384) + lds_ld<f32x4>(y + 24576));
      const f32x4 y1 = (lds_ld<f32x4>(y + 32768) + lds_ld<f32x4>(y + 40960)) + (lds_ld<f32x4>(y + 49152) + lds_ld<f32x4>(y + 57344));
      const u32 a0 = pkbf(fmaf(y0[0], ks0, base[0][0]), fmaf(y0[1], ks0, base[0][1]));
      const u32 a1 = pkbf(fmaf(y0[2], ks0, base[0][2]), fmaf(y0[3], ks0, base[0][3]));
      const u32 c0 = pkbf(fmaf(y1[0], ks1, base[1][0]), fmaf(y1[1], ks1, base[1][1]));
      const u32 c1 = pkbf(fmaf(y1[2], ks1, base[1][2]), fmaf(y1[3], ks1, base[1][3]));
      const bool odd = lane & 1;
      const u32 r0 = dppu<0xB1>(odd ? a0 : c0), r1 = dppu<0xB1>(odd ? a1 : c1);
      st16(rs, xo, notempty((u32x4){odd ? r0 : a0, odd ? r1 : a1, odd ? c0 : r0, odd ? c1 : r1}));
      STAMP(1 + 3 * layer, 3);
      const u32 xs0 = O_XT + layer * LAY_SZ + Ta * 32768, t16 = tid * 16;
      u32x4 xa[4];
      float cs0 = 0.f, cs1 = 0.f;
      auto colsum = [&](int r) {
#pragma unroll
        for (int j = 0; j < 8; ++j) {
          const u32x4 v = __builtin_bit_cast(u32x4, W[(j >> 1) * 8 + 2 * r + (j & 1)]);
          cs0 = dot2(v[0], 0x3f803f80u, cs0); cs1 = dot2(v[1], 0x3f803f80u, cs1);
          cs0 = dot2(v[2], 0x3f803f80u, cs0); cs1 = dot2(v[3], 0x3f803f80u, cs1);
        }
      };
      if (layer == 0) {
        w_get(1);
        u32 wa = L_F + w * 1024 + lane16, wb = wa - 65536;
        asm volatile("" : "+v"(wa), "+v"(wb));
        BAR();
        STAMP(1 + 3 * layer, 4);
#pragma unroll
        for (int j = 0; j < 8; ++j) lds_st<u32x4>(wa + j * 8192, R[j]);
        asm volatile("" :: "v"(R[15]) : "memory");
        xload<4>(rs, xa, t16, xs0, 8192);
        SCHED();
#pragma unroll
        for (int j = 0; j < 8; ++j) lds_st<u32x4>(wb + j * 8192, R[8 + j]);
        colsum(0);
        colsum(1);
        asm volatile("" : "+v"(cs0), "+v"(cs1));
        BAR();
        w_get(2);
#pragma unroll
        for (int g = 0; g < 4; ++g) { ra[g] -= 65536; asm volatile("" : "+v"(ra[g])); }
        w_get(3);
        colsum(2);
        colsum(3);
        cs0 += cs1;
        lds_st<float>(L_CS + w * 128 + ((r32 >> 2) & 1) * 64 + (r32 >> 3) * 16 + (r32 & 3) * 4, sw32(cs0, cs0));
        BAR();
        xpark<4>(rs, xa, t16, xs0, 8192, t16);
        STAMP(1 + 3 * layer, 5);
      }
    }
  };
  conv(Mode<0>{});
#pragma unroll 1
  for (int layer = 0; layer < 2; ++layer) {
    const u32 xt_off = O_XT + layer * LAY_SZ, h_off = O_H + layer * LAY_SZ;
    f32x4 e4, o4;
    auto ffn = [&](auto role) {
      constexpr bool A = decltype(role)::A;
      PHASE_IDS();
      auto hof = [&](int p) {
        const int i = p < 9 ? 2 * p : p < 18 ? 2 * p - 17 : 18 + 2 * (p & 1) + ((p - 18) >> 1);
        return h_off + (T2 * FT + min(kq + 4 * i, FT - 1)) * 1024;
      };
      const u32 w2s = O_W2F + (nt * FT + 12) * 2048;
      u32 xq[4];
#pragma unroll
      for (int g = 0; g < 4; ++g) xq[g] = hh * 512 + ((lane & 15) >> 2) * 64 + ((lane >> 4) & 1) * 32 + (lane & 3) * 8 + ((g + kq) & 3) * 8192;
      bf16x8 fr[PD];
      u32 csr = L_CS + w * 128 + hh * 64, str = L_ST + lane16;
      const u32 hso = (ftw < FT && ntile > 0 ? h_off + ftc * 1024 : OOB) + lane16;
      u32 hsa = hso + Ta * (FT * 1024), hsb = hso + Tb * (FT * 1024), hsc = hso + Tc * (FT * 1024);
      u32 rot16 = (((r32 + 8 * kq) & 31) + 32 * hh) * 16, r16 = r32 * 16;
      asm volatile("" : "+v"(r16), "+v"(xq[0]), "+v"(xq[1]), "+v"(xq[2]), "+v"(xq[3]), "+v"(csr), "+v"(str), "+v"(rot16), "+v"(hsa), "+v"(hsb), "+v"(hsc));
      u32 xsb = xt_off + Tb * 32768 + kq * 8192, xsc = xt_off + Tc * 32768 + kq * 8192, stw = str + kq * 4;
      asm volatile("" : "+s"(xsb), "+s"(xsc));
      asm volatile("" : "+v"(stw));
      const int sl = map >> 20, jt = (sl >> 2) & 1, Tj = jt ? Tc : Tb;
      const bool job = sl < 4 * (ntile - 1);
      u32 ja = job ? lane * 64 : OOB;
      const u32 js = (jt ? xsc : xsb) + (sl & 3) * 16;
      u32x4 jv[2];
      if (!A) asm volatile("" : "+v"(ja), "=v"(jv[0]), "=v"(jv[1]));
      if (layer == 1) {
        u32x4 xa[4];
        const u32 xs0 = xt_off + Ta * 32768, t16 = tid * 16;
        delay(X_DELAY);
        BAR();
        STAMP(4, 4);
        xload<4>(rs, xa, t16, xs0, 8192);
        if (!A) xload<2>(rs, jv, ja, js, 4096);
        xpark<4>(rs, xa, t16, xs0, 8192, t16);
        STAMP(4, 5);
      } else {
        if (!A) xload<2>(rs, jv, ja, js, 4096);
      }
      BAR();
      STAMP(1 + 3 * layer, 6);
      g1_pre(fr, xq, 0);
      bf16x8 Wr[3][2];
      u32x4 fh[13];
      auto slot = [](int p) { return p < 18 ? p % 9 : p - 9; };
      auto hcheck = [&](int p0, int p1) {
        u32 m = 0;
#pragma unroll
        for (int p = p0; p < p1; ++p) m = umax4(m, fh[slot(p)]);
        if (__builtin_expect(!none(m == EMPTY), 0)) {
#pragma unroll 1
          for (int s = 0; s < SPIN_MAX; ++s) {
            asm volatile("" ::: "memory");
            m = 0;
#pragma unroll
            for (int p = p0; p < p1; ++p)
              if (!none(umax4(0, fh[slot(p)]) == EMPTY)) fh[slot(p)] = ld16(rs, lane16, hof(p));
#pragma unroll
            for (int p = p0; p < p1; ++p) m = umax4(m, fh[slot(p)]);
            RETRY(p0);
            if (none(m == EMPTY)) break;
          }
        }
      };
      {
        f32x16 acc;
        auto nop = []() {};
        const u32 g1_off = O_G1 + layer * 32768;
        u32x4 gb, gc;
        auto refetch = [&](int T, int t, int k0, int k1) __attribute__((always_inline)) {
#pragma unroll 1
          for (int k = k0; k < k1; ++k) {
            u32x4 v[1];
            xload<1>(rs, v, lane16, xt_off + T * 32768 + k * 1024, 0);
            xpark<1>(rs, v, lane16, xt_off + T * 32768 + k * 1024, 0, t * 32768 + k * 1024 + lane16);
          }
          WAITL0();
        };
        auto recheck = [&](int T, int t, int toff, auto redo) __attribute__((always_inline)) {
          if (__builtin_expect(!none(acc[0] != acc[0]), 0)) {
            refetch(T, t, 0, 32);
            g1_pre(fr, xq, toff);
            g1_chain<0>(acc, W, fr, xq, toff, 0, [](int) {});
            MFMA_DRAIN(acc);
            redo();
          }
        };
        auto epi0 = [&]() {
          const f32x4 p = lds_ld<f32x4>(str);
          float s = p[0] + p[1], q = p[2] + p[3];
          asm volatile("" : "+v"(s), "+v"(q));
          s = (s + q) * (1.f / SD);
          const auto sw = __builtin_amdgcn_permlane32_swap(__float_as_uint(s), __float_as_uint(s), false, false);
          const float m = __uint_as_float(sw[0]);
          st16(rs, hsa, g1_epi<false>(acc, -m, fmaxf(fmaf(-m, m, __uint_as_float(sw[1])), 0.f) + 1e-5f, csr, nop));
        };
        const int Tg = ntile == 3 ? Tc : Tb;
        auto ldst = [&](int k) {
          if (k == 0) gb = ld16(rs, r16, g1_off + Tb * 512);
          if (k == 1) gc = ld16(rs, r16, g1_off + Tg * 512);
        };
        auto stat_wait = [&]() {
          if (!none(((gb[1] ^ tag) | (gb[3] ^ tag) | (gc[1] ^ tag) | (gc[3] ^ tag)) != 0)) {
#pragma unroll 1
            for (int s = 0; s < SPIN_MAX; ++s) {
              asm volatile("" ::: "memory");
              ldst(0);
              ldst(1);
              if (none(((gb[1] ^ tag) | (gb[3] ^ tag) | (gc[1] ^ tag) | (gc[3] ^ tag)) != 0)) break;
            }
          }
        };
        auto ldWr = [&](int k) {
          Wr[k >> 1][k & 1] = __builtin_bit_cast(bf16x8, ld16(rs, rot16 + (k & 1) * 1024, O_W2F + (nt * FT + kq + 4 * (k >> 1)) * 2048));
        };
        auto third = [&]() {
#pragma unroll
          for (int g = 0; g < 4; ++g) { xq[g] += 65536; asm volatile("" : "+v"(xq[g])); }
        };
        if constexpr (A) {
          g1_chain<8>(acc, W, fr, xq, 0, stw, [&](int k) {
            if (k >= 10) dmak(k - 2, rs, w2s + kq * 14336, L_R3 + kq * 14336, lane16);
          });
          BAR();
          STAMP(2 + 3 * layer, 0);
          epi0();
          g1_pre(fr, xq, 32768);
          BAR();
          STAMP(2 + 3 * layer, 1);
          g1_chain<0>(acc, W, fr, xq, 32768, 0, [&](int k) {
            if (k < 2) ldst(k);
            else if (k < 10) dmak(k - 2, rs, w2s + 57344 + kq * 8192, kq * 8192, lane16);
            else ldWr(k - 10);
          });
          BAR();
          STAMP(2 + 3 * layer, 2);
          asm volatile("" : "+v"(gb), "+v"(gc));
          recheck(Tb, 1, 32768, nop);
          stat_wait();
          st16(rs, hsb, g1_epi<false>(acc, __uint_as_float(gb[0]), __uint_as_float(gb[2]), csr, nop));
          third();
          g1_pre(fr, xq, 0);
          BAR();
          STAMP(2 + 3 * layer, 3);
          asm volatile("" : "=v"(fh[0]), "=v"(fh[1]), "=v"(fh[2]), "=v"(fh[3]), "=v"(fh[4]), "=v"(fh[5]), "=v"(fh[6]), "=v"(fh[7]),
                            "=v"(fh[8]), "=v"(fh[9]), "=v"(fh[10]));
          auto side = [&](int k) {
            if (k >= 5) fh[k - 5] = ld16(rs, lane16, hof(k < 14 ? k - 5 : k + 4));
          };
          if (ntile != 3) {
            delay(T2_DELAY);
#pragma unroll
            for (int k = 0; k < 16; ++k) side(k);
          } else {
            g1_chain<0>(acc, W, fr, xq, 0, 0, side);
          }
          BAR();
          STAMP(2 + 3 * layer, 4);
          if (ntile == 3) {
            recheck(Tc, 2, 0, [&]() {
#pragma unroll
              for (int k = 0; k < 16; ++k) side(k);
            });
            st16(rs, hsc, g1_epi<false>(acc, __uint_as_float(gc[0]), __uint_as_float(gc[2]), csr, nop));
          } else {
            delay(T2_DELAY);
          }
          hcheck(0, 9);
          fh[11] = ld16(rs, lane16, hof(20));
          fh[12] = ld16(rs, lane16, hof(21));
          STAMP(2 + 3 * layer, 5);
          BAR();
        } else {
          if (!xfull<2>(jv)) xretry<2>(rs, jv, ja, js, 4096);
#pragma unroll
          for (int k = 0; k < 8; ++k) dmak(k, rs, xsb, 32768 + kq * 8192, lane16);
          if (job) {
            const u32x4 v0 = jv[0], v1 = jv[1];
            float r[8];
#pragma unroll
            for (int k = 0; k < 8; ++k) {
              float x = (k & 1) ? bfhi(v0[k >> 1]) : bflo(v0[k >> 1]), y = (k & 1) ? bfhi(v1[k >> 1]) : bflo(v1[k >> 1]);
              float s1 = x + y, s2 = x * x;
              asm volatile("" : "+v"(s1), "+v"(s2));
              s2 = fmaf(y, y, s2);
              r[k] = sw32(s1, s2);
              asm volatile("" : "+v"(r[k]));
            }
#pragma unroll
            for (int k = 0; k < 4; ++k) {
              const auto sw = __builtin_amdgcn_permlane16_swap(__float_as_uint(r[k]), __float_as_uint(r[4 + k]), false, false);
              float t = __uint_as_float(sw[0]) + __uint_as_float(sw[1]);
              t += dppf<0xB1>(t); t += dppf<0x4E>(t); t += dppf<0x141>(t); t += dppf<0x140>(t);
              r[k] = t;
            }
            const bool b0 = lane & 1, b1 = lane & 2;
            lds_st<float>(L_ST + 1024 + (lane >> 4) * 64 + (lane & 3) * 16 + kq * 4, b1 ? (b0 ? r[3] : r[2]) : (b0 ? r[1] : r[0]));
            WAITV(0);
            WAITL0();
            u32 cnt = 0;
            if (lane == 0) cnt = __hip_atomic_fetch_add((LDSP u32*)(size_t)(L_TH + 2628), 1u, __ATOMIC_RELAXED, __HIP_MEMORY_SCOPE_WORKGROUP);
            if ((__builtin_amdgcn_readfirstlane(cnt) & 3) == 3) {
              asm volatile("" ::: "memory");
              const f32x4 p = lds_ld<f32x4>(L_ST + 1024 + (lane & 15) * 16);
              float s = p[0] + p[1], q = p[2] + p[3];
              asm volatile("" : "+v"(s), "+v"(q));
              const float m = (s + q) * (1.f / SD);
              const float v = fmaxf(fmaf(-m, m, dppf<0x128>(m)), 0.f) + 1e-5f;
              st16(rs, lane < 8 ? g1_off + (Tj * 32 + (sl & 3) * 8 + lane) * 16 : OOB, (u32x4){__float_as_uint(-m), tag, __float_as_uint(v), tag});
            }
          }
          if (!job) WAITV(0);
          BAR();
          STAMP(2 + 3 * layer, 0);
          g1_chain<0>(acc, W, fr, xq, 0, 0, [&](int k) {
            if (k < 8) dmak(k, rs, xsc, 65536 + kq * 8192, lane16);
            else dmak(k - 8, rs, w2s + kq * 14336, L_R3 + kq * 14336, lane16);
          });
          BAR();
          STAMP(2 + 3 * layer, 1);
          epi0();
          g1_pre(fr, xq, 32768);
          WAITV(1);
          if (layer == 0 && b == 0) st4(rs, tid == 256 ? O_EP : OOB, 0, tag);
          BAR();
          STAMP(2 + 3 * layer, 2);
          g1_chain<0>(acc, W, fr, xq, 32768, 0, [&](int k) {
            if (k < 2) ldst(k);
            else if (k < 8) ldWr(k - 2);
          });
          BAR();
          STAMP(2 + 3 * layer, 3);
          asm volatile("" : "+v"(gb), "+v"(gc));
          recheck(Tb, 1, 32768, nop);
          stat_wait();
          st16(rs, hsb, g1_epi<false>(acc, __uint_as_float(gb[0]), __uint_as_float(gb[2]), csr, nop));
          third();
          g1_pre(fr, xq, 0);
          BAR();
          STAMP(2 + 3 * layer, 4);
          auto side = [&](int k) {
            if (k < 8) dmak(k, rs, w2s + 90112 + kq * 8192, 32768 + kq * 8192, lane16);
          };
          if (ntile != 3) {
#pragma unroll
            for (int k = 0; k < 16; ++k) side(k);
          } else {
            g1_chain<0>(acc, W, fr, xq, 0, 0, side);
            recheck(Tc, 2, 0, nop);
          }
          WAITV(0);
          STAMP(2 + 3 * layer, 5);
          BAR();
          if (B_PRIO) __builtin_amdgcn_s_setprio(1);
          if (ntile != 3) hsc = OOB;
          st16(rs, hsc, g1_epi<false>(acc, __uint_as_float(gc[0]), __uint_as_float(gc[2]), csr, [&]() {
            SCHED();
#pragma unroll
            for (int p = 0; p < B_MID; ++p) fh[p] = ld16(rs, lane16, hof(p));
            SCHED();
          }));
#pragma unroll
          for (int p = B_MID; p < 11; ++p) fh[p] = ld16(rs, lane16, hof(p < 9 ? p : p + 9));
        }
      }
      STAMP(2 + 3 * layer, 6);
      f32x16 acc[2];
#pragma unroll
      for (int c = 0; c < 2; ++c)
#pragma unroll
        for (int e = 0; e < 16; ++e) acc[c][e] = 0.f;
      u32 wb0 = kq * 2048 + rot16, wb1 = wb0 + 65536, wb3 = wb0 + L_R3;
      asm volatile("" : "+v"(wb0), "+v"(wb1), "+v"(wb3));
      auto ldW = [&](int p, int c) {
        return lds_ld<bf16x8>((p < 10 ? wb3 + (p - 3) * 8192 : p < 18 ? wb0 + (p - 10) * 8192 : wb1 + (p - 18) * 8192) + c * 1024);
      };
      u16 rsd[8];
      SCHED();
#pragma unroll
      for (int p = 0; p < 22; ++p) {
        if (HCK == 0 || A) { if (p % 3 == 0 && p < 21 && (!A || p >= 9)) hcheck(p, p == 18 ? 22 : p + 3); }
        else if (p == 0) { hcheck(0, 9); delay(ODD_DELAY); }
        else if (p == 9) hcheck(9, 22);
        if (p < 21 || kq < 2) {
          acc[0] = __builtin_amdgcn_mfma_f32_32x32x16_bf16(Wr[p % 3][0], __builtin_bit_cast(bf16x8, fh[slot(p)]), acc[0], 0, 0, 0);
          acc[1] = __builtin_amdgcn_mfma_f32_32x32x16_bf16(Wr[p % 3][1], __builtin_bit_cast(bf16x8, fh[slot(p)]), acc[1], 0, 0, 0);
        }
        SCHED();
        if (A && p < 8) dmak(p & 1, rs, w2s + 122880 + (p >> 1) * 8192 + kq * 2048, 65536 + (p >> 1) * 8192 + kq * 2048, lane16);
        if (p < 9) fh[p] = ld16(rs, lane16, hof(p + 9));
        if (!A && p == 8) {
          fh[11] = ld16(rs, lane16, hof(20));
          fh[12] = ld16(rs, lane16, hof(21));
        }
        if (p == 8) {
          STAMP(3 + 3 * layer, 2);
          if (!A && B_PRIO) __builtin_amdgcn_s_setprio(0);
        }
        if (p == 11) {
#pragma unroll
          for (int j = 0; j < 8; ++j) rsd[j] = ld2(rs, (8 * hh + j) * 64 + r32 * 2, xt_off + T2 * 32768 + (nt * 64 + kq * 16) * 64);
        }
        if (p == 15) { if constexpr (!A) BAR(); else WAITV(8); }
        if (p + 3 < 22) { Wr[p % 3][0] = ldW(p + 3, 0); Wr[p % 3][1] = ldW(p + 3, 1); }
        SCHED();
      }
      STAMP(3 + 3 * layer, 3);
      if constexpr (A) BAR();
      const u32 pw = L_R3 + kq * 2048 + tt * 24576 + lane16;
#pragma unroll
      for (int q = 1; q < 4; ++q) {
        lds_st<f32x4>(pw + (q - 1) * 8192, (f32x4){acc[0][4 * q], acc[0][4 * q + 1], acc[0][4 * q + 2], acc[0][4 * q + 3]});
        lds_st<f32x4>(pw + (q - 1) * 8192 + 1024, (f32x4){acc[1][4 * q], acc[1][4 * q + 1], acc[1][4 * q + 2], acc[1][4 * q + 3]});
      }
#pragma unroll
      for (int e = 0; e < 4; ++e) { e4[e] = acc[0][e] + b2f(rsd[2 * e]); o4[e] = acc[1][e] + b2f(rsd[2 * e + 1]); }
      asm volatile("" : "+v"(e4), "+v"(o4));
    };
    asm volatile("" : "=v"(e4), "=v"(o4));
    if (w0 < 4) ffn(Role<true>{}); else ffn(Role<false>{});
    PHASE_IDS();
    {
      BAR();
      STAMP(3 + 3 * layer, 4);
#pragma unroll
      for (int q = 1; q < 4; ++q) {
        const u32 a = L_R3 + ((kq - q) & 3) * 2048 + tt * 24576 + lane16 + (q - 1) * 8192;
        e4 += lds_ld<f32x4>(a);
        o4 += lds_ld<f32x4>(a + 1024);
      }
      u32x4 o = {pkbf(e4[0], o4[0]), pkbf(e4[1], o4[1]), pkbf(e4[2], o4[2]), pkbf(e4[3], o4[3])};
      int last = layer;
      asm volatile("" : "+s"(last));
      if (last == 1) {
        *(u32x4*)(out + (size_t)(T2 * 32 + r32) * SD + nt * 64 + kq * 16 + hh * 8) = o;
        continue;
      }
      float s1 = 0.f, s2 = 0.f;
#pragma unroll
      for (int k = 0; k < 4; ++k) { s1 = dot2(o[k], 0x3f803f80u, s1); s2 = dot2(o[k], o[k], s2); }
      lds_st<float>(L_ST + tt * 1024 + lane16 + kq * 4, sw32(s1, s2));
      BAR();
      if (kq == 0) {
        const f32x4 p = lds_ld<f32x4>(L_ST + tt * 1024 + lane16);
        const float x = ((p[0] + p[1]) + (p[2] + p[3])) * (hh ? 0.0625f : 1.f), hi = floorf(x);
        const bool ok = fabsf(x) < 134217728.f;
        const u32 lo = ok ? ((u32)((x - hi) * 4294967296.f) & ~255u) | 1u : 0x11u;
        const int t = T2 * 32 + r32;
        __hip_atomic_fetch_add((GLBP u64*)(ws + O_S2 + hh * 16384 + ((t >> 1) & 1) * 8192 + (t >> 2) * 16 + (t & 1) * 8),
                               ((u64)(u32)(ok ? (int)hi : 0) << 32) | lo, __ATOMIC_RELAXED, __HIP_MEMORY_SCOPE_AGENT);
      }
      {
        const bool b0 = lane & 1, b1 = lane & 2;
        const u32 s0 = dppu<0xB1>(b0 ? o[0] : o[1]), s1_ = dppu<0xB1>(b0 ? o[2] : o[3]);
        const u32 p0 = b0 ? s0 : o[0], p1 = b0 ? o[1] : s0, p2 = b0 ? s1_ : o[2], p3 = b0 ? o[3] : s1_;
        const u32 t0 = dppu<0x4E>(b1 ? p0 : p2), t1 = dppu<0x4E>(b1 ? p1 : p3);
        o = (u32x4){b1 ? t0 : p0, b1 ? t1 : p1, b1 ? p2 : t0, b1 ? p3 : t1};
      }
      st16(rs, O_XP + (nt * 32 + kq * 8 + hh * 4 + (lane & 3)) * 8192 + (T2 * 32 + (r32 & ~3)) * 4, notempty(o));
    }
    if (S3_GATE && w == 4) st4(rs, lane == 0 ? O_F3 + b * 128 : OOB, 0, tag);
    STAMP(3, 5);
    {
      u32 kix[9];
      conv_prep(tid, w, kix);
      expand(tid, ch, kix);
    }
    delay(S3_DELAY);
    if (S3_GATE) {
      if (w < 4) poll(rs, O_F3 + tid * 128, 0, tag);
      BAR();
    }
    STAMP(3, 6);
    {
      u32x4 sv[4], xr;
      xload<4>(rs, sv, tid * 16, O_S2, 8192);
      xr = ld16(rs, tid * 16, O_XP + n * 8192);
      auto s3_ok = [&]() {
        u32 all = 8;
#pragma unroll
        for (int k = 0; k < 4; ++k) all &= sv[k][0] & sv[k][2];
        return none(all == 0 || umax4(0, xr) == EMPTY);
      };
      if (!s3_ok()) {
#pragma unroll 1
        for (int s = 0; s < SPIN_MAX; ++s) {
          asm volatile("" ::: "memory");
          u32x4 t[5];
#pragma unroll
          for (int k = 0; k < 4; ++k) t[k] = ld16(rs, (sv[k][0] & sv[k][2] & 8) ? OOB : tid * 16, O_S2 + k * 8192);
          t[4] = ld16(rs, umax4(0, xr) == EMPTY ? tid * 16 : OOB, O_XP + n * 8192);
#pragma unroll
          for (int k = 0; k < 4; ++k) sv[k] = (sv[k][0] & sv[k][2] & 8) ? sv[k] : t[k];
          xr = umax4(0, xr) == EMPTY ? t[4] : xr;
          if (s3_ok()) break;
          __builtin_amdgcn_s_sleep(4);
        }
      }
      u32 fl = 0;
#pragma unroll
      for (int k = 0; k < 4; ++k) fl |= sv[k][0] | sv[k][2];
      STAMP(3, 7);
      float s1[4], s2[4];
#pragma unroll
      for (int i = 0; i < 4; ++i) {
        s1[i] = fmaf((float)sv[i >> 1][2 * (i & 1)], 2.3283064365386963e-10f, (float)(int)sv[i >> 1][2 * (i & 1) + 1]);
        s2[i] = fmaf((float)sv[2 + (i >> 1)][2 * (i & 1)], 2.3283064365386963e-10f, (float)(int)sv[2 + (i >> 1)][2 * (i & 1) + 1]) * 16.f;
      }
      if (__builtin_expect(!none((fl & 0xf0) != 0), 0)) {
#pragma unroll
        for (int i = 0; i < 4; ++i) s1[i] = s2[i] = 0.f;
#pragma unroll 1
        for (int p = 0; p < 256; ++p) {
          u32x4 v;
#pragma unroll 1
          for (int s = 0; s < SPIN_MAX; ++s) {
            asm volatile("" ::: "memory");
            v = ld16(rs, tid * 16, O_XP + p * 8192);
            if (none(umax4(0, v) == EMPTY)) break;
          }
#pragma unroll
          for (int i = 0; i < 4; ++i) { s1[i] = dot2(v[i], 0x3f803f80u, s1[i]); s2[i] = dot2(v[i], v[i], s2[i]); }
        }
      }
      float mean[4], rstd[4];
#pragma unroll
      for (int i = 0; i < 4; ++i) {
        mean[i] = s1[i] * (1.f / SD);
        rstd[i] = __builtin_amdgcn_rsqf(fmaxf(fmaf(s2[i], 1.f / SD, -mean[i] * mean[i]), 0.f) + 1e-5f);
      }
      normalize(tid, xr, mean, rstd);
    }
    conv(Mode<1>{});
  }
  asm volatile("KCODE_END:");
}

extern "C" void kernel_launch(void* const* d_in, const int* in_sizes, int n_in, void* d_out, int out_size,
                              void* d_ws, size_t ws_size, hipStream_t stream) {
  k_all<<<256, 512, 0, stream>>>((const u16*)d_in[0], (const u16*)d_in[1], (const u16*)d_in[2], (const u16*)d_in[3],
                                 (const u16*)d_in[4], (const u16*)d_in[5], (const u16*)d_in[6], (const u16*)d_in[8],
                                 (const u16*)d_in[9], (const u16*)d_in[10], (char*)d_ws, (u16*)d_out);
}
```

```cpp
#define TSTEP (1u<<19)
#include <hip/hip_runtime.h>
#include <stdint.h>

typedef unsigned short u16;
typedef uint32_t u32;
typedef unsigned long long u64;
typedef __attribute__((ext_vector_type(2))) float f32x2;
typedef __attribute__((ext_vector_type(4))) float f32x4;
typedef __attribute__((ext_vector_type(16))) float f32x16;
typedef __attribute__((ext_vector_type(2))) _Float16 f16x2;
typedef __attribute__((ext_vector_type(8))) _Float16 f16x8;
typedef __attribute__((ext_vector_type(2))) __bf16 bf16x2;
typedef __attribute__((ext_vector_type(8))) __bf16 bf16x8;
typedef __attribute__((ext_vector_type(4))) short s16x4;
typedef __attribute__((ext_vector_type(2))) u32 u32x2;
typedef __attribute__((ext_vector_type(4))) u32 u32x4;
typedef __amdgpu_buffer_rsrc_t rsrc_t;
#define LDSP __attribute__((address_space(3)))
#define GLBP __attribute__((address_space(1)))
#define DEVI __device__ __forceinline__

constexpr int SL = 2048, SD = 512, NM = 128, DF = 1376, FT = 86;
constexpr int KCOPY = 2080;
constexpr int KCH_B = 2 * KCOPY * 2;
constexpr u32 O_EP = 0;
constexpr u32 O_F3 = 4096;
constexpr u32 O_G0 = O_F3 + 32768;
constexpr u32 O_G1 = O_G0 + 32768;
constexpr u32 O_S2 = O_G1 + 65536;
constexpr u32 O_W2F = O_S2 + 32768;
constexpr u32 H_SZ = 64 * FT * 1024;
constexpr u32 O_XT = O_W2F + 8 * FT * 2048 + 4096;
constexpr u32 O_H = O_XT + (2u << 20);
constexpr u32 O_XP = O_H + H_SZ;
constexpr u32 LAY_SZ = (4u << 20) + H_SZ;
constexpr u32 O_XG = O_H + LAY_SZ + (6u << 20);
constexpr u32 WS_END = O_XG + (4u << 20);
constexpr int L_Y = 0;
constexpr int TBP = 320, L_TB = 0, TB_W = 26 * TBP;
constexpr int L_K = 65536;
constexpr int L_U = L_K + 2 * KCH_B, U_CH = (240 + 2048) * 2;
constexpr int L_F = 91392;
constexpr int L_R3 = 98304;
constexpr int L_ST = 156928;
constexpr int L_CS = L_ST + 3072;
constexpr int L_TH = L_CS + 1024;
constexpr int L_RS = L_TH + 2640;
constexpr int L_SZ = 163840;
constexpr int SPIN_MAX = 1 << 16;
#define X_DELAY 2
#define CHAIN_PRIO 0
#define RAW0 8
#define RAW1 12
#define RAW2 16
#define S3_GATE 0
#define B_PRIO 1
#define B_AHEAD 4
#define T2_DELAY 6
#define B_MID 0
#define X_ROUND 2
#define GRAN_AT 1
#define HCK 0
#define ODD_DELAY 0
#define ST_A 4
#define S3_DELAY 8

DEVI float bflo(u32 v) { return __uint_as_float(v << 16); }
DEVI float bfhi(u32 v) { return __uint_as_float(v & 0xffff0000u); }
DEVI float b2f(u16 v) { return __uint_as_float(((u32)v) << 16); }
DEVI u32 pkbf(float a, float b) { bf16x2 r = __builtin_convertvector((f32x2){a, b}, bf16x2); return __builtin_bit_cast(u32, r); }
DEVI u32 pkf16(float a, float b) { f16x2 r = __builtin_convertvector((f32x2){a, b}, f16x2); return __builtin_bit_cast(u32, r); }
DEVI float rbf(float a) { return bflo(pkbf(a, 0.f)); }
template <int C> DEVI float dppf(float v) { return __uint_as_float(__builtin_amdgcn_update_dpp(0u, __float_as_uint(v), C, 0xf, 0xf, true)); }
template <int C> DEVI u32 dppu(u32 v) { return __builtin_amdgcn_update_dpp(0u, v, C, 0xf, 0xf, true); }
DEVI float sw16(float v) { auto r = __builtin_amdgcn_permlane16_swap(__float_as_uint(v), __float_as_uint(v), false, false); return __uint_as_float(r[0]) + __uint_as_float(r[1]); }
DEVI float sw32(float a, float b) { auto r = __builtin_amdgcn_permlane32_swap(__float_as_uint(a), __float_as_uint(b), false, false); return __uint_as_float(r[0]) + __uint_as_float(r[1]); }
DEVI float wsum(float v) { v += dppf<0xB1>(v); v += dppf<0x4E>(v); v += dppf<0x141>(v); v += dppf<0x140>(v); v = sw16(v); return sw32(v, v); }
DEVI float wmax(float v) {
  v = fmaxf(v, dppf<0xB1>(v)); v = fmaxf(v, dppf<0x4E>(v)); v = fmaxf(v, dppf<0x141>(v)); v = fmaxf(v, dppf<0x140>(v));
  auto r = __builtin_amdgcn_permlane16_swap(__float_as_uint(v), __float_as_uint(v), false, false);
  v = fmaxf(__uint_as_float(r[0]), __uint_as_float(r[1]));
  r = __builtin_amdgcn_permlane32_swap(__float_as_uint(v), __float_as_uint(v), false, false);
  return fmaxf(__uint_as_float(r[0]), __uint_as_float(r[1]));
}
DEVI float dot2(u32 a, u32 b, float c) { return __builtin_amdgcn_fdot2_f32_bf16(__builtin_bit_cast(bf16x2, a), __builtin_bit_cast(bf16x2, b), c, false); }

#define WAITV(n) asm volatile("s_waitcnt vmcnt(" #n ")" ::: "memory")
#define WAITL0() asm volatile("s_waitcnt lgkmcnt(0)" ::: "memory")
#define SCHED() __builtin_amdgcn_sched_barrier(0)
#define BAR() do { asm volatile("s_waitcnt lgkmcnt(0)\n\ts_barrier" ::: "memory"); } while (0)

#define STAMP(k, i)
#define RETRY(p)

DEVI u32x4 ld16(rsrc_t rs, u32 voff, u32 soff) { return __builtin_amdgcn_raw_buffer_load_b128(rs, voff, soff, 16); }
DEVI u32 ld4(rsrc_t rs, u32 voff, u32 soff) { return __builtin_amdgcn_raw_buffer_load_b32(rs, voff, soff, 16); }
DEVI u16 ld2(rsrc_t rs, u32 voff, u32 soff) { return __builtin_amdgcn_raw_buffer_load_b16(rs, voff, soff, 16); }
DEVI void st16(rsrc_t rs, u32 off, u32x4 v) { __builtin_amdgcn_raw_buffer_store_b128(v, rs, off, 0, 16); }
DEVI void st4(rsrc_t rs, u32 voff, u32 soff, u32 v) { __builtin_amdgcn_raw_buffer_store_b32(v, rs, voff, soff, 16); }
template <int K> DEVI void dma1(rsrc_t rs, u32 goff, u32 lds, u32 lane16) {
  const u32 g = goff + (K & ~3) * 1024;
  if constexpr ((K & 3) == 0)
    asm volatile("s_mov_b32 m0, %0\n\ts_nop 0\n\tbuffer_load_dwordx4 %1, %2, %3 offen sc1 lds"
                 :: "s"(lds + K * 1024), "v"(lane16), "s"(rs), "s"(g) : "memory");
  else
    asm volatile("buffer_load_dwordx4 %0, %1, %2 offen offset:%3 sc1 lds" :: "v"(lane16), "s"(rs), "s"(g), "i"((K & 3) * 1024) : "memory");
}
DEVI void dmak(int k, rsrc_t rs, u32 goff, u32 lds, u32 lane16) {
  switch (k) {
#define DK(n) case n: dma1<n>(rs, goff, lds, lane16); break;
    DK(0) DK(1) DK(2) DK(3) DK(4) DK(5) DK(6) DK(7) DK(8) DK(9) DK(10) DK(11) DK(12) DK(13)
#undef DK
  }
}
DEVI bool none(bool p) { return __builtin_amdgcn_ballot_w64(p) == 0; }
constexpr u32 OOB = 0x80000000u;
DEVI void poll(rsrc_t rs, u32 f, u32 voff, u32 soff, u32 tag) {
#pragma unroll 1
  for (int s = 0; s < SPIN_MAX; ++s) {
    if (none(f != tag)) break;
    asm volatile("" ::: "memory");
    f = ld4(rs, voff, soff);
  }
  asm volatile("" ::: "memory");
}
DEVI void poll(rsrc_t rs, u32 voff, u32 soff, u32 tag) {
  asm volatile("" ::: "memory");
  poll(rs, ld4(rs, voff, soff), voff, soff, tag);
}
constexpr u32 EMPTY = 0xffffffffu;
DEVI u32 umax4(u32 m, const u32x4& v) { return max(max(max(max(m, v[0]), v[1]), v[2]), v[3]); }
DEVI u32x4 notempty(u32x4 v) { return (u32x4){min(v[0], EMPTY - 1), min(v[1], EMPTY - 1), min(v[2], EMPTY - 1), min(v[3], EMPTY - 1)}; }
template <int N> DEVI void xload(rsrc_t rs, u32x4 (&v)[N], u32 voff, u32 soff, u32 stride) {
#pragma unroll
  for (int i = 0; i < N; ++i) v[i] = ld16(rs, voff, soff + i * stride);
}
template <int N> DEVI bool xfull(const u32x4 (&v)[N]) {
  u32 m = 0;
#pragma unroll
  for (int i = 0; i < N; ++i) m = umax4(m, v[i]);
  return none(m == EMPTY);
}
template <int N> DEVI void xretry(rsrc_t rs, u32x4 (&v)[N], u32 voff, u32 soff, u32 stride) {
#pragma unroll 1
  for (int s = 0; s < SPIN_MAX; ++s) {
    asm volatile("" ::: "memory");
    u32x4 t[N];
#pragma unroll
    for (int i = 0; i < N; ++i) t[i] = ld16(rs, umax4(0, v[i]) == EMPTY ? voff : OOB, soff + i * stride);
#pragma unroll
    for (int i = 0; i < N; ++i) v[i] = umax4(0, v[i]) == EMPTY ? t[i] : v[i];
    if (xfull<N>(v)) break;
    __builtin_amdgcn_s_sleep(4);
  }
}
template <int N> DEVI void xpark(rsrc_t rs, u32x4 (&v)[N], u32 voff, u32 soff, u32 stride, u32 lds) {
  if (!xfull<N>(v)) xretry<N>(rs, v, voff, soff, stride);
#pragma unroll
  for (int i = 0; i < N; ++i) *(LDSP u32x4*)(size_t)(lds + i * stride) = v[i];
}
DEVI void delay(int n) { for (int i = 0; i < n; ++i) __builtin_amdgcn_s_sleep(2); }

DEVI f32x2 cexp(float are, float aim, float p) {
  const float e = __builtin_amdgcn_exp2f(are * p), ph = __builtin_amdgcn_fractf(aim * p);
  return (f32x2){__builtin_amdgcn_cosf(ph), __builtin_amdgcn_sinf(ph)} * (f32x2){e, e};
}
DEVI f32x2 cmul(f32x2 a, f32x2 b) {
  f32x2 r;
  asm("v_pk_mul_f32 %0, %1, %2 op_sel:[0,0] op_sel_hi:[0,1]\n\t"
      "v_pk_fma_f32 %0, %1, %2, %0 op_sel:[1,1,0] op_sel_hi:[1,0,1] neg_lo:[1,0,0]" : "=&v"(r) : "v"(a), "v"(b));
  return r;
}
DEVI f16x8 cmul4(f32x4 a0, f32x4 a1, f32x4 b0, f32x4 b1) {
  f32x2 r0, r1, r2, r3;
  asm("v_pk_mul_f32 %0, %4, %8 op_sel:[0,0] op_sel_hi:[0,1]\n\tv_pk_mul_f32 %1, %5, %9 op_sel:[0,0] op_sel_hi:[0,1]\n\t"
      "v_pk_mul_f32 %2, %6, %10 op_sel:[0,0] op_sel_hi:[0,1]\n\tv_pk_mul_f32 %3, %7, %11 op_sel:[0,0] op_sel_hi:[0,1]\n\t"
      "v_pk_fma_f32 %0, %4, %8, %0 op_sel:[1,1,0] op_sel_hi:[1,0,1] neg_lo:[1,0,0]\n\t"
      "v_pk_fma_f32 %1, %5, %9, %1 op_sel:[1,1,0] op_sel_hi:[1,0,1] neg_lo:[1,0,0]\n\t"
      "v_pk_fma_f32 %2, %6, %10, %2 op_sel:[1,1,0] op_sel_hi:[1,0,1] neg_lo:[1,0,0]\n\t"
      "v_pk_fma_f32 %3, %7, %11, %3 op_sel:[1,1,0] op_sel_hi:[1,0,1] neg_lo:[1,0,0]"
      : "=&v"(r0), "=&v"(r1), "=&v"(r2), "=&v"(r3)
      : "v"(a0.xy), "v"(a0.zw), "v"(a1.xy), "v"(a1.zw), "v"(b0.xy), "v"(b0.zw), "v"(b1.xy), "v"(b1.zw));
  return __builtin_bit_cast(f16x8, (u32x4){pkf16(r0[0], r0[1]), pkf16(r1[0], r1[1]), pkf16(r2[0], r2[1]), pkf16(r3[0], r3[1])});
}
DEVI s16x4 trread(u32 a) { return __builtin_amdgcn_ds_read_tr16_b64_v4i16((LDSP s16x4*)(size_t)a); }
DEVI bf16x8 trfrag(u32 a) {
  const s16x4 lo = trread(a), hi = trread(a + 256);
  return __builtin_bit_cast(bf16x8, __builtin_shufflevector(lo, hi, 0, 1, 2, 3, 4, 5, 6, 7));
}
template <class T> DEVI T lds_ld(u32 a) { return *(const LDSP T*)(size_t)a; }
template <class T> DEVI void lds_st(u32 a, T v) { *(LDSP T*)(size_t)a = v; }

constexpr int PD = 4;
DEVI void g1_pre(bf16x8 (&fr)[PD], const u32 (&xq)[4], int toff) {
#pragma unroll
  for (int i = 0; i < PD; ++i) fr[i] = trfrag(xq[0] + toff + i * 1024);
}
template <int ST, class F>
DEVI float g1_chain(f32x16& acc, const bf16x8 (&W)[32], bf16x8 (&fr)[PD], const u32 (&xq)[4], int toff, u32 stdst, F side) {
#pragma unroll
  for (int e = 0; e < 16; ++e) acc[e] = 0.f;
  f32x4 q1 = {0.f, 0.f, 0.f, 0.f}, q2 = {0.f, 0.f, 0.f, 0.f};
  u32 one2 = 0x3f803f80u;
  if (ST) asm volatile("" : "+v"(one2));
  const s16x4 ones = __builtin_bit_cast(s16x4, (u32x2){one2, one2});
  auto fin = [&]() {
    const bool b0 = stdst & 16, b1 = stdst & 32;
    return sw32(q1[0], b1 ? (b0 ? q2[3] : q2[2]) : (b0 ? q2[1] : q2[0]));
  };
  if (CHAIN_PRIO) __builtin_amdgcn_s_setprio(1);
  SCHED();
#pragma unroll
  for (int i = 0; i < 32; ++i) {
    if (i < ST) {
      const u32x4 v = __builtin_bit_cast(u32x4, fr[i % PD]);
#pragma unroll
      for (int j = 0; j < 2; ++j) {
        const s16x4 x = __builtin_bit_cast(s16x4, (u32x2){v[2 * j], v[2 * j + 1]});
        q2 = __builtin_amdgcn_mfma_f32_4x4x4bf16_1k(x, x, q2, 0, 0, 0);
        q1 = __builtin_amdgcn_mfma_f32_4x4x4bf16_1k(ones, x, q1, 0, 0, 0);
      }
      SCHED();
    }
    acc = __builtin_amdgcn_mfma_f32_32x32x16_bf16(W[i], fr[i % PD], acc, 0, 0, 0);
    SCHED();
    if (ST > 0 && ST < 32 && i == ST + 1) lds_st<float>(stdst, fin());
    if (i + PD < 32) fr[i % PD] = trfrag(xq[(i + PD) >> 3] + toff + ((i + PD) & 7) * 1024);
    if (!(i & 1)) side(i >> 1);
    SCHED();
  }
  if (CHAIN_PRIO) __builtin_amdgcn_s_setprio(0);
  return ST == 32 ? fin() : 0.f;
}
template <bool V> struct Role { static constexpr bool A = V; };
template <int M> struct Mode { static constexpr int V = M; };
template <bool PK, class F> DEVI u32x4 g1_epi(const f32x16& acc, float nm, float v, u32 cssrc, F mid) {
  f32x4 cs[4];
#pragma unroll
  for (int q = 0; q < 4; ++q) cs[q] = lds_ld<f32x4>(cssrc + q * 16);
  const float rs = __builtin_amdgcn_rsqf(v);
  const float k0 = rs * -2.302208198f, k1 = rs * rs * rs * -0.102943239f;
  if constexpr (PK) {
    u32x4 o;
    f32x2 g[4], e[4];
#pragma unroll
    for (int p = 0; p < 4; ++p) {
      g[p] = __builtin_elementwise_fma((f32x2){cs[p >> 1][2 * (p & 1)], cs[p >> 1][2 * (p & 1) + 1]}, (f32x2){nm, nm}, (f32x2){acc[2 * p], acc[2 * p + 1]});
      const f32x2 t = g[p] * __builtin_elementwise_fma(g[p] * g[p], (f32x2){k1, k1}, (f32x2){k0, k0});
      e[p] = (f32x2){__builtin_amdgcn_exp2f(t[0]), __builtin_amdgcn_exp2f(t[1])};
    }
#pragma unroll
    for (int p = 0; p < 4; ++p) {
      const f32x2 d = __builtin_elementwise_fma(e[p], (f32x2){v, v}, (f32x2){v, v});
      const f32x2 uu = __builtin_elementwise_fma((f32x2){cs[2 + (p >> 1)][2 * (p & 1)], cs[2 + (p >> 1)][2 * (p & 1) + 1]}, (f32x2){nm, nm},
                                                 (f32x2){acc[8 + 2 * p], acc[9 + 2 * p]});
      const f32x2 r = g[p] * uu * (f32x2){__builtin_amdgcn_rcpf(d[0]), __builtin_amdgcn_rcpf(d[1])};
      o[p] = pkbf(r[0], r[1]);
    }
    return notempty(o);
  }
  u32x4 o;
#pragma unroll
  for (int q = 0; q < 2; ++q) {
    float g0 = cs[q][0], g1 = cs[q][1], g2 = cs[q][2], g3 = cs[q][3];
    float u0 = cs[2 + q][0], u1 = cs[2 + q][1], u2 = cs[2 + q][2], u3 = cs[2 + q][3];
    float t0, t1, t2, t3;
    asm("v_fma_f32 %0, %20, %0, %12\n\tv_fma_f32 %1, %20, %1, %13\n\tv_fma_f32 %2, %20, %2, %14\n\tv_fma_f32 %3, %20, %3, %15\n\t"
        "v_mul_f32 %8, %0, %0\n\tv_mul_f32 %9, %1, %1\n\tv_mul_f32 %10, %2, %2\n\tv_mul_f32 %11, %3, %3\n\t"
        "v_fma_f32 %8, %8, %22, %21\n\tv_fma_f32 %9, %9, %22, %21\n\tv_fma_f32 %10, %10, %22, %21\n\tv_fma_f32 %11, %11, %22, %21\n\t"
        "v_mul_f32 %8, %0, %8\n\tv_mul_f32 %9, %1, %9\n\tv_mul_f32 %10, %2, %10\n\tv_mul_f32 %11, %3, %11\n\t"
        "v_exp_f32 %8, %8\n\tv_exp_f32 %9, %9\n\tv_exp_f32 %10, %10\n\tv_exp_f32 %11, %11\n\t"
        "v_fma_f32 %4, %20, %4, %16\n\tv_fma_f32 %5, %20, %5, %17\n\tv_fma_f32 %6, %20, %6, %18\n\tv_fma_f32 %7, %20, %7, %19\n\t"
        "v_fma_f32 %8, %8, %23, %23\n\tv_fma_f32 %9, %9, %23, %23\n\tv_fma_f32 %10, %10, %23, %23\n\tv_fma_f32 %11, %11, %23, %23\n\t"
        "v_rcp_f32 %8, %8\n\tv_rcp_f32 %9, %9\n\tv_rcp_f32 %10, %10\n\tv_rcp_f32 %11, %11\n\t"
        "v_mul_f32 %0, %0, %4\n\tv_mul_f32 %1, %1, %5\n\tv_mul_f32 %2, %2, %6\n\tv_mul_f32 %3, %3, %7\n\t"
        "v_mul_f32 %0, %0, %8\n\tv_mul_f32 %1, %1, %9\n\tv_mul_f32 %2, %2, %10\n\tv_mul_f32 %3, %3, %11"
        : "+v"(g0), "+v"(g1), "+v"(g2), "+v"(g3), "+v"(u0), "+v"(u1), "+v"(u2), "+v"(u3), "=&v"(t0), "=&v"(t1), "=&v"(t2), "=&v"(t3)
        : "v"(acc[4 * q]), "v"(acc[4 * q + 1]), "v"(acc[4 * q + 2]), "v"(acc[4 * q + 3]),
          "v"(acc[8 + 4 * q]), "v"(acc[9 + 4 * q]), "v"(acc[10 + 4 * q]), "v"(acc[11 + 4 * q]), "v"(nm), "v"(k0), "v"(k1), "v"(v));
    o[2 * q] = pkbf(g0, g1);
    o[2 * q + 1] = pkbf(g2, g3);
    if (q == 0) mid();
  }
  return notempty(o);
}
#define MFMA_DRAIN(a) asm volatile("s_nop 15\n\ts_nop 3" : "+v"(a))

__device__ const u32 k_map[256] = {
    0x2e003, 0x62e003, 0x432053, 0xa32053, 0x3370b3, 0x9370b3, 0x23a116, 0x83f112, 0x12e003, 0x72e003, 0x33063, 0x633063, 0x4370b3, 0xa370b3, 0x33b116, 0x93f112,
    0x22e003, 0x82e003, 0x133063, 0x733063, 0x2d0c6, 0x6330c6, 0x43c116, 0xa3f112, 0x32e003, 0x92e003, 0x233063, 0x833063, 0x12e0c6, 0x7340c6, 0x3c123, 0x63c123,
    0x42e003, 0xa2e003, 0x333063, 0x933063, 0x22f0c6, 0x8350c6, 0x13c123, 0x73c123, 0x52e003, 0x52f013, 0x433063, 0xa33063, 0x3300c6, 0x9360c6, 0x23c123, 0x83c123,
    0x2f013, 0x62f013, 0x533063, 0x527076, 0x4310c6, 0xa370c6, 0x33c123, 0x93c123, 0x12f013, 0x72f013, 0x22076, 0x628076, 0x5320c6, 0x5380d3, 0x43c123, 0xa3c123,
    0x22f013, 0x82f013, 0x123076, 0x729076, 0x380d3, 0x6380d3, 0x53c123, 0x53d133, 0x32f013, 0x92f013, 0x224076, 0x82a076, 0x1380d3, 0x7380d3, 0x3d133, 0x63d133,
    0x42f013, 0xa2f013, 0x325076, 0x92b076, 0x2380d3, 0x8380d3, 0x13d133, 0x73d133, 0x17026, 0x61d026, 0x426076, 0xa2c076, 0x3380d3, 0x9380d3, 0x23d133, 0x83d133,
    0x118026, 0x71e026, 0x34083, 0x634083, 0x4380d3, 0xa380d3, 0x33d133, 0x93d133, 0x219026, 0x81f026, 0x134083, 0x734083, 0x390e3, 0x6390e3, 0x43d133, 0xa3d133,
    0x31a026, 0x920026, 0x234083, 0x834083, 0x1390e3, 0x7390e3, 0x3e143, 0x63e143, 0x41b026, 0xa21026, 0x334083, 0x934083, 0x2390e3, 0x8390e3, 0x13e143, 0x73e143,
    0x51c026, 0x530033, 0x434083, 0xa34083, 0x3390e3, 0x9390e3, 0x23e143, 0x83e143, 0x30033, 0x630033, 0x534083, 0x535093, 0x4390e3, 0xa390e3, 0x33e143, 0x93e143,
    0x130033, 0x730033, 0x35093, 0x635093, 0x5390e3, 0x53a0f3, 0x43e143, 0xa3e143, 0x230033, 0x830033, 0x135093, 0x735093, 0x3a0f3, 0x63a0f3, 0x53e143, 0x53f153,
    0x330033, 0x930033, 0x235093, 0x835093, 0x13a0f3, 0x73a0f3, 0x3f153, 0x63f153, 0x430033, 0xa30033, 0x335093, 0x935093, 0x23a0f3, 0x83a0f3, 0x13f153, 0x73f153,
    0x31043, 0x631043, 0x435093, 0xa35093, 0x33a0f3, 0x93a0f3, 0x23f153, 0x83f153, 0x131043, 0x731043, 0x360a3, 0x6360a3, 0x43a0f3, 0xa3a0f3, 0x33f153, 0x93f153,
    0x231043, 0x831043, 0x1360a3, 0x7360a3, 0x3b103, 0x63b103, 0x43f153, 0xa3f153, 0x331043, 0x931043, 0x2360a3, 0x8360a3, 0x13b103, 0x73b103, 0x3f162, 0x63f162,
    0x431043, 0xa31043, 0x3360a3, 0x9360a3, 0x23b103, 0x83b103, 0x13f162, 0x73f162, 0x531043, 0x532053, 0x4360a3, 0xa360a3, 0x33b103, 0x93b103, 0x23f162, 0x83f162,
    0x32053, 0x632053, 0x5360a3, 0x5370b3, 0x43b103, 0xa3b103, 0x33f162, 0x93f162, 0x132053, 0x732053, 0x370b3, 0x6370b3, 0x53b103, 0x53d116, 0x43f162, 0xa3f162,
    0x232053, 0x832053, 0x1370b3, 0x7370b3, 0x38116, 0x63e116, 0x53f162, 0x53f160, 0x332053, 0x932053, 0x2370b3, 0x8370b3, 0x139116, 0x73f116, 0x3f160, 0x63f160,
};

__global__ __launch_bounds__(512, 2) void k_all(const u16* __restrict__ u, const u16* __restrict__ logA,
                                                const u16* __restrict__ Aim, const u16* __restrict__ Cre,
                                                const u16* __restrict__ Cim, const u16* __restrict__ logdt,
                                                const u16* __restrict__ dsk, const u16* __restrict__ wg,
                                                const u16* __restrict__ wu, const u16* __restrict__ wd,
                                                char* ws, u16* __restrict__ out) {
  __shared__ __attribute__((aligned(16))) char sm[L_SZ];
  const int tid0 = threadIdx.x, w0 = __builtin_amdgcn_readfirstlane(tid0 >> 6), b = blockIdx.x;
  const int n0 = (b & 7) * 32 + (b >> 3);
  asm volatile("" :: "v"((LDSP char*)sm) : "memory");
  STAMP(0, 0);
  u32 pf;
  {
    u64 pc;
    u32 len;
    asm volatile("s_getpc_b64 %0\nKCODE_BEG:\n\ts_mov_b32 %1, KCODE_END-KCODE_BEG" : "=s"(pc), "=s"(len));
    const u32 o = min((u32)(((b >> 3) + 32 * (tid0 & 15)) * 128), len);
    asm volatile("global_load_dword %0, %1, %2" : "=v"(pf) : "v"(o), "s"(pc) : "memory");
  }
  asm volatile("" :: "s"(u), "s"(logA), "s"(Aim), "s"(Cre), "s"(Cim), "s"(logdt), "s"(dsk), "s"(wg), "s"(wu), "s"(wd), "s"(ws), "s"(out));
  const rsrc_t rs = __builtin_amdgcn_make_buffer_rsrc(ws, 0, WS_END, 0x00020000);
  u32 map0;
#define PHASE_IDS()                                                                               \
  int tid = tid0, w = w0, n = n0;                                                                 \
  u32 tag = tag0, map = map0;                                                                     \
  asm volatile("" : "+v"(tid));                                                                   \
  asm volatile("" : "+s"(w), "+s"(n), "+s"(map), "+s"(tag));                                      \
  const int lane = tid & 63, r32 = lane & 31, hh = lane >> 5, ch = w >> 2;                        \
  const u32 lane16 = lane * 16;                                                                   \
  const int ntile = map & 3, Ta = (map >> 4) & 255, Tb = 23 + Ta, Tc = (map >> 12) & 255;         \
  const int ftw = (map >> 20) * 8 + w, ftc = ftw < FT ? ftw : FT - 1;                             \
  const int nt = n & 7, tt = w >> 2, kq = w & 3, T2 = (n >> 3) + 32 * tt;                         \
  (void)r32; (void)hh; (void)ch; (void)lane16; (void)ftc; (void)nt; (void)kq; (void)T2; (void)Tb; (void)Tc; (void)ntile

  u32 tag0, dsk2;
  float ks0, ks1;
  float base[2][4];
  u32 kb, ub;
  auto normalize = [&](int tid, const u32x4& xr, const float (&mean)[4], const float (&rstd)[4]) {
    const f32x2 d = {bflo(dsk2), bfhi(dsk2)};
    f32x2 un[4];
#pragma unroll
    for (int i = 0; i < 4; ++i) {
      const f32x2 x = {bflo(xr[i]), bfhi(xr[i])};
      un[i] = (x - mean[i]) * rstd[i];
      const f32x2 t = __builtin_elementwise_fma(un[i], d, x);
      base[0][i] = t[0]; base[1][i] = t[1];
    }
    lds_st<u32x2>(L_U + 480 + tid * 8, (u32x2){pkf16(un[0][0], un[1][0]), pkf16(un[2][0], un[3][0])});
    lds_st<u32x2>(L_U + U_CH + 480 + tid * 8, (u32x2){pkf16(un[0][1], un[1][1]), pkf16(un[2][1], un[3][1])});
  };
  auto conv_prep = [&](int tid, int w, u32 (&kix)[9]) {
    u32 z = 0;
    asm volatile("" : "+v"(z));
    if (tid < 76)
      lds_st<u32x4>(tid < 60 ? L_U + (tid >= 30 ? U_CH + (tid - 30) * 16 : tid * 16) : L_K + ((tid - 60) >> 2) * KCOPY * 2 + 4096 + (tid & 3) * 16,
                    (u32x4){z, z, z, z});
    const int i = tid & 255, ch = w >> 2;
    const int sh = min(__builtin_clz((u32)(255 - i) | 16) - 24, 3);
    const float sc = __uint_as_float((124 + sh) << 23);
    float l0 = (float)(2047 - 8 * i);
    asm volatile("" : "+v"(l0));
    const float mg = 8388608.f + (float)(384 - 128 * sh);
#pragma unroll
    for (int j = 0; j < 9; ++j) kix[j] = (__float_as_uint(fmaf(l0 - (float)j, sc, mg)) << 1) + (u32)(L_TH + ch * 1312 - 0x96000000u);
    asm volatile("" : "+v"(kix[8]));
    kix[8] -= i == 223 ? 2 : 0;
    kix[8] = i == 255 ? L_TH + 2624 : kix[8];
#pragma unroll
    for (int j = 0; j < 9; ++j) asm volatile("" : "+v"(kix[j]));
    const int r = tid & 15, h = (tid >> 4) & 3;
    kb = L_K + ch * KCH_B + 4 * (((2031 - r + 8 * h) >> 1) + ((r & 1) ? 0 : KCOPY / 2)) - 128 * (w & 3) - 64 * 9;
    ub = L_U + ch * U_CH + 2 * (224 + 16 * r + 8 * h) - 128 * (w & 3) - 64;
    asm volatile("" : "+v"(kb), "+v"(ub));
  };
  auto expand = [&](int tid, int ch, const u32 (&kix)[9]) {
    const int i = tid & 255;
    u32 kv[9];
#pragma unroll
    for (int j = 0; j < 9; ++j) kv[j] = lds_ld<u16>(kix[j]);
    lds_st<u32x4>(L_K + ch * KCH_B + i * 16,
                  (u32x4){kv[0] | (kv[1] << 16), kv[2] | (kv[3] << 16), kv[4] | (kv[5] << 16), kv[6] | (kv[7] << 16)});
    lds_st<u32x4>(L_K + ch * KCH_B + i * 16 + KCOPY * 2,
                  (u32x4){kv[1] | (kv[2] << 16), kv[3] | (kv[4] << 16), kv[5] | (kv[6] << 16), kv[7] | (kv[8] << 16)});
  };
  bf16x8 W[32];
  rsrc_t wrs;
  u32 wvo, ra[4];
  u32x4 R[16];
  auto ldR = [&](int i) {
    SCHED();
    R[i] = __builtin_amdgcn_raw_buffer_load_b128(wrs, wvo, (((i & 7) >> 1) * 8 + 4 + 2 * (i >> 3) + (i & 1)) * (16 * DF * 2), 0);
    SCHED();
  };
  auto w_put = [&](int r, int j) {
    asm volatile("s_mov_b32 m0, %0\n\ts_nop 0\n\tbuffer_load_dwordx4 %1, %2, %3 offen lds"
                 :: "s"(L_F + j * 8192 + w0 * 1024), "v"(wvo), "s"(wrs), "s"(((j >> 1) * 8 + 2 * r + (j & 1)) * (16 * DF * 2)) : "memory");
  };
  auto w_get = [&](int r) {
#pragma unroll
    for (int j = 0; j < 8; ++j) {
      const u32 a = ra[j >> 1] + (j & 1) * 8192;
      const s16x4 lo = trread(a), hi = trread(a + 1024);
      W[(j >> 1) * 8 + 2 * r + (j & 1)] = __builtin_bit_cast(bf16x8, __builtin_shufflevector(lo, hi, 0, 1, 2, 3, 4, 5, 6, 7));
    }
  };
  {
    const int tid = tid0, w = w0, n = n0, lane = tid & 63, r32 = lane & 31, hh = lane >> 5, ch = w >> 2, kq = w & 3;
    const u32 lane16 = lane * 16;
    const int pidx = n * 2 * NM + w * 32 + r32;
    const u16 pla = logA[pidx], pai = Aim[pidx], pcr = Cre[pidx], pci = Cim[pidx];
    const u32 car = *(const u32*)(Cre + (n * 2 + ch) * NM + lane * 2), cai = *(const u32*)(Cim + (n * 2 + ch) * NM + lane * 2);
    const u32x4 til = *(const u32x4*)(u + (size_t)((b >> 2) * 32 + r32) * SD + (b & 3) * 128 + (w * 2 + hh) * 8);
    const u32x4 row = *(const u32x4*)(u + (size_t)(4 * (8 * (b >> 2) + w) + (b & 3)) * SD + lane * 8);
    const u32 ep = ld4(rs, O_EP, 0);
    int vz = 0;
    asm volatile("" : "+v"(vz));
    const u16 ldt = logdt[2 * n + ch + vz];
    const u32 dskv = ((const u32*)dsk)[n + vz], mapv = k_map[b + vz];
    SCHED();
    __builtin_amdgcn_s_barrier();
    SCHED();
    st16(rs, O_XT + b * 8192 + tid * 16, (u32x4){EMPTY, EMPTY, EMPTY, EMPTY});
    asm volatile("" ::: "memory");
    const u32 pf2 = __builtin_amdgcn_raw_buffer_load_b32(
        __builtin_amdgcn_make_buffer_rsrc(w < 2 ? (void*)wg : w < 4 ? (void*)wu : w < 6 ? (void*)wd : w == 6 ? (void*)ws : (void*)out, 0,
                                          w < 6 ? SD * DF * 2 : w == 6 ? WS_END : SL * SD * 2, 0x00020000),
        w < 6 ? (lane < 43 ? lane * 64 : OOB) : lane * TSTEP, w < 6 ? (b * 2 + (w & 1)) * 2752 : 2048 + ((b >> 3) & 7) * 65536, 0);
    {
      const int pe = (n >> 6) * 32 + (n & 31), pq = pe / 11, pr = pe - pq * 11, up = (n >> 5) & 1;
      const int sl = up ? (pr < 5 ? 6 + pr : pr) : (pr < 6 ? pr : pr - 6);
      wrs = __builtin_amdgcn_make_buffer_rsrc((void*)(ch ? wu : wg), 0, SD * DF * 2, 0x00020000);
      const int q = lane >> 4;
      wvo = (kq * 4 + q) * (DF * 2) + sl * 256 + ((((lane & 15) >> 1) ^ (2 * q + ch)) * 32) + (lane & 1) * 16;
      const int mat = q & 1, rq = (lane & 15) >> 2;
#pragma unroll
      for (int g = 0; g < 4; ++g) {
        ra[g] = L_F + mat * 4096 + (8 * hh + rq) * 256 + ((w ^ (2 * rq + mat)) * 32) + (lane & 3) * 8 + ((g + kq) & 3) * 16384;
        asm volatile("" : "+v"(ra[g]));
      }
    }
    u32 kix[9];
    conv_prep(tid, w, kix);
    const bool odd = lane & 1;
    u32 xgo = O_XG + ((b & 3) * 64 + (w * 2 + hh) * 4 + (odd ? 2 : 0)) * 16384 + ((r32 >> 1) & 1) * 8192 + ((b >> 2) * 8 + (r32 >> 2)) * 16;
    const u32 tb0 = L_TB + w * TB_W + r32 * 8;
    u32 tb = tb0 + hh * 13 * TBP, tbe = tb0 + (hh ? 18 : 12) * TBP, tbf = tb0 + (hh ? 19 : 5) * TBP, tbg = tb0 + (hh ? 20 : 25) * TBP,
        tb1 = tb0 + (hh ? 17 : 0) * TBP;
    float pa = hh ? 4.f : 1.f, pb = hh ? 64.f : 256.f, pe = hh ? 16.f : 2044.f, sg = hh ? -1.f : 1.f, sc = hh ? 16384.f : 1.f;
    u32 aL = L_TB + w * TB_W + (lane >> 4) * 32 + (lane & 3) * TBP, aM = L_TB + w * TB_W + (lane >> 4) * 32 + ((lane & 15) >> 2) * TBP;
    u32 a2x = (lane & 15) == 4 ? aL + 4 * TBP : aL;
    asm volatile("" : "+v"(xgo), "+v"(tb), "+v"(tbe), "+v"(tbf), "+v"(tbg), "+v"(tb1), "+v"(pa), "+v"(pb), "+v"(pe), "+v"(sg), "+v"(sc));
    asm volatile("" : "+v"(aL), "+v"(aM), "+v"(a2x));
    lds_st<f32x2>(tb0 + 13 * TBP, (f32x2){16384.f, 0.f});
    if (tid == 0) lds_st<u32x2>(L_TH + 2624, (u32x2){0, 0});
    SCHED();
    const u32 tag = tag0 = __builtin_amdgcn_readfirstlane(ep) + 1u;
    STAMP(0, 1);
    {
      const u32 r0 = dppu<0xB1>(odd ? til[0] : til[2]), r1 = dppu<0xB1>(odd ? til[1] : til[3]);
      st16(rs, xgo, (u32x4){odd ? r0 : til[0], tag, odd ? til[2] : r0, tag});
      st16(rs, xgo + 16384, (u32x4){odd ? r1 : til[1], tag, odd ? til[3] : r1, tag});
      float s1 = 0.f, s2 = 0.f;
#pragma unroll
      for (int c = 0; c < 4; ++c) { s1 = dot2(row[c], 0x3f803f80u, s1); s2 = dot2(row[c], row[c], s2); }
      float s = sw32(s1, s2);
      s += dppf<0xB1>(s); s += dppf<0x4E>(s); s += dppf<0x141>(s); s += dppf<0x140>(s);
      s = sw16(s);
      if (r32 == 0) lds_st<float>(L_RS + w * 8 + hh * 4, s);
    }
    STAMP(0, 2);
    {
      const float dt = rbf(__expf(b2f(ldt)));
      const float ar = -rbf(__expf(b2f(pla))) * dt * 1.44269504088896f, ai = (b2f(pai) * dt) * 0.159154943091895f, aj = ai * sg;
      const int ce = __builtin_amdgcn_frexp_expf(wmax(fmaxf(fmaxf(fabsf(bflo(car)), fabsf(bfhi(car))), fmaxf(fabsf(bflo(cai)), fabsf(bfhi(cai))))));
      if (lane == 0) lds_st<float>(L_RS + 64 + ch * 4, ldexpf(dt, ce - 7));
      const f32x2 a = cexp(ar, ai, pa), bb = cexp(ar, aj, pb);
      const f32x2 e = cexp(ar, aj, pe), e1k = cexp(ar, ai, 1024.f);
      const f32x2 d = hh ? (f32x2){ldexpf(b2f(pcr), 14 - ce), ldexpf(-b2f(pci), 14 - ce)} : e1k;
      const f32x2 as = a * sc, a2 = cmul(as, a), b2 = cmul(bb, bb), b3 = cmul(b2, bb);
      const f32x2 f = cmul(hh ? e : bb, hh ? e : a2);
      lds_st<f32x2>(tb + 1 * TBP, as);
      lds_st<f32x2>(tb + 2 * TBP, a2);
      lds_st<f32x2>(tb + 3 * TBP, cmul(a2, a));
      lds_st<f32x2>(tb + 4 * TBP, bb);
      lds_st<f32x2>(tb + 6 * TBP, b2);
      lds_st<f32x2>(tb + 7 * TBP, b3);
      lds_st<f32x2>(tb + 8 * TBP, d);
      lds_st<f32x2>(tb + 9 * TBP, cmul(d, bb));
      lds_st<f32x2>(tb + 10 * TBP, cmul(d, b2));
      lds_st<f32x2>(tb + 11 * TBP, cmul(d, b3));
      asm volatile("" ::: "memory");
      lds_st<f32x2>(tbe, e);
      lds_st<f32x2>(tbf, f);
      lds_st<f32x2>(tbg, cmul(f, e));
      lds_st<f32x2>(tb1, (f32x2){1.f, 0.f});
    }
    asm volatile("" :: "v"(pf));
    dsk2 = __builtin_amdgcn_readfirstlane(dskv);
    map0 = __builtin_amdgcn_readfirstlane(mapv);
    asm volatile("" :: "v"(mapv));
    WAITV(3);
    BAR();
    if (w == 3) {
      const f32x2 v = lds_ld<f32x2>(L_RS + (lane & 7) * 8) * (1.f / SD);
      const float r = __builtin_amdgcn_rsqf(fmaxf(fmaf(-v[0], v[0], v[1]), 0.f) + 1e-5f);
      st16(rs, lane < 8 ? O_G0 + (b & 3) * 8192 + ((b >> 2) * 8 + lane) * 16 : OOB, (u32x4){__float_as_uint(v[0]), tag, __float_as_uint(r), tag});
      st16(rs, lane < 8 ? O_S2 + b * 128 + lane16 : OOB, (u32x4){0, 0, 0, 0});
    }
    STAMP(0, 3);
    u32x4 g[6];
    auto gran_loads = [&]() {
#pragma unroll
      for (int i = 0; i < 2; ++i) g[4 + i] = ld16(rs, tid * 16, O_XG + n * 16384 + i * 8192);
#pragma unroll
      for (int i = 0; i < 4; ++i) g[i] = ld16(rs, tid * 16, O_G0 + i * 8192);
    };
    {
      f32x4 a0 = {0, 0, 0, 0}, a1 = {0, 0, 0, 0}, a2 = {0, 0, 0, 0};
      f32x4 tv[2][12];
#pragma unroll
      for (int s = 0; s < 2; ++s) {
        auto L = [&](int i, u32 a) { tv[s][i] = lds_ld<f32x4>(a + s * 128); tv[s][i + 1] = lds_ld<f32x4>(a + s * 128 + 16); };
        L(0, aL + 17 * TBP); L(2, aM + 21 * TBP); L(4, aM + 13 * TBP); L(6, aL); L(8, aL + 4 * TBP); L(10, a2x + 8 * TBP);
      }
#pragma unroll
      for (int s = 0; s < 2; ++s) {
        const f32x4* t = tv[s];
        SCHED(); w_put(0, 4 * s + 0); SCHED();
        const f16x8 bb = cmul4(t[0], t[1], t[2], t[3]);
        SCHED(); w_put(0, 4 * s + 1); SCHED();
        a0 = __builtin_amdgcn_mfma_f32_16x16x32_f16(cmul4(t[6], t[7], t[4], t[5]), bb, a0, 0, 0, 0);
        SCHED(); w_put(0, 4 * s + 2); SCHED();
        a1 = __builtin_amdgcn_mfma_f32_16x16x32_f16(cmul4(t[8], t[9], t[4], t[5]), bb, a1, 0, 0, 0);
        SCHED(); w_put(0, 4 * s + 3); SCHED();
        a2 = __builtin_amdgcn_mfma_f32_16x16x32_f16(cmul4(t[10], t[11], t[4], t[5]), bb, a2, 0, 0, 0);
        if (s == GRAN_AT) { SCHED(); gran_loads(); SCHED(); }
      }
      const u32 T = L_TB + w * TB_W + lane16;
      lds_st<f32x4>(T, a0);
      lds_st<f32x4>(T + 1024, a1);
      lds_st<f32x4>(T + 2048, a2);
    }
    u32 wt[8], wo;
    {
      const int ft = b / 3, fi = ft >> 2, pos = fi < 18 ? (fi & 1) * 9 + (fi >> 1) : 9 + (fi & 1) * 2 + (fi >> 1);
      const rsrc_t wr = __builtin_amdgcn_make_buffer_rsrc((void*)wd, 0, b == ft * 3 ? SD * DF * 2 : 0, 0x00020000);
#pragma unroll
      for (int j = 0; j < 8; ++j)
        wt[j] = __builtin_amdgcn_raw_buffer_load_b32(wr, hh * (4 * SD * 2) + r32 * 4 + (j & 3) * (SD * 2), (ft * 16 + 8 * (j >> 2)) * (SD * 2) + w * 128, 0);
      wo = b == ft * 3 ? O_W2F + (w * FT + 4 * pos + (ft & 3)) * 2048 : OOB;
    }
    STAMP(0, 4);
    BAR();
    if (kq < 3) {
      const u32 T = L_TB + ch * 4 * TB_W + kq * 1024 + lane16;
      const f32x4 v = ((lds_ld<f32x4>(T) + lds_ld<f32x4>(T + TB_W)) + (lds_ld<f32x4>(T + 2 * TB_W) + lds_ld<f32x4>(T + 3 * TB_W))) * 9.5367431640625e-07f;
      const u32 p0 = pkf16(v[0], v[1]), p1 = pkf16(v[2], v[3]);
      const int j = lane & 15, h = lane >> 4;
      const u32 H = L_TH + ch * 1312;
      if (kq == 0) {
        lds_st<u32x2>(H + 2 * (16 * j + 4 * h), (u32x2){p0, p1});
      } else if (kq == 1) {
        lds_st<u32>(H + 2 * (256 + 8 * j + 2 * h), p0);
        lds_st<u16>(H + 2 * (384 + 4 * j + h), (u16)p1);
        lds_st<u16>(H + 2 * (448 + 4 * j + h), (u16)(p1 >> 16));
      } else if (!(h & 1)) {
        const u32 a = H + 2 * (512 + 2 * j + (h >> 1));
        lds_st<u16>(a, (u16)p0);
        lds_st<u16>(a + 64, (u16)(p0 >> 16));
        lds_st<u16>(a + 128, (u16)p1);
        lds_st<u16>(a + 192, (u16)(p1 >> 16));
      } else if (lane == 16) {
        lds_st<u16>(H + 2 * 640, (u16)p0);
      }
    }
    {
      const f32x2 k2 = lds_ld<f32x2>(L_RS + 64);
      ks0 = __uint_as_float(__builtin_amdgcn_readfirstlane(__float_as_uint(k2[0])));
      ks1 = __uint_as_float(__builtin_amdgcn_readfirstlane(__float_as_uint(k2[1])));
    }
    BAR();
    STAMP(0, 5);
    ldR(0); ldR(1);
    expand(tid, ch, kix);
    ldR(2); ldR(3);
    asm volatile("" : "+v"(g[0]), "+v"(g[1]), "+v"(g[2]), "+v"(g[3]), "+v"(g[4]), "+v"(g[5]) :: "memory");
    asm volatile("" :: "v"(wt[7]), "v"(pf2));
    if (wo != OOB) {
      u32x4 lo, hi;
#pragma unroll
      for (int j = 0; j < 4; ++j) {
        lo[j] = __builtin_amdgcn_perm(wt[2 * j + 1], wt[2 * j], 0x05040100u);
        hi[j] = __builtin_amdgcn_perm(wt[2 * j + 1], wt[2 * j], 0x07060302u);
      }
      st16(rs, wo + lane16, lo);
      st16(rs, wo + 1024 + lane16, hi);
    }
    STAMP(0, 6);
    auto gran_ok = [&]() {
      u32 bad = 0;
#pragma unroll
      for (int k = 0; k < 6; ++k) bad |= (g[k][1] ^ tag) | (g[k][3] ^ tag);
      return none(bad != 0);
    };
    if (!gran_ok()) {
#pragma unroll 1
      for (int s = 0; s < SPIN_MAX; ++s) {
        asm volatile("" ::: "memory");
        gran_loads();
        if (gran_ok()) break;
      }
    }
    ldR(4); ldR(5);
    {
      float mean[4], rstd[4];
#pragma unroll
      for (int k = 0; k < 4; ++k) { mean[k] = __uint_as_float(g[k][0]); rstd[k] = __uint_as_float(g[k][2]); }
      normalize(tid, (u32x4){g[4][0], g[4][2], g[5][0], g[5][2]}, mean, rstd);
    }
    ldR(6);
    ldR(7);
    STAMP(0, 7);
  }

  auto conv = [&](auto mode) {
    constexpr int layer = decltype(mode)::V;
    {
      BAR();
      if (layer == 0) {
        w_get(0);
        BAR();
      }
      STAMP(1 + 3 * layer, 0);
      PHASE_IDS();
      f32x4 acc[8];
#pragma unroll
      for (int g = 0; g < 8; ++g) acc[g] = (f32x4){0, 0, 0, 0};
      u32 kp[4];
#pragma unroll
      for (int p = 0; p < 4; ++p) {
        kp[p] = kb - 1024 * p;
        asm volatile("" : "+v"(kp[p]));
      }
      auto lag = [](int st) { return (st & 1) ? 7 - ((st & 7) >> 1) : (st & 7) >> 1; };
      auto ldA = [&](int st) {
        const int a = lag(st);
        const u32 p = kp[a >> 1] + 64 * (9 - (8 * (a & 1) + (st >> 3)));
        return __builtin_bit_cast(f16x8, (u32x4){lds_ld<u32>(p), lds_ld<u32>(p + 4), lds_ld<u32>(p + 8), lds_ld<u32>(p + 12)});
      };
      auto ldB = [&](int rr, int q) { return lds_ld<f16x8>(ub + 64 * (8 * q + 1 - rr)); };
      u32 ce = EMPTY, cof = (layer == 0 ? O_H + b * 40960 : O_H + LAY_SZ + b * 24576) + tid * 16;
      asm volatile("" : "+v"(ce), "+v"(cof));
      f16x8 A[3], B[8];
      A[0] = ldA(0);
#pragma unroll
      for (int q = 7; q >= 0; --q) B[q] = ldB(0, q);
      A[1] = ldA(1);
      A[2] = ldA(2);
      SCHED();
#pragma unroll
      for (int st = 0; st < 16; ++st) {
        const int a = lag(st);
#pragma unroll
        for (int q = 7 - a; q >= 0; --q) {
          acc[a + q] = __builtin_amdgcn_mfma_f32_16x16x32_f16(A[st % 3], B[q], acc[a + q], 0, 0, 0);
          if (st == 7) { SCHED(); B[q] = ldB(1, q); SCHED(); }
        }
        SCHED();
        if (st + 3 < 16) A[st % 3] = ldA(st + 3);
        if (st < 7 && !(st & 1)) B[7 - a] = ldB(1, 7 - a);
        if (st < (layer == 0 ? 5 : 3)) st16(rs, cof + st * 8192, (u32x4){ce, ce, ce, ce});
        if (layer == 0 && st < 8) w_put(1, st);
        if (layer == 0 && st >= 8) ldR(st);
        SCHED();
      }
      STAMP(1 + 3 * layer, 1);
      const int r = lane & 15, h = lane >> 4;
#pragma unroll
      for (int g = 0; g < 8; ++g) lds_st<f32x4>(L_Y + w * 8192 + (64 * g + 16 * h + (r ^ (4 * h))) * 16, acc[g]);
      if (layer == 0) WAITV(8); else WAITV(0);
    }
    {
      PHASE_IDS();
      const int c = lane >> 2, h = lane & 3;
      u32 y = L_Y + (64 * w + 16 * h + (c ^ (4 * h))) * 16;
      u32 xo = O_XT + layer * LAY_SZ + n * 128 + (tid >> 3) * 32768 + (tid & 1) * 64 + ((tid >> 1) & 3) * 16;
      asm volatile("" : "+v"(y), "+v"(xo));
      BAR();
      STAMP(1 + 3 * layer, 2);
      const f32x4 y0 = (lds_ld<f32x4>(y) + lds_ld<f32x4>(y + 8192)) + (lds_ld<f32x4>(y + 16384) + lds_ld<f32x4>(y + 24576));
      const f32x4 y1 = (lds_ld<f32x4>(y + 32768) + lds_ld<f32x4>(y + 40960)) + (lds_ld<f32x4>(y + 49152) + lds_ld<f32x4>(y + 57344));
      const u32 a0 = pkbf(fmaf(y0[0], ks0, base[0][0]), fmaf(y0[1], ks0, base[0][1]));
      const u32 a1 = pkbf(fmaf(y0[2], ks0, base[0][2]), fmaf(y0[3], ks0, base[0][3]));
      const u32 c0 = pkbf(fmaf(y1[0], ks1, base[1][0]), fmaf(y1[1], ks1, base[1][1]));
      const u32 c1 = pkbf(fmaf(y1[2], ks1, base[1][2]), fmaf(y1[3], ks1, base[1][3]));
      const bool odd = lane & 1;
      const u32 r0 = dppu<0xB1>(odd ? a0 : c0), r1 = dppu<0xB1>(odd ? a1 : c1);
      st16(rs, xo, notempty((u32x4){odd ? r0 : a0, odd ? r1 : a1, odd ? c0 : r0, odd ? c1 : r1}));
      STAMP(1 + 3 * layer, 3);
      const u32 xs0 = O_XT + layer * LAY_SZ + Ta * 32768, t16 = tid * 16;
      u32x4 xa[4];
      float cs0 = 0.f, cs1 = 0.f;
      auto colsum = [&](int r) {
#pragma unroll
        for (int j = 0; j < 8; ++j) {
          const u32x4 v = __builtin_bit_cast(u32x4, W[(j >> 1) * 8 + 2 * r + (j & 1)]);
          cs0 = dot2(v[0], 0x3f803f80u, cs0); cs1 = dot2(v[1], 0x3f803f80u, cs1);
          cs0 = dot2(v[2], 0x3f803f80u, cs0); cs1 = dot2(v[3], 0x3f803f80u, cs1);
        }
      };
      if (layer == 0) {
        w_get(1);
        u32 wa = L_F + w * 1024 + lane16, wb = wa - 65536;
        asm volatile("" : "+v"(wa), "+v"(wb));
        BAR();
        STAMP(1 + 3 * layer, 4);
#pragma unroll
        for (int j = 0; j < 8; ++j) lds_st<u32x4>(wa + j * 8192, R[j]);
        asm volatile("" :: "v"(R[15]) : "memory");
        xload<4>(rs, xa, t16, xs0, 8192);
        SCHED();
#pragma unroll
        for (int j = 0; j < 8; ++j) lds_st<u32x4>(wb + j * 8192, R[8 + j]);
        colsum(0);
        colsum(1);
        asm volatile("" : "+v"(cs0), "+v"(cs1));
        BAR();
        w_get(2);
#pragma unroll
        for (int g = 0; g < 4; ++g) { ra[g] -= 65536; asm volatile("" : "+v"(ra[g])); }
        w_get(3);
        colsum(2);
        colsum(3);
        cs0 += cs1;
        lds_st<float>(L_CS + w * 128 + ((r32 >> 2) & 1) * 64 + (r32 >> 3) * 16 + (r32 & 3) * 4, sw32(cs0, cs0));
        BAR();
        xpark<4>(rs, xa, t16, xs0, 8192, t16);
        STAMP(1 + 3 * layer, 5);
      }
    }
  };
  conv(Mode<0>{});
#pragma unroll 1
  for (int layer = 0; layer < 2; ++layer) {
    const u32 xt_off = O_XT + layer * LAY_SZ, h_off = O_H + layer * LAY_SZ;
    f32x4 e4, o4;
    auto ffn = [&](auto role) {
      constexpr bool A = decltype(role)::A;
      PHASE_IDS();
      auto hof = [&](int p) {
        const int i = p < 9 ? 2 * p : p < 18 ? 2 * p - 17 : 18 + 2 * (p & 1) + ((p - 18) >> 1);
        return h_off + (T2 * FT + min(kq + 4 * i, FT - 1)) * 1024;
      };
      const u32 w2s = O_W2F + (nt * FT + 12) * 2048;
      u32 xq[4];
#pragma unroll
      for (int g = 0; g < 4; ++g) xq[g] = hh * 512 + ((lane & 15) >> 2) * 64 + ((lane >> 4) & 1) * 32 + (lane & 3) * 8 + ((g + kq) & 3) * 8192;
      bf16x8 fr[PD];
      u32 csr = L_CS + w * 128 + hh * 64, str = L_ST + lane16;
      const u32 hso = (ftw < FT && ntile > 0 ? h_off + ftc * 1024 : OOB) + lane16;
      u32 hsa = hso + Ta * (FT * 1024), hsb = hso + Tb * (FT * 1024), hsc = hso + Tc * (FT * 1024);
      u32 rot16 = (((r32 + 8 * kq) & 31) + 32 * hh) * 16, r16 = r32 * 16;
      asm volatile("" : "+v"(r16), "+v"(xq[0]), "+v"(xq[1]), "+v"(xq[2]), "+v"(xq[3]), "+v"(csr), "+v"(str), "+v"(rot16), "+v"(hsa), "+v"(hsb), "+v"(hsc));
      u32 xsb = xt_off + Tb * 32768 + kq * 8192, xsc = xt_off + Tc * 32768 + kq * 8192, stw = str + kq * 4;
      asm volatile("" : "+s"(xsb), "+s"(xsc));
      asm volatile("" : "+v"(stw));
      const int sl = map >> 20, jt = (sl >> 2) & 1, Tj = jt ? Tc : Tb;
      const bool job = sl < 4 * (ntile - 1);
      u32 ja = job ? lane * 64 : OOB;
      const u32 js = (jt ? xsc : xsb) + (sl & 3) * 16;
      u32x4 jv[2];
      if (!A) asm volatile("" : "+v"(ja), "=v"(jv[0]), "=v"(jv[1]));
      if (layer == 1) {
        u32x4 xa[4];
        const u32 xs0 = xt_off + Ta * 32768, t16 = tid * 16;
        delay(X_DELAY);
        BAR();
        STAMP(4, 4);
        xload<4>(rs, xa, t16, xs0, 8192);
        if (!A) xload<2>(rs, jv, ja, js, 4096);
        xpark<4>(rs, xa, t16, xs0, 8192, t16);
        STAMP(4, 5);
      } else {
        if (!A) xload<2>(rs, jv, ja, js, 4096);
      }
      BAR();
      STAMP(1 + 3 * layer, 6);
      g1_pre(fr, xq, 0);
      bf16x8 Wr[3][2];
      u32x4 fh[13];
      auto slot = [](int p) { return p < 18 ? p % 9 : p - 9; };
      auto hcheck = [&](int p0, int p1) {
        u32 m = 0;
#pragma unroll
        for (int p = p0; p < p1; ++p) m = umax4(m, fh[slot(p)]);
        if (__builtin_expect(!none(m == EMPTY), 0)) {
#pragma unroll 1
          for (int s = 0; s < SPIN_MAX; ++s) {
            asm volatile("" ::: "memory");
            m = 0;
#pragma unroll
            for (int p = p0; p < p1; ++p)
              if (!none(umax4(0, fh[slot(p)]) == EMPTY)) fh[slot(p)] = ld16(rs, lane16, hof(p));
#pragma unroll
            for (int p = p0; p < p1; ++p) m = umax4(m, fh[slot(p)]);
            RETRY(p0);
            if (none(m == EMPTY)) break;
          }
        }
      };
      {
        f32x16 acc;
        auto nop = []() {};
        const u32 g1_off = O_G1 + layer * 32768;
        u32x4 gb, gc;
        auto refetch = [&](int T, int t, int k0, int k1) __attribute__((always_inline)) {
#pragma unroll 1
          for (int k = k0; k < k1; ++k) {
            u32x4 v[1];
            xload<1>(rs, v, lane16, xt_off + T * 32768 + k * 1024, 0);
            xpark<1>(rs, v, lane16, xt_off + T * 32768 + k * 1024, 0, t * 32768 + k * 1024 + lane16);
          }
          WAITL0();
        };
        auto recheck = [&](int T, int t, int toff, auto redo) __attribute__((always_inline)) {
          if (__builtin_expect(!none(acc[0] != acc[0]), 0)) {
            refetch(T, t, 0, 32);
            g1_pre(fr, xq, toff);
            g1_chain<0>(acc, W, fr, xq, toff, 0, [](int) {});
            MFMA_DRAIN(acc);
            redo();
          }
        };
        auto epi0 = [&]() {
          const f32x4 p = lds_ld<f32x4>(str);
          float s = p[0] + p[1], q = p[2] + p[3];
          asm volatile("" : "+v"(s), "+v"(q));
          s = (s + q) * (1.f / SD);
          const auto sw = __builtin_amdgcn_permlane32_swap(__float_as_uint(s), __float_as_uint(s), false, false);
          const float m = __uint_as_float(sw[0]);
          st16(rs, hsa, g1_epi<false>(acc, -m, fmaxf(fmaf(-m, m, __uint_as_float(sw[1])), 0.f) + 1e-5f, csr, nop));
        };
        const int Tg = ntile == 3 ? Tc : Tb;
        auto ldst = [&](int k) {
          if (k == 0) gb = ld16(rs, r16, g1_off + Tb * 512);
          if (k == 1) gc = ld16(rs, r16, g1_off + Tg * 512);
        };
        auto stat_wait = [&]() {
          if (!none(((gb[1] ^ tag) | (gb[3] ^ tag) | (gc[1] ^ tag) | (gc[3] ^ tag)) != 0)) {
#pragma unroll 1
            for (int s = 0; s < SPIN_MAX; ++s) {
              asm volatile("" ::: "memory");
              ldst(0);
              ldst(1);
              if (none(((gb[1] ^ tag) | (gb[3] ^ tag) | (gc[1] ^ tag) | (gc[3] ^ tag)) != 0)) break;
            }
          }
        };
        auto ldWr = [&](int k) {
          Wr[k >> 1][k & 1] = __builtin_bit_cast(bf16x8, ld16(rs, rot16 + (k & 1) * 1024, O_W2F + (nt * FT + kq + 4 * (k >> 1)) * 2048));
        };
        auto third = [&]() {
#pragma unroll
          for (int g = 0; g < 4; ++g) { xq[g] += 65536; asm volatile("" : "+v"(xq[g])); }
        };
        if constexpr (A) {
          g1_chain<8>(acc, W, fr, xq, 0, stw, [&](int k) {
            if (k >= 10) dmak(k - 2, rs, w2s + kq * 14336, L_R3 + kq * 14336, lane16);
          });
          BAR();
          STAMP(2 + 3 * layer, 0);
          epi0();
          g1_pre(fr, xq, 32768);
          BAR();
          STAMP(2 + 3 * layer, 1);
          g1_chain<0>(acc, W, fr, xq, 32768, 0, [&](int k) {
            if (k < 2) ldst(k);
            else if (k < 10) dmak(k - 2, rs, w2s + 57344 + kq * 8192, kq * 8192, lane16);
            else ldWr(k - 10);
          });
          BAR();
          STAMP(2 + 3 * layer, 2);
          asm volatile("" : "+v"(gb), "+v"(gc));
          recheck(Tb, 1, 32768, nop);
          stat_wait();
          st16(rs, hsb, g1_epi<false>(acc, __uint_as_float(gb[0]), __uint_as_float(gb[2]), csr, nop));
          third();
          g1_pre(fr, xq, 0);
          BAR();
          STAMP(2 + 3 * layer, 3);
          asm volatile("" : "=v"(fh[0]), "=v"(fh[1]), "=v"(fh[2]), "=v"(fh[3]), "=v"(fh[4]), "=v"(fh[5]), "=v"(fh[6]), "=v"(fh[7]),
                            "=v"(fh[8]), "=v"(fh[9]), "=v"(fh[10]));
          auto side = [&](int k) {
            if (k >= 5) fh[k - 5] = ld16(rs, lane16, hof(k < 14 ? k - 5 : k + 4));
          };
          if (ntile != 3) {
            delay(T2_DELAY);
#pragma unroll
            for (int k = 0; k < 16; ++k) side(k);
          } else {
            g1_chain<0>(acc, W, fr, xq, 0, 0, side);
          }
          BAR();
          STAMP(2 + 3 * layer, 4);
          if (ntile == 3) {
            recheck(Tc, 2, 0, [&]() {
#pragma unroll
              for (int k = 0; k < 16; ++k) side(k);
            });
            st16(rs, hsc, g1_epi<false>(acc, __uint_as_float(gc[0]), __uint_as_float(gc[2]), csr, nop));
          } else {
            delay(T2_DELAY);
          }
          hcheck(0, 9);
          fh[11] = ld16(rs, lane16, hof(20));
          fh[12] = ld16(rs, lane16, hof(21));
          STAMP(2 + 3 * layer, 5);
          BAR();
        } else {
          if (!xfull<2>(jv)) xretry<2>(rs, jv, ja, js, 4096);
#pragma unroll
          for (int k = 0; k < 8; ++k) dmak(k, rs, xsb, 32768 + kq * 8192, lane16);
          if (job) {
            const u32x4 v0 = jv[0], v1 = jv[1];
            float r[8];
#pragma unroll
            for (int k = 0; k < 8; ++k) {
              float x = (k & 1) ? bfhi(v0[k >> 1]) : bflo(v0[k >> 1]), y = (k & 1) ? bfhi(v1[k >> 1]) : bflo(v1[k >> 1]);
              float s1 = x + y, s2 = x * x;
              asm volatile("" : "+v"(s1), "+v"(s2));
              s2 = fmaf(y, y, s2);
              r[k] = sw32(s1, s2);
              asm volatile("" : "+v"(r[k]));
            }
#pragma unroll
            for (int k = 0; k < 4; ++k) {
              const auto sw = __builtin_amdgcn_permlane16_swap(__float_as_uint(r[k]), __float_as_uint(r[4 + k]), false, false);
              float t = __uint_as_float(sw[0]) + __uint_as_float(sw[1]);
              t += dppf<0xB1>(t); t += dppf<0x4E>(t); t += dppf<0x141>(t); t += dppf<0x140>(t);
              r[k] = t;
            }
            const bool b0 = lane & 1, b1 = lane & 2;
            lds_st<float>(L_ST + 1024 + (lane >> 4) * 64 + (lane & 3) * 16 + kq * 4, b1 ? (b0 ? r[3] : r[2]) : (b0 ? r[1] : r[0]));
            WAITV(0);
            WAITL0();
            u32 cnt = 0;
            if (lane == 0) cnt = __hip_atomic_fetch_add((LDSP u32*)(size_t)(L_TH + 2628), 1u, __ATOMIC_RELAXED, __HIP_MEMORY_SCOPE_WORKGROUP);
            if ((__builtin_amdgcn_readfirstlane(cnt) & 3) == 3) {
              asm volatile("" ::: "memory");
              const f32x4 p = lds_ld<f32x4>(L_ST + 1024 + (lane & 15) * 16);
              float s = p[0] + p[1], q = p[2] + p[3];
              asm volatile("" : "+v"(s), "+v"(q));
              const float m = (s + q) * (1.f / SD);
              const float v = fmaxf(fmaf(-m, m, dppf<0x128>(m)), 0.f) + 1e-5f;
              st16(rs, lane < 8 ? g1_off + (Tj * 32 + (sl & 3) * 8 + lane) * 16 : OOB, (u32x4){__float_as_uint(-m), tag, __float_as_uint(v), tag});
            }
          }
          if (!job) WAITV(0);
          BAR();
          STAMP(2 + 3 * layer, 0);
          g1_chain<0>(acc, W, fr, xq, 0, 0, [&](int k) {
            if (k < 8) dmak(k, rs, xsc, 65536 + kq * 8192, lane16);
            else dmak(k - 8, rs, w2s + kq * 14336, L_R3 + kq * 14336, lane16);
          });
          BAR();
          STAMP(2 + 3 * layer, 1);
          epi0();
          g1_pre(fr, xq, 32768);
          WAITV(1);
          if (layer == 0 && b == 0) st4(rs, tid == 256 ? O_EP : OOB, 0, tag);
          BAR();
          STAMP(2 + 3 * layer, 2);
          g1_chain<0>(acc, W, fr, xq, 32768, 0, [&](int k) {
            if (k < 2) ldst(k);
            else if (k < 8) ldWr(k - 2);
          });
          BAR();
          STAMP(2 + 3 * layer, 3);
          asm volatile("" : "+v"(gb), "+v"(gc));
          recheck(Tb, 1, 32768, nop);
          stat_wait();
          st16(rs, hsb, g1_epi<false>(acc, __uint_as_float(gb[0]), __uint_as_float(gb[2]), csr, nop));
          third();
          g1_pre(fr, xq, 0);
          BAR();
          STAMP(2 + 3 * layer, 4);
          auto side = [&](int k) {
            if (k < 8) dmak(k, rs, w2s + 90112 + kq * 8192, 32768 + kq * 8192, lane16);
          };
          if (ntile != 3) {
#pragma unroll
            for (int k = 0; k < 16; ++k) side(k);
          } else {
            g1_chain<0>(acc, W, fr, xq, 0, 0, side);
            recheck(Tc, 2, 0, nop);
          }
          WAITV(0);
          STAMP(2 + 3 * layer, 5);
          BAR();
          if (B_PRIO) __builtin_amdgcn_s_setprio(1);
          if (ntile != 3) hsc = OOB;
          st16(rs, hsc, g1_epi<false>(acc, __uint_as_float(gc[0]), __uint_as_float(gc[2]), csr, [&]() {
            SCHED();
#pragma unroll
            for (int p = 0; p < B_MID; ++p) fh[p] = ld16(rs, lane16, hof(p));
            SCHED();
          }));
#pragma unroll
          for (int p = B_MID; p < 11; ++p) fh[p] = ld16(rs, lane16, hof(p < 9 ? p : p + 9));
        }
      }
      STAMP(2 + 3 * layer, 6);
      f32x16 acc[2];
#pragma unroll
      for (int c = 0; c < 2; ++c)
#pragma unroll
        for (int e = 0; e < 16; ++e) acc[c][e] = 0.f;
      u32 wb0 = kq * 2048 + rot16, wb1 = wb0 + 65536, wb3 = wb0 + L_R3;
      asm volatile("" : "+v"(wb0), "+v"(wb1), "+v"(wb3));
      auto ldW = [&](int p, int c) {
        return lds_ld<bf16x8>((p < 10 ? wb3 + (p - 3) * 8192 : p < 18 ? wb0 + (p - 10) * 8192 : wb1 + (p - 18) * 8192) + c * 1024);
      };
      u16 rsd[8];
      SCHED();
#pragma unroll
      for (int p = 0; p < 22; ++p) {
        if (HCK == 0 || A) { if (p % 3 == 0 && p < 21 && (!A || p >= 9)) hcheck(p, p == 18 ? 22 : p + 3); }
        else if (p == 0) { hcheck(0, 9); delay(ODD_DELAY); }
        else if (p == 9) hcheck(9, 22);
        if (p < 21 || kq < 2) {
          acc[0] = __builtin_amdgcn_mfma_f32_32x32x16_bf16(Wr[p % 3][0], __builtin_bit_cast(bf16x8, fh[slot(p)]), acc[0], 0, 0, 0);
          acc[1] = __builtin_amdgcn_mfma_f32_32x32x16_bf16(Wr[p % 3][1], __builtin_bit_cast(bf16x8, fh[slot(p)]), acc[1], 0, 0, 0);
        }
        SCHED();
        if (A && p < 8) dmak(p & 1, rs, w2s + 122880 + (p >> 1) * 8192 + kq * 2048, 65536 + (p >> 1) * 8192 + kq * 2048, lane16);
        if (p < 9) fh[p] = ld16(rs, lane16, hof(p + 9));
        if (!A && p == 8) {
          fh[11] = ld16(rs, lane16, hof(20));
          fh[12] = ld16(rs, lane16, hof(21));
        }
        if (p == 8) {
          STAMP(3 + 3 * layer, 2);
          if (!A && B_PRIO) __builtin_amdgcn_s_setprio(0);
        }
        if (p == 11) {
#pragma unroll
          for (int j = 0; j < 8; ++j) rsd[j] = ld2(rs, (8 * hh + j) * 64 + r32 * 2, xt_off + T2 * 32768 + (nt * 64 + kq * 16) * 64);
        }
        if (p == 15) { if constexpr (!A) BAR(); else WAITV(8); }
        if (p + 3 < 22) { Wr[p % 3][0] = ldW(p + 3, 0); Wr[p % 3][1] = ldW(p + 3, 1); }
        SCHED();
      }
      STAMP(3 + 3 * layer, 3);
      if constexpr (A) BAR();
      const u32 pw = L_R3 + kq * 2048 + tt * 24576 + lane16;
#pragma unroll
      for (int q = 1; q < 4; ++q) {
        lds_st<f32x4>(pw + (q - 1) * 8192, (f32x4){acc[0][4 * q], acc[0][4 * q + 1], acc[0][4 * q + 2], acc[0][4 * q + 3]});
        lds_st<f32x4>(pw + (q - 1) * 8192 + 1024, (f32x4){acc[1][4 * q], acc[1][4 * q + 1], acc[1][4 * q + 2], acc[1][4 * q + 3]});
      }
#pragma unroll
      for (int e = 0; e < 4; ++e) { e4[e] = acc[0][e] + b2f(rsd[2 * e]); o4[e] = acc[1][e] + b2f(rsd[2 * e + 1]); }
      asm volatile("" : "+v"(e4), "+v"(o4));
    };
    asm volatile("" : "=v"(e4), "=v"(o4));
    if (w0 < 4) ffn(Role<true>{}); else ffn(Role<false>{});
    PHASE_IDS();
    {
      BAR();
      STAMP(3 + 3 * layer, 4);
#pragma unroll
      for (int q = 1; q < 4; ++q) {
        const u32 a = L_R3 + ((kq - q) & 3) * 2048 + tt * 24576 + lane16 + (q - 1) * 8192;
        e4 += lds_ld<f32x4>(a);
        o4 += lds_ld<f32x4>(a + 1024);
      }
      u32x4 o = {pkbf(e4[0], o4[0]), pkbf(e4[1], o4[1]), pkbf(e4[2], o4[2]), pkbf(e4[3], o4[3])};
      int last = layer;
      asm volatile("" : "+s"(last));
      if (last == 1) {
        *(u32x4*)(out + (size_t)(T2 * 32 + r32) * SD + nt * 64 + kq * 16 + hh * 8) = o;
        continue;
      }
      float s1 = 0.f, s2 = 0.f;
#pragma unroll
      for (int k = 0; k < 4; ++k) { s1 = dot2(o[k], 0x3f803f80u, s1); s2 = dot2(o[k], o[k], s2); }
      lds_st<float>(L_ST + tt * 1024 + lane16 + kq * 4, sw32(s1, s2));
      BAR();
      if (kq == 0) {
        const f32x4 p = lds_ld<f32x4>(L_ST + tt * 1024 + lane16);
        const float x = ((p[0] + p[1]) + (p[2] + p[3])) * (hh ? 0.0625f : 1.f), hi = floorf(x);
        const bool ok = fabsf(x) < 134217728.f;
        const u32 lo = ok ? ((u32)((x - hi) * 4294967296.f) & ~255u) | 1u : 0x11u;
        const int t = T2 * 32 + r32;
        __hip_atomic_fetch_add((GLBP u64*)(ws + O_S2 + hh * 16384 + ((t >> 1) & 1) * 8192 + (t >> 2) * 16 + (t & 1) * 8),
                               ((u64)(u32)(ok ? (int)hi : 0) << 32) | lo, __ATOMIC_RELAXED, __HIP_MEMORY_SCOPE_AGENT);
      }
      {
        const bool b0 = lane & 1, b1 = lane & 2;
        const u32 s0 = dppu<0xB1>(b0 ? o[0] : o[1]), s1_ = dppu<0xB1>(b0 ? o[2] : o[3]);
        const u32 p0 = b0 ? s0 : o[0], p1 = b0 ? o[1] : s0, p2 = b0 ? s1_ : o[2], p3 = b0 ? o[3] : s1_;
        const u32 t0 = dppu<0x4E>(b1 ? p0 : p2), t1 = dppu<0x4E>(b1 ? p1 : p3);
        o = (u32x4){b1 ? t0 : p0, b1 ? t1 : p1, b1 ? p2 : t0, b1 ? p3 : t1};
      }
      st16(rs, O_XP + (nt * 32 + kq * 8 + hh * 4 + (lane & 3)) * 8192 + (T2 * 32 + (r32 & ~3)) * 4, notempty(o));
    }
    if (S3_GATE && w == 4) st4(rs, lane == 0 ? O_F3 + b * 128 : OOB, 0, tag);
    STAMP(3, 5);
    {
      u32 kix[9];
      conv_prep(tid, w, kix);
      expand(tid, ch, kix);
    }
    delay(S3_DELAY);
    if (S3_GATE) {
      if (w < 4) poll(rs, O_F3 + tid * 128, 0, tag);
      BAR();
    }
    STAMP(3, 6);
    {
      u32x4 sv[4], xr;
      xload<4>(rs, sv, tid * 16, O_S2, 8192);
      xr = ld16(rs, tid * 16, O_XP + n * 8192);
      auto s3_ok = [&]() {
        u32 all = 8;
#pragma unroll
        for (int k = 0; k < 4; ++k) all &= sv[k][0] & sv[k][2];
        return none(all == 0 || umax4(0, xr) == EMPTY);
      };
      if (!s3_ok()) {
#pragma unroll 1
        for (int s = 0; s < SPIN_MAX; ++s) {
          asm volatile("" ::: "memory");
          u32x4 t[5];
#pragma unroll
          for (int k = 0; k < 4; ++k) t[k] = ld16(rs, (sv[k][0] & sv[k][2] & 8) ? OOB : tid * 16, O_S2 + k * 8192);
          t[4] = ld16(rs, umax4(0, xr) == EMPTY ? tid * 16 : OOB, O_XP + n * 8192);
#pragma unroll
          for (int k = 0; k < 4; ++k) sv[k] = (sv[k][0] & sv[k][2] & 8) ? sv[k] : t[k];
          xr = umax4(0, xr) == EMPTY ? t[4] : xr;
          if (s3_ok()) break;
          __builtin_amdgcn_s_sleep(4);
        }
      }
      u32 fl = 0;
#pragma unroll
      for (int k = 0; k < 4; ++k) fl |= sv[k][0] | sv[k][2];
      STAMP(3, 7);
      float s1[4], s2[4];
#pragma unroll
      for (int i = 0; i < 4; ++i) {
        s1[i] = fmaf((float)sv[i >> 1][2 * (i & 1)], 2.3283064365386963e-10f, (float)(int)sv[i >> 1][2 * (i & 1) + 1]);
        s2[i] = fmaf((float)sv[2 + (i >> 1)][2 * (i & 1)], 2.3283064365386963e-10f, (float)(int)sv[2 + (i >> 1)][2 * (i & 1) + 1]) * 16.f;
      }
      if (__builtin_expect(!none((fl & 0xf0) != 0), 0)) {
#pragma unroll
        for (int i = 0; i < 4; ++i) s1[i] = s2[i] = 0.f;
#pragma unroll 1
        for (int p = 0; p < 256; ++p) {
          u32x4 v;
#pragma unroll 1
          for (int s = 0; s < SPIN_MAX; ++s) {
            asm volatile("" ::: "memory");
            v = ld16(rs, tid * 16, O_XP + p * 8192);
            if (none(umax4(0, v) == EMPTY)) break;
          }
#pragma unroll
          for (int i = 0; i < 4; ++i) { s1[i] = dot2(v[i], 0x3f803f80u, s1[i]); s2[i] = dot2(v[i], v[i], s2[i]); }
        }
      }
      float mean[4], rstd[4];
#pragma unroll
      for (int i = 0; i < 4; ++i) {
        mean[i] = s1[i] * (1.f / SD);
        rstd[i] = __builtin_amdgcn_rsqf(fmaxf(fmaf(s2[i], 1.f / SD, -mean[i] * mean[i]), 0.f) + 1e-5f);
      }
      normalize(tid, xr, mean, rstd);
    }
    conv(Mode<1>{});
  }
  asm volatile("KCODE_END:");
}

extern "C" void kernel_launch(void* const* d_in, const int* in_sizes, int n_in, void* d_out, int out_size,
                              void* d_ws, size_t ws_size, hipStream_t stream) {
  k_all<<<256, 512, 0, stream>>>((const u16*)d_in[0], (const u16*)d_in[1], (const u16*)d_in[2], (const u16*)d_in[3],
                                 (const u16*)d_in[4], (const u16*)d_in[5], (const u16*)d_in[6], (const u16*)d_in[8],
                                 (const u16*)d_in[9], (const u16*)d_in[10], (char*)d_ws, (u16*)d_out);
}
```

```cpp
#include <hip/hip_runtime.h>
#include <stdint.h>

typedef unsigned short u16;
typedef uint32_t u32;
typedef unsigned long long u64;
typedef __attribute__((ext_vector_type(2))) float f32x2;
typedef __attribute__((ext_vector_type(4))) float f32x4;
typedef __attribute__((ext_vector_type(16))) float f32x16;
typedef __attribute__((ext_vector_type(2))) _Float16 f16x2;
typedef __attribute__((ext_vector_type(8))) _Float16 f16x8;
typedef __attribute__((ext_vector_type(2))) __bf16 bf16x2;
typedef __attribute__((ext_vector_type(8))) __bf16 bf16x8;
typedef __attribute__((ext_vector_type(4))) short s16x4;
typedef __attribute__((ext_vector_type(2))) u32 u32x2;
typedef __attribute__((ext_vector_type(4))) u32 u32x4;
typedef __amdgpu_buffer_rsrc_t rsrc_t;
#define LDSP __attribute__((address_space(3)))
#define GLBP __attribute__((address_space(1)))
#define DEVI __device__ __forceinline__

constexpr int SL = 2048, SD = 512, NM = 128, DF = 1376, FT = 86;
constexpr int KCOPY = 2080;
constexpr int KCH_B = 2 * KCOPY * 2;
constexpr u32 O_EP = 0;
constexpr u32 O_F3 = 4096;
constexpr u32 O_G0 = O_F3 + 32768;
constexpr u32 O_G1 = O_G0 + 32768;
constexpr u32 O_S2 = O_G1 + 65536;
constexpr u32 O_W2F = O_S2 + 32768;
constexpr u32 H_SZ = 64 * FT * 1024;
constexpr u32 O_XT = O_W2F + 8 * FT * 2048 + 4096;
constexpr u32 O_H = O_XT + (2u << 20);
constexpr u32 O_XP = O_H + H_SZ;
constexpr u32 LAY_SZ = (4u << 20) + H_SZ;
constexpr u32 O_XG = O_H + LAY_SZ + (6u << 20);
constexpr u32 WS_END = O_XG + (4u << 20);
constexpr int L_Y = 0;
constexpr int TBP = 320, L_TB = 0, TB_W = 26 * TBP;
constexpr int L_K = 32768;
constexpr int L_U = L_K + 2 * KCH_B, U_CH = (240 + 2048) * 2;
constexpr int L_F = 91392;
constexpr int L_R3 = 98304;
constexpr int L_ST = 156928;
constexpr int L_CS = L_ST + 3072;
constexpr int L_TH = L_CS + 1024;
constexpr int L_RS = L_TH + 2640;
constexpr int L_SZ = 163840;
constexpr int SPIN_MAX = 1 << 16;
#define X_DELAY 2
#define CHAIN_PRIO 0
#define RAW0 8
#define RAW1 12
#define RAW2 16
#define S3_GATE 0
#define B_PRIO 1
#define B_AHEAD 4
#define T2_DELAY 6
#define B_MID 0
#define X_ROUND 2
#define GRAN_AT 2
#define HCK 0
#define ODD_DELAY 0
#define ST_A 4
#define WROT 0
#define CLR_E 0
#define CLR_M 0
#define LATE_DELAY 9
#define ST1_AT 8
#define WT_AT 4
#define NHW 12
#define EARLY_T 44
#define S3_STEP 4
#define S3_DELAY 9

DEVI float bflo(u32 v) { return __uint_as_float(v << 16); }
DEVI float bfhi(u32 v) { return __uint_as_float(v & 0xffff0000u); }
DEVI float b2f(u16 v) { return __uint_as_float(((u32)v) << 16); }
DEVI u32 pkbf(float a, float b) { bf16x2 r = __builtin_convertvector((f32x2){a, b}, bf16x2); return __builtin_bit_cast(u32, r); }
DEVI u32 pkf16(float a, float b) { f16x2 r = __builtin_convertvector((f32x2){a, b}, f16x2); return __builtin_bit_cast(u32, r); }
DEVI float rbf(float a) { return bflo(pkbf(a, 0.f)); }
template <int C> DEVI float dppf(float v) { return __uint_as_float(__builtin_amdgcn_update_dpp(0u, __float_as_uint(v), C, 0xf, 0xf, true)); }
template <int C> DEVI u32 dppu(u32 v) { return __builtin_amdgcn_update_dpp(0u, v, C, 0xf, 0xf, true); }
DEVI float sw16(float v) { auto r = __builtin_amdgcn_permlane16_swap(__float_as_uint(v), __float_as_uint(v), false, false); return __uint_as_float(r[0]) + __uint_as_float(r[1]); }
DEVI float sw32(float a, float b) { auto r = __builtin_amdgcn_permlane32_swap(__float_as_uint(a), __float_as_uint(b), false, false); return __uint_as_float(r[0]) + __uint_as_float(r[1]); }
DEVI float wsum(float v) { v += dppf<0xB1>(v); v += dppf<0x4E>(v); v += dppf<0x141>(v); v += dppf<0x140>(v); v = sw16(v); return sw32(v, v); }
DEVI float wmax(float v) {
  v = fmaxf(v, dppf<0xB1>(v)); v = fmaxf(v, dppf<0x4E>(v)); v = fmaxf(v, dppf<0x141>(v)); v = fmaxf(v, dppf<0x140>(v));
  auto r = __builtin_amdgcn_permlane16_swap(__float_as_uint(v), __float_as_uint(v), false, false);
  v = fmaxf(__uint_as_float(r[0]), __uint_as_float(r[1]));
  r = __builtin_amdgcn_permlane32_swap(__float_as_uint(v), __float_as_uint(v), false, false);
  return fmaxf(__uint_as_float(r[0]), __uint_as_float(r[1]));
}
DEVI float dot2(u32 a, u32 b, float c) { return __builtin_amdgcn_fdot2_f32_bf16(__builtin_bit_cast(bf16x2, a), __builtin_bit_cast(bf16x2, b), c, false); }

#define WAITV(n) asm volatile("s_waitcnt vmcnt(" #n ")" ::: "memory")
#define WAITL0() asm volatile("s_waitcnt lgkmcnt(0)" ::: "memory")
#define SCHED() __builtin_amdgcn_sched_barrier(0)
#define BAR() do { asm volatile("s_waitcnt lgkmcnt(0)\n\ts_barrier" ::: "memory"); } while (0)

#define STAMP(k, i)
#define RETRY(p)

DEVI u32x4 ld16(rsrc_t rs, u32 voff, u32 soff) { return __builtin_amdgcn_raw_buffer_load_b128(rs, voff, soff, 16); }
DEVI u32 ld4(rsrc_t rs, u32 voff, u32 soff) { return __builtin_amdgcn_raw_buffer_load_b32(rs, voff, soff, 16); }
DEVI u16 ld2(rsrc_t rs, u32 voff, u32 soff) { return __builtin_amdgcn_raw_buffer_load_b16(rs, voff, soff, 16); }
DEVI void st16(rsrc_t rs, u32 off, u32x4 v) { __builtin_amdgcn_raw_buffer_store_b128(v, rs, off, 0, 16); }
DEVI void st4(rsrc_t rs, u32 voff, u32 soff, u32 v) { __builtin_amdgcn_raw_buffer_store_b32(v, rs, voff, soff, 16); }
template <int K> DEVI void dma1(rsrc_t rs, u32 goff, u32 lds, u32 lane16) {
  const u32 g = goff + (K & ~3) * 1024;
  if constexpr ((K & 3) == 0)
    asm volatile("s_mov_b32 m0, %0\n\ts_nop 0\n\tbuffer_load_dwordx4 %1, %2, %3 offen sc1 lds"
                 :: "s"(lds + K * 1024), "v"(lane16), "s"(rs), "s"(g) : "memory");
  else
    asm volatile("buffer_load_dwordx4 %0, %1, %2 offen offset:%3 sc1 lds" :: "v"(lane16), "s"(rs), "s"(g), "i"((K & 3) * 1024) : "memory");
}
DEVI void dmak(int k, rsrc_t rs, u32 goff, u32 lds, u32 lane16) {
  switch (k) {
#define DK(n) case n: dma1<n>(rs, goff, lds, lane16); break;
    DK(0) DK(1) DK(2) DK(3) DK(4) DK(5) DK(6) DK(7) DK(8) DK(9) DK(10) DK(11) DK(12) DK(13)
#undef DK
  }
}
DEVI bool none(bool p) { return __builtin_amdgcn_ballot_w64(p) == 0; }
constexpr u32 OOB = 0x80000000u;
DEVI void poll(rsrc_t rs, u32 f, u32 voff, u32 soff, u32 tag) {
#pragma unroll 1
  for (int s = 0; s < SPIN_MAX; ++s) {
    if (none(f != tag)) break;
    asm volatile("" ::: "memory");
    f = ld4(rs, voff, soff);
  }
  asm volatile("" ::: "memory");
}
DEVI void poll(rsrc_t rs, u32 voff, u32 soff, u32 tag) {
  asm volatile("" ::: "memory");
  poll(rs, ld4(rs, voff, soff), voff, soff, tag);
}
constexpr u32 EMPTY = 0xffffffffu;
DEVI u32 umax4(u32 m, const u32x4& v) { return max(max(max(max(m, v[0]), v[1]), v[2]), v[3]); }
DEVI u32x4 notempty(u32x4 v) { return (u32x4){min(v[0], EMPTY - 1), min(v[1], EMPTY - 1), min(v[2], EMPTY - 1), min(v[3], EMPTY - 1)}; }
template <int N> DEVI void xload(rsrc_t rs, u32x4 (&v)[N], u32 voff, u32 soff, u32 stride) {
#pragma unroll
  for (int i = 0; i < N; ++i) v[i] = ld16(rs, voff, soff + i * stride);
}
template <int N> DEVI bool xfull(const u32x4 (&v)[N]) {
  u32 m = 0;
#pragma unroll
  for (int i = 0; i < N; ++i) m = umax4(m, v[i]);
  return none(m == EMPTY);
}
template <int N> DEVI void xretry(rsrc_t rs, u32x4 (&v)[N], u32 voff, u32 soff, u32 stride) {
#pragma unroll 1
  for (int s = 0; s < SPIN_MAX; ++s) {
    asm volatile("" ::: "memory");
    u32x4 t[N];
#pragma unroll
    for (int i = 0; i < N; ++i) t[i] = ld16(rs, umax4(0, v[i]) == EMPTY ? voff : OOB, soff + i * stride);
#pragma unroll
    for (int i = 0; i < N; ++i) v[i] = umax4(0, v[i]) == EMPTY ? t[i] : v[i];
    if (xfull<N>(v)) break;
    __builtin_amdgcn_s_sleep(4);
  }
}
template <int N> DEVI void xpark(rsrc_t rs, u32x4 (&v)[N], u32 voff, u32 soff, u32 stride, u32 lds) {
  if (!xfull<N>(v)) xretry<N>(rs, v, voff, soff, stride);
#pragma unroll
  for (int i = 0; i < N; ++i) *(LDSP u32x4*)(size_t)(lds + i * stride) = v[i];
}
DEVI void delay(int n) { for (int i = 0; i < n; ++i) __builtin_amdgcn_s_sleep(2); }

DEVI f32x2 cexp(float are, float aim, float p) {
  const float e = __builtin_amdgcn_exp2f(are * p), ph = __builtin_amdgcn_fractf(aim * p);
  return (f32x2){__builtin_amdgcn_cosf(ph), __builtin_amdgcn_sinf(ph)} * (f32x2){e, e};
}
DEVI f32x2 cmul(f32x2 a, f32x2 b) {
  f32x2 r;
  asm("v_pk_mul_f32 %0, %1, %2 op_sel:[0,0] op_sel_hi:[0,1]\n\t"
      "v_pk_fma_f32 %0, %1, %2, %0 op_sel:[1,1,0] op_sel_hi:[1,0,1] neg_lo:[1,0,0]" : "=&v"(r) : "v"(a), "v"(b));
  return r;
}
DEVI f16x8 cmul4(f32x4 a0, f32x4 a1, f32x4 b0, f32x4 b1) {
  f32x2 r0, r1, r2, r3;
  asm("v_pk_mul_f32 %0, %4, %8 op_sel:[0,0] op_sel_hi:[0,1]\n\tv_pk_mul_f32 %1, %5, %9 op_sel:[0,0] op_sel_hi:[0,1]\n\t"
      "v_pk_mul_f32 %2, %6, %10 op_sel:[0,0] op_sel_hi:[0,1]\n\tv_pk_mul_f32 %3, %7, %11 op_sel:[0,0] op_sel_hi:[0,1]\n\t"
      "v_pk_fma_f32 %0, %4, %8, %0 op_sel:[1,1,0] op_sel_hi:[1,0,1] neg_lo:[1,0,0]\n\t"
      "v_pk_fma_f32 %1, %5, %9, %1 op_sel:[1,1,0] op_sel_hi:[1,0,1] neg_lo:[1,0,0]\n\t"
      "v_pk_fma_f32 %2, %6, %10, %2 op_sel:[1,1,0] op_sel_hi:[1,0,1] neg_lo:[1,0,0]\n\t"
      "v_pk_fma_f32 %3, %7, %11, %3 op_sel:[1,1,0] op_sel_hi:[1,0,1] neg_lo:[1,0,0]"
      : "=&v"(r0), "=&v"(r1), "=&v"(r2), "=&v"(r3)
      : "v"(a0.xy), "v"(a0.zw), "v"(a1.xy), "v"(a1.zw), "v"(b0.xy), "v"(b0.zw), "v"(b1.xy), "v"(b1.zw));
  return __builtin_bit_cast(f16x8, (u32x4){pkf16(r0[0], r0[1]), pkf16(r1[0], r1[1]), pkf16(r2[0], r2[1]), pkf16(r3[0], r3[1])});
}
DEVI s16x4 trread(u32 a) { return __builtin_amdgcn_ds_read_tr16_b64_v4i16((LDSP s16x4*)(size_t)a); }
DEVI bf16x8 trfrag(u32 a) {
  const s16x4 lo = trread(a), hi = trread(a + 256);
  return __builtin_bit_cast(bf16x8, __builtin_shufflevector(lo, hi, 0, 1, 2, 3, 4, 5, 6, 7));
}
template <class T> DEVI T lds_ld(u32 a) { return *(const LDSP T*)(size_t)a; }
template <class T> DEVI void lds_st(u32 a, T v) { *(LDSP T*)(size_t)a = v; }

constexpr int PD = 4;
DEVI void g1_pre(bf16x8 (&fr)[PD], const u32 (&xq)[4], int toff) {
#pragma unroll
  for (int i = 0; i < PD; ++i) fr[i] = trfrag(xq[0] + toff + i * 1024);
}
template <int ST, class F>
DEVI float g1_chain(f32x16& acc, const bf16x8 (&W)[32], bf16x8 (&fr)[PD], const u32 (&xq)[4], int toff, u32 stdst, F side) {
#pragma unroll
  for (int e = 0; e < 16; ++e) acc[e] = 0.f;
  f32x4 q1 = {0.f, 0.f, 0.f, 0.f}, q2 = {0.f, 0.f, 0.f, 0.f};
  u32 one2 = 0x3f803f80u;
  if (ST) asm volatile("" : "+v"(one2));
  const s16x4 ones = __builtin_bit_cast(s16x4, (u32x2){one2, one2});
  auto fin = [&]() {
    const bool b0 = stdst & 16, b1 = stdst & 32;
    return sw32(q1[0], b1 ? (b0 ? q2[3] : q2[2]) : (b0 ? q2[1] : q2[0]));
  };
  if (CHAIN_PRIO) __builtin_amdgcn_s_setprio(1);
  SCHED();
#pragma unroll
  for (int i = 0; i < 32; ++i) {
    if (i < ST) {
      const u32x4 v = __builtin_bit_cast(u32x4, fr[i % PD]);
#pragma unroll
      for (int j = 0; j < 2; ++j) {
        const s16x4 x = __builtin_bit_cast(s16x4, (u32x2){v[2 * j], v[2 * j + 1]});
        q2 = __builtin_amdgcn_mfma_f32_4x4x4bf16_1k(x, x, q2, 0, 0, 0);
        q1 = __builtin_amdgcn_mfma_f32_4x4x4bf16_1k(ones, x, q1, 0, 0, 0);
      }
      SCHED();
    }
    acc = __builtin_amdgcn_mfma_f32_32x32x16_bf16(W[i], fr[i % PD], acc, 0, 0, 0);
    SCHED();
    if (ST > 0 && ST < 32 && i == ST + 1) lds_st<float>(stdst, fin());
    if (i + PD < 32) fr[i % PD] = trfrag(xq[(i + PD) >> 3] + toff + ((i + PD) & 7) * 1024);
    if (!(i & 1)) side(i >> 1);
    SCHED();
  }
  if (CHAIN_PRIO) __builtin_amdgcn_s_setprio(0);
  return ST == 32 ? fin() : 0.f;
}
template <int I> struct Idx { static constexpr int V = I; };
template <int I0, int I1, class F> DEVI void sfor(F f) {
  if constexpr (I0 < I1) { f(Idx<I0>{}); sfor<I0 + 1, I1>(f); }
}
constexpr int cv_lag(int st) { return st >= 24 ? st - 24 : (st & 1) ? 7 - ((st & 7) >> 1) : (st & 7) >> 1; }
template <bool V> struct Role { static constexpr bool A = V; };
template <int M> struct Mode { static constexpr int V = M; };
template <bool PK, class F> DEVI u32x4 g1_epi(const f32x16& acc, float nm, float v, u32 cssrc, F mid) {
  f32x4 cs[4];
#pragma unroll
  for (int q = 0; q < 4; ++q) cs[q] = lds_ld<f32x4>(cssrc + q * 16);
  const float rs = __builtin_amdgcn_rsqf(v);
  const float k0 = rs * -2.302208198f, k1 = rs * rs * rs * -0.102943239f;
  if constexpr (PK) {
    u32x4 o;
    f32x2 g[4], e[4];
#pragma unroll
    for (int p = 0; p < 4; ++p) {
      g[p] = __builtin_elementwise_fma((f32x2){cs[p >> 1][2 * (p & 1)], cs[p >> 1][2 * (p & 1) + 1]}, (f32x2){nm, nm}, (f32x2){acc[2 * p], acc[2 * p + 1]});
      const f32x2 t = g[p] * __builtin_elementwise_fma(g[p] * g[p], (f32x2){k1, k1}, (f32x2){k0, k0});
      e[p] = (f32x2){__builtin_amdgcn_exp2f(t[0]), __builtin_amdgcn_exp2f(t[1])};
    }
#pragma unroll
    for (int p = 0; p < 4; ++p) {
      const f32x2 d = __builtin_elementwise_fma(e[p], (f32x2){v, v}, (f32x2){v, v});
      const f32x2 uu = __builtin_elementwise_fma((f32x2){cs[2 + (p >> 1)][2 * (p & 1)], cs[2 + (p >> 1)][2 * (p & 1) + 1]}, (f32x2){nm, nm},
                                                 (f32x2){acc[8 + 2 * p], acc[9 + 2 * p]});
      const f32x2 r = g[p] * uu * (f32x2){__builtin_amdgcn_rcpf(d[0]), __builtin_amdgcn_rcpf(d[1])};
      o[p] = pkbf(r[0], r[1]);
    }
    return notempty(o);
  }
  u32x4 o;
#pragma unroll
  for (int q = 0; q < 2; ++q) {
    float g0 = cs[q][0], g1 = cs[q][1], g2 = cs[q][2], g3 = cs[q][3];
    float u0 = cs[2 + q][0], u1 = cs[2 + q][1], u2 = cs[2 + q][2], u3 = cs[2 + q][3];
    float t0, t1, t2, t3;
    asm("v_fma_f32 %0, %20, %0, %12\n\tv_fma_f32 %1, %20, %1, %13\n\tv_fma_f32 %2, %20, %2, %14\n\tv_fma_f32 %3, %20, %3, %15\n\t"
        "v_mul_f32 %8, %0, %0\n\tv_mul_f32 %9, %1, %1\n\tv_mul_f32 %10, %2, %2\n\tv_mul_f32 %11, %3, %3\n\t"
        "v_fma_f32 %8, %8, %22, %21\n\tv_fma_f32 %9, %9, %22, %21\n\tv_fma_f32 %10, %10, %22, %21\n\tv_fma_f32 %11, %11, %22, %21\n\t"
        "v_mul_f32 %8, %0, %8\n\tv_mul_f32 %9, %1, %9\n\tv_mul_f32 %10, %2, %10\n\tv_mul_f32 %11, %3, %11\n\t"
        "v_exp_f32 %8, %8\n\tv_exp_f32 %9, %9\n\tv_exp_f32 %10, %10\n\tv_exp_f32 %11, %11\n\t"
        "v_fma_f32 %4, %20, %4, %16\n\tv_fma_f32 %5, %20, %5, %17\n\tv_fma_f32 %6, %20, %6, %18\n\tv_fma_f32 %7, %20, %7, %19\n\t"
        "v_fma_f32 %8, %8, %23, %23\n\tv_fma_f32 %9, %9, %23, %23\n\tv_fma_f32 %10, %10, %23, %23\n\tv_fma_f32 %11, %11, %23, %23\n\t"
        "v_rcp_f32 %8, %8\n\tv_rcp_f32 %9, %9\n\tv_rcp_f32 %10, %10\n\tv_rcp_f32 %11, %11\n\t"
        "v_mul_f32 %0, %0, %4\n\tv_mul_f32 %1, %1, %5\n\tv_mul_f32 %2, %2, %6\n\tv_mul_f32 %3, %3, %7\n\t"
        "v_mul_f32 %0, %0, %8\n\tv_mul_f32 %1, %1, %9\n\tv_mul_f32 %2, %2, %10\n\tv_mul_f32 %3, %3, %11"
        : "+v"(g0), "+v"(g1), "+v"(g2), "+v"(g3), "+v"(u0), "+v"(u1), "+v"(u2), "+v"(u3), "=&v"(t0), "=&v"(t1), "=&v"(t2), "=&v"(t3)
        : "v"(acc[4 * q]), "v"(acc[4 * q + 1]), "v"(acc[4 * q + 2]), "v"(acc[4 * q + 3]),
          "v"(acc[8 + 4 * q]), "v"(acc[9 + 4 * q]), "v"(acc[10 + 4 * q]), "v"(acc[11 + 4 * q]), "v"(nm), "v"(k0), "v"(k1), "v"(v));
    o[2 * q] = pkbf(g0, g1);
    o[2 * q + 1] = pkbf(g2, g3);
    if (q == 0) mid();
  }
  return notempty(o);
}
#define MFMA_DRAIN(a) asm volatile("s_nop 15\n\ts_nop 3" : "+v"(a))

__global__ __launch_bounds__(512, 2) void k_all(const u16* __restrict__ u, const u16* __restrict__ logA,
                                                const u16* __restrict__ Aim, const u16* __restrict__ Cre,
                                                const u16* __restrict__ Cim, const u16* __restrict__ logdt,
                                                const u16* __restrict__ dsk, const u16* __restrict__ wg,
                                                const u16* __restrict__ wu, const u16* __restrict__ wd,
                                                char* ws, u16* __restrict__ out) {
  __shared__ __attribute__((aligned(16))) char sm[L_SZ];
  const int tid0 = threadIdx.x, w0 = __builtin_amdgcn_readfirstlane(tid0 >> 6), b = blockIdx.x;
  const int n0 = (b & 7) * 32 + (b >> 3);
  asm volatile("" :: "v"((LDSP char*)sm) : "memory");
  STAMP(0, 0);
  u32 pf;
  {
    u64 pc;
    u32 len;
    asm volatile("s_getpc_b64 %0\nKCODE_BEG:\n\ts_mov_b32 %1, KCODE_END-KCODE_BEG-4\n\t.subsection 1\nKCODE_END:\n\t.subsection 0" : "=s"(pc), "=s"(len));
    const u32 o = min((u32)(((b >> 3) + 32 * (tid0 & 15)) * 128), len);
    asm volatile("global_load_dword %0, %1, %2" : "=v"(pf) : "v"(o), "s"(pc) : "memory");
  }
  asm volatile("" :: "s"(u), "s"(logA), "s"(Aim), "s"(Cre), "s"(Cim), "s"(logdt), "s"(dsk), "s"(wg), "s"(wu), "s"(wd), "s"(ws), "s"(out));
  const rsrc_t rs = __builtin_amdgcn_make_buffer_rsrc(ws, 0, WS_END, 0x00020000);
  u32 map0;
#define PHASE_IDS()                                                                               \
  int tid = tid0, w = w0, n = n0;                                                                 \
  u32 tag = tag0, map = map0;                                                                     \
  asm volatile("" : "+v"(tid));                                                                   \
  asm volatile("" : "+s"(w), "+s"(n), "+s"(map), "+s"(tag));                                      \
  const int lane = tid & 63, r32 = lane & 31, hh = lane >> 5, ch = w >> 2;                        \
  const u32 lane16 = lane * 16;                                                                   \
  const int grp = map & 15, sl = map >> 4, Ta = min(grp, 10), ntile = grp < 9 ? 6 : grp < 11 ? 5 : 0; \
  const int ftw = sl * 4 + (w & 3);                                                               \
  const int nt = n & 7, tt = w >> 2, kq = w & 3, T2 = (n >> 3) + 32 * tt;                         \
  (void)r32; (void)hh; (void)ch; (void)lane16; (void)ftw; (void)nt; (void)kq; (void)T2; (void)Ta; (void)ntile

  u32 tag0, dsk2;
  float ks0, ks1;
  float base[2][4];
  u32 kb, ub;
  auto normalize = [&](int tid, const u32x4& xr, const float (&mean)[4], const float (&rstd)[4]) {
    const f32x2 d = {bflo(dsk2), bfhi(dsk2)};
    f32x2 un[4];
#pragma unroll
    for (int i = 0; i < 4; ++i) {
      const f32x2 x = {bflo(xr[i]), bfhi(xr[i])};
      un[i] = (x - mean[i]) * rstd[i];
      const f32x2 t = __builtin_elementwise_fma(un[i], d, x);
      base[0][i] = t[0]; base[1][i] = t[1];
    }
    lds_st<u32x2>(L_U + 480 + tid * 8, (u32x2){pkf16(un[0][0], un[1][0]), pkf16(un[2][0], un[3][0])});
    lds_st<u32x2>(L_U + U_CH + 480 + tid * 8, (u32x2){pkf16(un[0][1], un[1][1]), pkf16(un[2][1], un[3][1])});
  };
  auto conv_prep = [&](int tid, int w, u32 (&kix)[9]) {
    const int i = tid & 255, ch = w >> 2;
    const int sh = min(__builtin_clz((u32)(255 - i) | 16) - 24, 3);
    const float sc = __uint_as_float((124 + sh) << 23);
    float l0 = (float)(2047 - 8 * i);
    asm volatile("" : "+v"(l0));
    const float mg = 8388608.f + (float)(384 - 128 * sh);
#pragma unroll
    for (int j = 0; j < 9; ++j) kix[j] = (__float_as_uint(fmaf(l0 - (float)j, sc, mg)) << 1) + (u32)(L_TH + ch * 1312 - 0x96000000u);
    asm volatile("" : "+v"(kix[8]));
    kix[8] -= i == 223 ? 2 : 0;
    kix[8] = i == 255 ? L_TH + 2624 : kix[8];
#pragma unroll
    for (int j = 0; j < 9; ++j) asm volatile("" : "+v"(kix[j]));
    const int r = tid & 15, h = (tid >> 4) & 3;
    kb = L_K + ((w >> 1) & 1) * KCH_B + 4 * (((2031 - r + 8 * h) >> 1) + ((r & 1) ? 0 : KCOPY / 2)) - 256 * (w & 1) - 64 * 11;
    ub = L_U + ((w >> 1) & 1) * U_CH + 2 * (224 + 16 * r + 8 * h) - 256 * (w & 1) - 192;
    asm volatile("" : "+v"(kb), "+v"(ub));
  };
  auto expand = [&](int tid, int ch, const u32 (&kix)[9]) {
    u32 z = 0;
    asm volatile("" : "+v"(z));
    if (tid < 76)
      lds_st<u32x4>(tid < 60 ? L_U + (tid >= 30 ? U_CH + (tid - 30) * 16 : tid * 16) : L_K + ((tid - 60) >> 2) * KCOPY * 2 + 4096 + (tid & 3) * 16,
                    (u32x4){z, z, z, z});
    const int i = tid & 255;
    u32 kv[9];
#pragma unroll
    for (int j = 0; j < 9; ++j) kv[j] = lds_ld<u16>(kix[j]);
    lds_st<u32x4>(L_K + ch * KCH_B + i * 16,
                  (u32x4){kv[0] | (kv[1] << 16), kv[2] | (kv[3] << 16), kv[4] | (kv[5] << 16), kv[6] | (kv[7] << 16)});
    lds_st<u32x4>(L_K + ch * KCH_B + i * 16 + KCOPY * 2,
                  (u32x4){kv[1] | (kv[2] << 16), kv[3] | (kv[4] << 16), kv[5] | (kv[6] << 16), kv[7] | (kv[8] << 16)});
  };
  bf16x8 W[32];
  rsrc_t wrs;
  u32 wvo, ra0;
  auto w_put = [&](rsrc_t src, u32 vo, int mat, int r, int j) {
    asm volatile("s_mov_b32 m0, %0\n\ts_nop 0\n\tbuffer_load_dwordx4 %1, %2, %3 offen lds"
                 :: "s"(L_F + j * 8192 + (w0 & 2) * 2048 + mat * 2048 + (w0 & 1) * 1024), "v"(vo), "s"(src),
                    "s"(((j >> 1) * 8 + 4 * r + 2 * (j & 1)) * (16 * DF * 2)) : "memory");
  };
  auto w_get = [&](int r) {
#pragma unroll
    for (int g = 0; g < 4; ++g) {
      u32 a = ra0 + ((g + w0) & 3) * 16384;
      asm volatile("" : "+v"(a));
#pragma unroll
      for (int j = 0; j < 4; ++j) {
        const s16x4 lo = trread(a + j * 4096), hi = trread(a + j * 4096 + 512);
        W[g * 8 + 4 * r + j] = __builtin_bit_cast(bf16x8, __builtin_shufflevector(lo, hi, 0, 1, 2, 3, 4, 5, 6, 7));
      }
    }
  };
  {
    const int tid = tid0, w = w0, n = n0, lane = tid & 63, r32 = lane & 31, hh = lane >> 5, ch = w >> 2, kq = w & 3;
    const u32 lane16 = lane * 16;
    const int pidx = n * 2 * NM + w * 32 + r32;
    const u16 pla = logA[pidx], pai = Aim[pidx], pcr = Cre[pidx], pci = Cim[pidx];
    const u32 car = *(const u32*)(Cre + (n * 2 + ch) * NM + lane * 2), cai = *(const u32*)(Cim + (n * 2 + ch) * NM + lane * 2);
    const u32x4 til = *(const u32x4*)(u + (size_t)((b >> 2) * 32 + r32) * SD + (b & 3) * 128 + (w * 2 + hh) * 8);
    const u32x4 row = *(const u32x4*)(u + (size_t)(4 * (8 * (b >> 2) + w) + (b & 3)) * SD + lane * 8);
    const u32 ep = ld4(rs, O_EP, 0);
    int vz = 0;
    asm volatile("" : "+v"(vz));
    const u16 ldt = logdt[2 * n + ch + vz];
    const u32 dskv = ((const u32*)dsk)[n + vz];
    SCHED();
    __builtin_amdgcn_s_barrier();
    SCHED();
    st16(rs, O_XT + b * 8192 + tid * 16, (u32x4){EMPTY, EMPTY, EMPTY, EMPTY});
    asm volatile("" ::: "memory");
    const u32 pf2 = __builtin_amdgcn_raw_buffer_load_b32(
        __builtin_amdgcn_make_buffer_rsrc((void*)(w < 2 ? wg : w < 4 ? wu : wd), 0, w < 6 ? SD * DF * 2 : 0, 0x00020000),
        lane < 43 ? lane * 64 : OOB, (b * 2 + (w & 1)) * 2752, 0);
    {
      const int pe = (n >> 6) * 32 + (n & 31), grp = pe / 11, sl = pe - grp * 11 + ((n >> 5) & 1) * 11;
      map0 = grp | (sl << 4);
      asm volatile("" : "+s"(map0));
      wrs = __builtin_amdgcn_make_buffer_rsrc((void*)(ch ? wu : wg), 0, SD * DF * 2, 0x00020000);
      wvo = (kq * 8 + (lane >> 3)) * (DF * 2) + sl * 128 + ((((lane & 7) >> 1) ^ (((lane >> 3) & 2) + ch)) * 32) + (lane & 1) * 16;
      const int mat = (lane >> 4) & 1, rq = (lane & 15) >> 2;
      ra0 = L_F + mat * 2048 + (8 * hh + rq) * 128 + ((kq ^ ((rq & 2) + mat)) * 32) + (lane & 3) * 8;
      asm volatile("" : "+v"(ra0), "+v"(wvo));
    }
    u32 kix[9];
    conv_prep(tid, w, kix);
    const bool odd = lane & 1;
    u32 xgo = O_XG + ((b & 3) * 64 + (w * 2 + hh) * 4 + (odd ? 2 : 0)) * 16384 + ((r32 >> 1) & 1) * 8192 + ((b >> 2) * 8 + (r32 >> 2)) * 16;
    const u32 tb0 = L_TB + w * TB_W + r32 * 8;
    u32 tb = tb0 + hh * 13 * TBP, tbe = tb0 + (hh ? 18 : 12) * TBP, tbf = tb0 + (hh ? 19 : 5) * TBP, tbg = tb0 + (hh ? 20 : 25) * TBP,
        tb1 = tb0 + (hh ? 17 : 0) * TBP;
    float pa = hh ? 4.f : 1.f, pb = hh ? 64.f : 256.f, pe = hh ? 16.f : 2044.f, sg = hh ? -1.f : 1.f, sc = hh ? 16384.f : 1.f;
    u32 aL = L_TB + w * TB_W + (lane >> 4) * 32 + (lane & 3) * TBP, aM = L_TB + w * TB_W + (lane >> 4) * 32 + ((lane & 15) >> 2) * TBP;
    u32 a2x = (lane & 15) == 4 ? aL + 4 * TBP : aL;
    asm volatile("" : "+v"(xgo), "+v"(tb), "+v"(tbe), "+v"(tbf), "+v"(tbg), "+v"(tb1), "+v"(pa), "+v"(pb), "+v"(pe), "+v"(sg), "+v"(sc));
    asm volatile("" : "+v"(aL), "+v"(aM), "+v"(a2x));
    lds_st<f32x2>(tb0 + 13 * TBP, (f32x2){16384.f, 0.f});
    if (tid == 0) lds_st<u32x2>(L_TH + 2624, (u32x2){0, 0});
    SCHED();
    const u32 tag = tag0 = __builtin_amdgcn_readfirstlane(ep) + 1u;
    STAMP(0, 1);
    {
      const u32 r0 = dppu<0xB1>(odd ? til[0] : til[2]), r1 = dppu<0xB1>(odd ? til[1] : til[3]);
      st16(rs, xgo, (u32x4){odd ? r0 : til[0], tag, odd ? til[2] : r0, tag});
      st16(rs, xgo + 16384, (u32x4){odd ? r1 : til[1], tag, odd ? til[3] : r1, tag});
      float s1 = 0.f, s2 = 0.f;
#pragma unroll
      for (int c = 0; c < 4; ++c) { s1 = dot2(row[c], 0x3f803f80u, s1); s2 = dot2(row[c], row[c], s2); }
      float s = sw32(s1, s2);
      s += dppf<0xB1>(s); s += dppf<0x4E>(s); s += dppf<0x141>(s); s += dppf<0x140>(s);
      s = sw16(s);
      if (r32 == 0) lds_st<float>(L_RS + w * 8 + hh * 4, s);
    }
    STAMP(0, 2);
    {
      const float dt = rbf(__expf(b2f(ldt)));
      const float ar = -rbf(__expf(b2f(pla))) * dt * 1.44269504088896f, ai = (b2f(pai) * dt) * 0.159154943091895f, aj = ai * sg;
      const int ce = __builtin_amdgcn_frexp_expf(wmax(fmaxf(fmaxf(fabsf(bflo(car)), fabsf(bfhi(car))), fmaxf(fabsf(bflo(cai)), fabsf(bfhi(cai))))));
      if (lane == 0) lds_st<float>(L_RS + 64 + ch * 4, ldexpf(dt, ce - 7));
      const f32x2 a = cexp(ar, ai, pa), bb = cexp(ar, aj, pb);
      const f32x2 e = cexp(ar, aj, pe), e1k = cexp(ar, ai, 1024.f);
      const f32x2 d = hh ? (f32x2){ldexpf(b2f(pcr), 14 - ce), ldexpf(-b2f(pci), 14 - ce)} : e1k;
      const f32x2 as = a * sc, a2 = cmul(as, a), b2 = cmul(bb, bb), b3 = cmul(b2, bb);
      const f32x2 f = cmul(hh ? e : bb, hh ? e : a2);
      lds_st<f32x2>(tb + 1 * TBP, as);
      lds_st<f32x2>(tb + 2 * TBP, a2);
      lds_st<f32x2>(tb + 3 * TBP, cmul(a2, a));
      lds_st<f32x2>(tb + 4 * TBP, bb);
      lds_st<f32x2>(tb + 6 * TBP, b2);
      lds_st<f32x2>(tb + 7 * TBP, b3);
      lds_st<f32x2>(tb + 8 * TBP, d);
      lds_st<f32x2>(tb + 9 * TBP, cmul(d, bb));
      lds_st<f32x2>(tb + 10 * TBP, cmul(d, b2));
      lds_st<f32x2>(tb + 11 * TBP, cmul(d, b3));
      asm volatile("" ::: "memory");
      lds_st<f32x2>(tbe, e);
      lds_st<f32x2>(tbf, f);
      lds_st<f32x2>(tbg, cmul(f, e));
      lds_st<f32x2>(tb1, (f32x2){1.f, 0.f});
    }
    asm volatile("" :: "v"(pf));
    dsk2 = __builtin_amdgcn_readfirstlane(dskv);
    asm volatile("" :: "v"(dskv));
    WAITV(3);
    BAR();
    if (w == 3) {
      const f32x2 v = lds_ld<f32x2>(L_RS + (lane & 7) * 8) * (1.f / SD);
      const float r = __builtin_amdgcn_rsqf(fmaxf(fmaf(-v[0], v[0], v[1]), 0.f) + 1e-5f);
      st16(rs, lane < 8 ? O_G0 + (b & 3) * 8192 + ((b >> 2) * 8 + lane) * 16 : OOB, (u32x4){__float_as_uint(v[0]), tag, __float_as_uint(r), tag});
      st16(rs, lane < 8 ? O_S2 + b * 128 + lane16 : OOB, (u32x4){0, 0, 0, 0});
    }
    STAMP(0, 3);
    u32x4 g[6];
    auto gran_loads = [&]() {
#pragma unroll
      for (int i = 0; i < 2; ++i) g[4 + i] = ld16(rs, tid * 16, O_XG + n * 16384 + i * 8192);
#pragma unroll
      for (int i = 0; i < 4; ++i) g[i] = ld16(rs, tid * 16, O_G0 + i * 8192);
    };
    {
      f32x4 a0 = {0, 0, 0, 0}, a1 = {0, 0, 0, 0}, a2 = {0, 0, 0, 0};
      f32x4 tv[2][12];
#pragma unroll
      for (int s = 0; s < 2; ++s) {
        auto L = [&](int i, u32 a) { tv[s][i] = lds_ld<f32x4>(a + s * 128); tv[s][i + 1] = lds_ld<f32x4>(a + s * 128 + 16); };
        L(0, aL + 17 * TBP); L(2, aM + 21 * TBP); L(4, aM + 13 * TBP); L(6, aL); L(8, aL + 4 * TBP); L(10, a2x + 8 * TBP);
      }
#pragma unroll
      for (int s = 0; s < 2; ++s) {
        const f32x4* t = tv[s];
        SCHED(); w_put(wrs, wvo, ch, 0, 4 * s + 0); SCHED();
        const f16x8 bb = cmul4(t[0], t[1], t[2], t[3]);
        SCHED(); w_put(wrs, wvo, ch, 0, 4 * s + 1); SCHED();
        a0 = __builtin_amdgcn_mfma_f32_16x16x32_f16(cmul4(t[6], t[7], t[4], t[5]), bb, a0, 0, 0, 0);
        SCHED(); w_put(wrs, wvo, ch, 0, 4 * s + 2); SCHED();
        a1 = __builtin_amdgcn_mfma_f32_16x16x32_f16(cmul4(t[8], t[9], t[4], t[5]), bb, a1, 0, 0, 0);
        SCHED(); w_put(wrs, wvo, ch, 0, 4 * s + 3); SCHED();
        a2 = __builtin_amdgcn_mfma_f32_16x16x32_f16(cmul4(t[10], t[11], t[4], t[5]), bb, a2, 0, 0, 0);
      }
      const u32 T = L_TB + w * TB_W + lane16;
      lds_st<f32x4>(T, a0);
      lds_st<f32x4>(T + 1024, a1);
      lds_st<f32x4>(T + 2048, a2);
    }
    if (GRAN_AT == 1) gran_loads();
    STAMP(0, 4);
    BAR();
    if (GRAN_AT == 2) gran_loads();
    if (kq < 3) {
      const u32 T = L_TB + ch * 4 * TB_W + kq * 1024 + lane16;
      const f32x4 v = ((lds_ld<f32x4>(T) + lds_ld<f32x4>(T + TB_W)) + (lds_ld<f32x4>(T + 2 * TB_W) + lds_ld<f32x4>(T + 3 * TB_W))) * 9.5367431640625e-07f;
      const u32 p0 = pkf16(v[0], v[1]), p1 = pkf16(v[2], v[3]);
      const int j = lane & 15, h = lane >> 4;
      const u32 H = L_TH + ch * 1312;
      if (kq == 0) {
        lds_st<u32x2>(H + 2 * (16 * j + 4 * h), (u32x2){p0, p1});
      } else if (kq == 1) {
        lds_st<u32>(H + 2 * (256 + 8 * j + 2 * h), p0);
        lds_st<u16>(H + 2 * (384 + 4 * j + h), (u16)p1);
        lds_st<u16>(H + 2 * (448 + 4 * j + h), (u16)(p1 >> 16));
      } else if (!(h & 1)) {
        const u32 a = H + 2 * (512 + 2 * j + (h >> 1));
        lds_st<u16>(a, (u16)p0);
        lds_st<u16>(a + 64, (u16)(p0 >> 16));
        lds_st<u16>(a + 128, (u16)p1);
        lds_st<u16>(a + 192, (u16)(p1 >> 16));
      } else if (lane == 16) {
        lds_st<u16>(H + 2 * 640, (u16)p0);
      }
    }
    {
      const f32x2 k2 = lds_ld<f32x2>(L_RS + 64);
      ks0 = __uint_as_float(__builtin_amdgcn_readfirstlane(__float_as_uint(k2[0])));
      ks1 = __uint_as_float(__builtin_amdgcn_readfirstlane(__float_as_uint(k2[1])));
    }
    BAR();
    STAMP(0, 5);
#pragma unroll
    for (int k = 0; k < CLR_M; ++k) st16(rs, O_H + b * 30208 + k * 8192 + tid * 16, (u32x4){EMPTY, EMPTY, EMPTY, EMPTY});
    expand(tid, ch, kix);
    asm volatile("" : "+v"(g[0]), "+v"(g[1]), "+v"(g[2]), "+v"(g[3]), "+v"(g[4]), "+v"(g[5]) :: "memory");
    asm volatile("" :: "v"(pf2));
    STAMP(0, 6);
    auto gran_ok = [&]() {
      u32 bad = 0;
#pragma unroll
      for (int k = 0; k < 6; ++k) bad |= (g[k][1] ^ tag) | (g[k][3] ^ tag);
      return none(bad != 0);
    };
    if (!gran_ok()) {
#pragma unroll 1
      for (int s = 0; s < SPIN_MAX; ++s) {
        asm volatile("" ::: "memory");
        gran_loads();
        if (gran_ok()) break;
      }
    }
    {
      float mean[4], rstd[4];
#pragma unroll
      for (int k = 0; k < 4; ++k) { mean[k] = __uint_as_float(g[k][0]); rstd[k] = __uint_as_float(g[k][2]); }
      normalize(tid, (u32x4){g[4][0], g[4][2], g[5][0], g[5][2]}, mean, rstd);
    }
    STAMP(0, 7);
  }

  auto conv = [&](auto mode) {
    constexpr int layer = decltype(mode)::V;
    BAR();
    if (layer == 0) {
      w_get(0);
      BAR();
    }
    STAMP(1 + 3 * layer, 0);
    if (w0 < 4) {
      constexpr int NA = 6;
      f32x4 acc[8];
#pragma unroll
      for (int g = 0; g < 8; ++g) acc[g] = (f32x4){0, 0, 0, 0};
      u32 kp[4];
#pragma unroll
      for (int p = 0; p < 4; ++p) {
        kp[p] = kb - 1024 * p;
        asm volatile("" : "+v"(kp[p]));
      }
      u32x4 A[NA];
      f16x8 B[8];
      auto ldA = [&](auto st_, auto half_) {
        constexpr int st = decltype(st_)::V, half = decltype(half_)::V, a = cv_lag(st);
        if constexpr (st < 32) {
          const u32 p = kp[a >> 1] + 64 * (11 - (8 * (a & 1) + (st >> 3))) + half * 8;
          A[st % NA][2 * half] = lds_ld<u32>(p);
          A[st % NA][2 * half + 1] = lds_ld<u32>(p + 4);
        }
      };
      auto ldB = [&](int rr, int q) { return lds_ld<f16x8>(ub + 64 * (8 * q + 3 - rr)); };
      ldA(Idx<0>{}, Idx<0>{});
      ldA(Idx<0>{}, Idx<1>{});
#pragma unroll
      for (int q = 7; q >= 0; --q) B[q] = ldB(0, q);
      ldA(Idx<1>{}, Idx<0>{});
      ldA(Idx<1>{}, Idx<1>{});
      u32 yb = tid0;
      asm volatile("" : "+v"(yb));
      yb = L_Y + w0 * 8192 + ((yb & 48) + ((yb & 15) ^ ((yb >> 2) & 12))) * 16;
      asm volatile("" : "+v"(yb));
      SCHED();
      sfor<0, 32>([&](auto st_) {
        constexpr int st = decltype(st_)::V, a = cv_lag(st), rr = st >> 3;
        sfor<0, 8 - a>([&](auto j_) {
          constexpr int j = decltype(j_)::V, q = 7 - a - j;
          acc[a + q] = __builtin_amdgcn_mfma_f32_16x16x32_f16(__builtin_bit_cast(f16x8, A[st % NA]), B[q], acc[a + q], 0, 0, 0);
          SCHED();
          if constexpr (st == 0) ldA(Idx<2 + (j >> 1)>{}, Idx<(j & 1)>{});
          if constexpr ((st & 7) == 7 && rr < 3) B[q] = ldB(rr + 1, q);
          if constexpr (!(st & 1) && st < 24 && j == 0) B[7 - a] = ldB(rr + 1, 7 - a);
          if constexpr (!(st & 1) && st >= 2 && st <= 24 && j >= 1 && j <= 4) ldA(Idx<st - 2 + ((j - 1) >> 1) + NA>{}, Idx<((j - 1) & 1)>{});
          if constexpr (st > 24 && j < 2) ldA(Idx<st - 1 + NA>{}, Idx<(j & 1)>{});
          if constexpr (st > 24 && j == (7 - a >= 2 ? 2 : 7 - a)) lds_st<f32x4>(yb + (a - 1) * 1024, acc[a - 1]);
          SCHED();
        });
      });
      lds_st<f32x4>(yb + 7 * 1024, acc[7]);
      if (layer == 0) WAITV(0);
    } else {
      u32 ce = EMPTY, t8 = tid0;
      asm volatile("" : "+v"(ce), "+v"(t8));
      t8 = (t8 & 255) * 16;
      if (layer == 0) {
        const rsrc_t wr0 = __builtin_amdgcn_make_buffer_rsrc((void*)wg, 0, SD * DF * 2, 0x00020000);
        const u32 wv0 = wvo ^ 32;
        const int ft = b >> 1, nb = (b & 1) * 4 + (w0 & 3);
        const rsrc_t wr = __builtin_amdgcn_make_buffer_rsrc((void*)wd, 0, SD * DF * 2, 0x00020000);
        u32 wt[8];
#pragma unroll
        for (int j = 0; j < 8; ++j)
          wt[j] = __builtin_amdgcn_raw_buffer_load_b32(wr, (t8 & 512) * 8 + ((t8 >> 2) & 124) + (j & 3) * (SD * 2),
                                                       (ft * 16 + 8 * (j >> 2)) * (SD * 2) + nb * 128, 0);
#pragma unroll
        for (int j = 2 * CLR_M; j < 4; ++j) st16(rs, O_H + b * 30208 + j * 4096 + t8, (u32x4){ce, ce, ce, ce});
        u32x4 lo, hi;
#pragma unroll
        for (int j = 0; j < 8; ++j) {
          if (j == WT_AT) {
#pragma unroll
            for (int i = 0; i < 4; ++i) {
              lo[i] = __builtin_amdgcn_perm(wt[2 * i + 1], wt[2 * i], 0x05040100u);
              hi[i] = __builtin_amdgcn_perm(wt[2 * i + 1], wt[2 * i], 0x07060302u);
            }
            const u32 wo = (ft < FT ? O_W2F + (nb * FT + ft) * 2048 : OOB) + (t8 & 1023);
            st16(rs, wo, lo);
            st16(rs, wo + 1024, hi);
          }
          w_put(wr0, wv0, 0, 1, j);
          w_put(wrs, wvo, 1, 1, j);
          if (j & 1) st16(rs, (j < 7 || t8 < 1536 ? O_H + b * 30208 + (4 + (j >> 1)) * 4096 : OOB) + t8, (u32x4){ce, ce, ce, ce});
        }
      } else {
#pragma unroll
        for (int j = 0; j < 6; ++j) st16(rs, (j < 5 || t8 < 1536 ? O_H + LAY_SZ + b * 22016 + j * 4096 : OOB) + t8, (u32x4){ce, ce, ce, ce});
        int ws_ = w0 & 3, nn = n0 & 7;
        asm volatile("" : "+s"(ws_), "+s"(nn));
#pragma unroll
        for (int j = 0; j < 14; ++j) dmak(j, rs, O_W2F + (nn * FT + 12) * 2048 + ws_ * 14336, L_R3 + ws_ * 14336, t8 & 1023);
      }
      if (layer == 0) WAITV(0); else WAITV(14);
    }
    STAMP(1 + 3 * layer, 1);
    {
      PHASE_IDS();
      const int c = lane >> 2, h = lane & 3;
      u32 y = L_Y + (64 * w + 16 * h + (c ^ (4 * h))) * 16;
      u32 xo = O_XT + layer * LAY_SZ + n * 128 + (tid >> 3) * 32768 + (tid & 1) * 64 + ((tid >> 1) & 3) * 16;
      asm volatile("" : "+v"(y), "+v"(xo));
      BAR();
      STAMP(1 + 3 * layer, 2);
      const f32x4 y0 = lds_ld<f32x4>(y) + lds_ld<f32x4>(y + 8192), y1 = lds_ld<f32x4>(y + 16384) + lds_ld<f32x4>(y + 24576);
      const u32 a0 = pkbf(fmaf(y0[0], ks0, base[0][0]), fmaf(y0[1], ks0, base[0][1]));
      const u32 a1 = pkbf(fmaf(y0[2], ks0, base[0][2]), fmaf(y0[3], ks0, base[0][3]));
      const u32 c0 = pkbf(fmaf(y1[0], ks1, base[1][0]), fmaf(y1[1], ks1, base[1][1]));
      const u32 c1 = pkbf(fmaf(y1[2], ks1, base[1][2]), fmaf(y1[3], ks1, base[1][3]));
      const bool odd = lane & 1;
      const u32 r0 = dppu<0xB1>(odd ? a0 : c0), r1 = dppu<0xB1>(odd ? a1 : c1);
      st16(rs, xo, notempty((u32x4){odd ? r0 : a0, odd ? r1 : a1, odd ? c0 : r0, odd ? c1 : r1}));
      STAMP(1 + 3 * layer, 3);
      if (layer == 0) {
        w_get(1);
      }
    }
  };
  conv(Mode<0>{});
#pragma unroll 1
  for (int layer = 0; layer < 2; ++layer) {
    const u32 xt_off = O_XT + layer * LAY_SZ, h_off = O_H + layer * LAY_SZ;
    f32x4 e4, o4;
    auto ffn = [&](auto role) {
      constexpr bool A = decltype(role)::A;
      PHASE_IDS();
      auto hof = [&](int p) { return h_off + (T2 * FT + min(kq + 4 * p, FT - 1)) * 1024; };
      const u32 w2s = O_W2F + (nt * FT + 12) * 2048;
      u32 xq[4];
#pragma unroll
      for (int g = 0; g < 4; ++g) xq[g] = hh * 512 + ((lane & 15) >> 2) * 64 + ((lane >> 4) & 1) * 32 + (lane & 3) * 8 + ((g + kq) & 3) * 8192;
      bf16x8 fr[PD];
      u32 csr = L_CS + kq * 128 + hh * 64, str = L_ST + lane16;
      u32 hs = (ftw < FT && ntile > 0 ? h_off + ((A ? Ta : 11 + Ta) * FT + ftw) * 1024 : OOB) + lane16;
      u32 rot16 = (((r32 + 8 * kq) & 31) + 32 * hh) * 16, r16 = r32 * 16, stw = str + kq * 4;
      asm volatile("" : "+v"(r16), "+v"(xq[0]), "+v"(xq[1]), "+v"(xq[2]), "+v"(xq[3]), "+v"(csr), "+v"(str), "+v"(rot16), "+v"(hs), "+v"(stw));
      u32 xs = xt_off + Ta * 32768 + kq * 8192;
      asm volatile("" : "+s"(xs));
      constexpr u32 XS = 11 * 32768;
      const int Tj = 11 * (1 + (sl >> 2)) + Ta;
      const bool job = sl < 4 * (ntile - 1);
      u32 ja = job ? lane * 64 : OOB;
      const u32 js = xs + (1 + (sl >> 2)) * XS + (sl & 3) * 16;
      u32x4 jv[2];
      {
        u32x4 xa[4];
        const u32 xs0 = xt_off + Ta * 32768, t16 = tid * 16;
        if (layer == 1) delay(X_DELAY);
        BAR();
        STAMP(1 + 3 * layer, 4);
        xload<4>(rs, xa, t16, xs0, 8192);
        if (!A) xload<2>(rs, jv, ja, js, 4096);
        if (layer == 0) {
          float c0 = 0.f, c1 = 0.f;
          u32 one2 = 0x3f803f80u;
          asm volatile("" : "+s"(one2));
#pragma unroll
          for (int i = 0; i < 16; ++i) {
            const u32x4 v = __builtin_bit_cast(u32x4, W[A ? i : 16 + i]);
            c0 = dot2(v[0], one2, c0); c1 = dot2(v[1], one2, c1);
            c0 = dot2(v[2], one2, c0); c1 = dot2(v[3], one2, c1);
          }
          c0 += c1;
          lds_st<float>(L_CS + w * 128 + ((r32 >> 2) & 1) * 64 + (r32 >> 3) * 16 + (r32 & 3) * 4, sw32(c0, c0));
        }
        xpark<4>(rs, xa, t16, xs0, 8192, t16);
        STAMP(1 + 3 * layer, 5);
      }
      BAR();
      STAMP(1 + 3 * layer, 6);
      bf16x8 Wr[3][2];
      u32x4 fh[NHW];
      auto slot = [](int p) { return p % NHW; };
      auto hcheck = [&](int p0, int p1) {
        u32 m = 0;
#pragma unroll
        for (int p = p0; p < p1; ++p) m = umax4(m, fh[slot(p)]);
        if (__builtin_expect(!none(m == EMPTY), 0)) {
#pragma unroll 1
          for (int s = 0; s < SPIN_MAX; ++s) {
            asm volatile("" ::: "memory");
            m = 0;
#pragma unroll
            for (int p = p0; p < (p0 + NHW < 22 ? p0 + NHW : 22); ++p)
              if (!none(umax4(0, fh[slot(p)]) == EMPTY)) fh[slot(p)] = ld16(rs, lane16, hof(p));
#pragma unroll
            for (int p = p0; p < p1; ++p) m = umax4(m, fh[slot(p)]);
            RETRY(p0);
            if (none(m == EMPTY)) break;
          }
        }
      };
      {
        f32x16 acc;
        auto nop = []() {};
        const u32 g1_off = O_G1 + layer * 32768;
        u32x4 gs;
        auto recheck = [&](int k, int toff, auto redo) __attribute__((always_inline)) {
          if (__builtin_expect(!none(acc[0] != acc[0]), 0)) {
#pragma unroll 1
            for (int j = 0; j < 32; ++j) {
              u32x4 v[1];
              const u32 so = xt_off + (11 * k + Ta) * 32768 + j * 1024;
              xload<1>(rs, v, lane16, so, 0);
              xpark<1>(rs, v, lane16, so, 0, (k % 3) * 32768 + j * 1024 + lane16);
            }
            WAITL0();
            g1_pre(fr, xq, toff);
            g1_chain<0>(acc, W, fr, xq, toff, 0, [](int) {});
            redo();
          }
        };
        auto ldst = [&](int k) { gs = ld16(rs, r16, g1_off + (11 * k + Ta) * 512); };
        auto epi = [&](int k, auto mid) {
          asm volatile("" : "+v"(gs));
          if (!none(((gs[1] ^ tag) | (gs[3] ^ tag)) != 0)) {
#pragma unroll 1
            for (int s = 0; s < SPIN_MAX; ++s) {
              asm volatile("" ::: "memory");
              ldst(k);
              if (none(((gs[1] ^ tag) | (gs[3] ^ tag)) != 0)) break;
            }
          }
          st16(rs, hs + (k >> 1) * (22 * FT * 1024), g1_epi<false>(acc, __uint_as_float(gs[0]), __uint_as_float(gs[2]), csr, mid));
        };
        auto ldWr = [&](int k) {
          Wr[k >> 1][k & 1] = __builtin_bit_cast(bf16x8, ld16(rs, rot16 + (k & 1) * 1024, O_W2F + (nt * FT + kq + 4 * (k >> 1)) * 2048));
        };
        auto shift = [&]() {
#pragma unroll
          for (int g = 0; g < 4; ++g) { xq[g] += 32768; asm volatile("" : "+v"(xq[g])); }
        };
        if constexpr (A) {
          g1_pre(fr, xq, 0);
          g1_chain<8>(acc, W, fr, xq, 0, stw, [&](int k) {
            if (k >= 2 && k < 10) dmak(k - 2, rs, xs + 2 * XS, 65536 + kq * 8192, lane16);
            else if (k >= 10 && layer == 0) dmak(k - 2, rs, w2s + kq * 14336, L_R3 + kq * 14336, lane16);
          });
          BAR();
          STAMP(2 + 3 * layer, 0);
          {
            const f32x4 p = lds_ld<f32x4>(str);
            float s = p[0] + p[1], q = p[2] + p[3];
            asm volatile("" : "+v"(s), "+v"(q));
            s = (s + q) * (1.f / SD);
            const auto sw = __builtin_amdgcn_permlane32_swap(__float_as_uint(s), __float_as_uint(s), false, false);
            const float m = __uint_as_float(sw[0]);
            st16(rs, hs, g1_epi<false>(acc, -m, fmaxf(fmaf(-m, m, __uint_as_float(sw[1])), 0.f) + 1e-5f, csr, nop));
          }
          shift();
          WAITV(1);
          g1_pre(fr, xq, 32768);
          BAR();
          STAMP(2 + 3 * layer, 1);
          g1_chain<0>(acc, W, fr, xq, 32768, 0, [&](int k) {
            if (k < 8) dmak(k, rs, xs + 4 * XS, 32768 + kq * 8192, lane16);
            else if (k == 8) ldst(2);
            else if (k < 15) ldWr(k - 9);
          });
          recheck(2, 32768, nop);
          BAR();
          STAMP(2 + 3 * layer, 2);
          epi(2, nop);
          g1_pre(fr, xq, 0);
          BAR();
          STAMP(2 + 3 * layer, 3);
          auto side = [&](int k) {
            if (k < 8) dmak(k, rs, w2s + 57344 + kq * 8192, kq * 8192, lane16);
            else if (k == 8) ldst(4);
            else fh[k - 9] = ld16(rs, lane16, hof(k - 9));
          };
          g1_chain<0>(acc, W, fr, xq, 0, 0, side);
          recheck(4, 0, [&]() {
#pragma unroll
            for (int k = 8; k < 16; ++k) side(k);
          });
          BAR();
          STAMP(2 + 3 * layer, 4);
          epi(4, [&]() {
            SCHED();
#pragma unroll
            for (int p = 7; p < 10; ++p) fh[p] = ld16(rs, lane16, hof(p));
            SCHED();
          });
#pragma unroll
          for (int p = 10; p < NHW; ++p) fh[p] = ld16(rs, lane16, hof(p));
          hcheck(0, 7);
          STAMP(2 + 3 * layer, 5);
          BAR();
        } else {
          if (!xfull<2>(jv)) xretry<2>(rs, jv, ja, js, 4096);
#pragma unroll
          for (int k = 0; k < 8; ++k) dmak(k, rs, xs + XS, 32768 + kq * 8192, lane16);
          if (job) {
            const u32x4 v0 = jv[0], v1 = jv[1];
            float r[8];
#pragma unroll
            for (int k = 0; k < 8; ++k) {
              float x = (k & 1) ? bfhi(v0[k >> 1]) : bflo(v0[k >> 1]), y = (k & 1) ? bfhi(v1[k >> 1]) : bflo(v1[k >> 1]);
              float s1 = x + y, s2 = x * x;
              asm volatile("" : "+v"(s1), "+v"(s2));
              s2 = fmaf(y, y, s2);
              r[k] = sw32(s1, s2);
              asm volatile("" : "+v"(r[k]));
            }
#pragma unroll
            for (int k = 0; k < 4; ++k) {
              const auto sw = __builtin_amdgcn_permlane16_swap(__float_as_uint(r[k]), __float_as_uint(r[4 + k]), false, false);
              float t = __uint_as_float(sw[0]) + __uint_as_float(sw[1]);
              t += dppf<0xB1>(t); t += dppf<0x4E>(t); t += dppf<0x141>(t); t += dppf<0x140>(t);
              r[k] = t;
            }
            const bool b0 = lane & 1, b1 = lane & 2;
            lds_st<float>(L_ST + 1024 + (lane >> 4) * 64 + (lane & 3) * 16 + kq * 4, b1 ? (b0 ? r[3] : r[2]) : (b0 ? r[1] : r[0]));
            WAITV(0);
            WAITL0();
            u32 cnt = 0;
            if (lane == 0) cnt = __hip_atomic_fetch_add((LDSP u32*)(size_t)(L_TH + 2628), 1u, __ATOMIC_RELAXED, __HIP_MEMORY_SCOPE_WORKGROUP);
            if ((__builtin_amdgcn_readfirstlane(cnt) & 3) == 3) {
              asm volatile("" ::: "memory");
              const f32x4 p = lds_ld<f32x4>(L_ST + 1024 + (lane & 15) * 16);
              float s = p[0] + p[1], q = p[2] + p[3];
              asm volatile("" : "+v"(s), "+v"(q));
              const float m = (s + q) * (1.f / SD);
              const float v = fmaxf(fmaf(-m, m, dppf<0x128>(m)), 0.f) + 1e-5f;
              st16(rs, lane < 8 ? g1_off + (Tj * 32 + (sl & 3) * 8 + lane) * 16 : OOB, (u32x4){__float_as_uint(-m), tag, __float_as_uint(v), tag});
            }
          }
          if (!job) WAITV(0);
          g1_pre(fr, xq, 32768);
          if (layer == 0) {
            const u32 a = L_CS + kq * 128 + r32 * 4;
            lds_st<float>(a, lds_ld<float>(a) + lds_ld<float>(a + 512));
            u32 e = EMPTY;
            asm volatile("" : "+v"(e));
            st16(rs, O_XT + LAY_SZ + b * 8192 + (tid & 255) * 16, (u32x4){e, e, e, e});
            st16(rs, O_XT + LAY_SZ + b * 8192 + 4096 + (tid & 255) * 16, (u32x4){e, e, e, e});
          }
          BAR();
          STAMP(2 + 3 * layer, 0);
          g1_chain<0>(acc, W, fr, xq, 32768, 0, [&](int k) {
            if (k < 8) dmak(k, rs, xs + 3 * XS, kq * 8192, lane16);
            else if (k == ST1_AT) ldst(1);
            if ((k == 3 || k == 7) && layer == 0) {
#pragma unroll
              for (int j = k - 3; j <= k; ++j) dmak(j, rs, w2s + kq * 14336, L_R3 + kq * 14336, lane16);
            }
          });
          recheck(1, 32768, nop);
          BAR();
          STAMP(2 + 3 * layer, 1);
          epi(1, nop);
          g1_pre(fr, xq, 0);
          if (layer == 0 && b == 0) st4(rs, tid == 256 ? O_EP : OOB, 0, tag);
          BAR();
          STAMP(2 + 3 * layer, 2);
          g1_chain<0>(acc, W, fr, xq, 0, 0, [&](int k) {
            if (k < 8) dmak(k, rs, xs + (ntile == 6 ? 5 : 4) * XS, 65536 + kq * 8192, lane16);
            else if (k == 8) ldst(3);
            else if (k < 15) ldWr(k - 9);
          });
          recheck(3, 0, nop);
          BAR();
          STAMP(2 + 3 * layer, 3);
          epi(3, nop);
          shift();
          g1_pre(fr, xq, 32768);
          BAR();
          STAMP(2 + 3 * layer, 4);
          ldst(ntile == 6 ? 5 : 3);
          u32 el = T2 < EARLY_T ? lane16 : OOB;
          asm volatile("" : "+v"(el), "=v"(fh[0]), "=v"(fh[1]), "=v"(fh[2]), "=v"(fh[3]), "=v"(fh[4]), "=v"(fh[5]), "=v"(fh[6]), "=v"(fh[7]));
          auto side = [&](int k) {
            if (k < 8) dmak(k, rs, w2s + 90112 + kq * 8192, 32768 + kq * 8192, lane16);
            else fh[k - 8] = ld16(rs, el, hof(k - 8));
          };
          if (ntile != 6) {
#pragma unroll
            for (int k = 0; k < 16; ++k) side(k);
            __builtin_amdgcn_s_waitcnt(0x0F70);
          } else {
            g1_chain<0>(acc, W, fr, xq, 32768, 0, side);
            recheck(5, 32768, [&]() {
#pragma unroll
              for (int k = 8; k < 16; ++k) side(k);
            });
          }
          WAITV(8);
          STAMP(2 + 3 * layer, 5);
          BAR();
          if (B_PRIO) __builtin_amdgcn_s_setprio(1);
          if (ntile != 6) hs = OOB;
          epi(5, nop);
          if (T2 >= EARLY_T) {
            if (T2 >= 55) delay(LATE_DELAY);
#pragma unroll
            for (int p = 0; p < 8; ++p) fh[p] = ld16(rs, lane16, hof(p));
          }
#pragma unroll
          for (int p = 8; p < NHW; ++p) fh[p] = ld16(rs, lane16, hof(p));
        }
      }
      STAMP(2 + 3 * layer, 6);
      f32x16 acc[2];
#pragma unroll
      for (int c = 0; c < 2; ++c)
#pragma unroll
        for (int e = 0; e < 16; ++e) acc[c][e] = 0.f;
      u32 wb0 = kq * 2048 + rot16, wb1 = wb0 + 65536, wb3 = wb0 + L_R3;
      asm volatile("" : "+v"(wb0), "+v"(wb1), "+v"(wb3));
      auto ldW = [&](int p, int c) {
        return lds_ld<bf16x8>((p < 10 ? wb3 + (p - 3) * 8192 : p < 18 ? wb0 + (p - 10) * 8192 : wb1 + (p - 18) * 8192) + c * 1024);
      };
      u16 rsd[8];
      const u32 rso = hh * 512 + r32 * 2, rss = xt_off + T2 * 32768 + (nt * 64 + kq * 16) * 64;
      SCHED();
#pragma unroll
      for (int p = 0; p < 22; ++p) {
        if (!A && p % 3 == 0 && p < 21) hcheck(p, p == 18 ? 22 : p + 3);
        const bf16x8 f = __builtin_bit_cast(bf16x8, fh[slot(p)]);
        if (p == 21 && kq >= 2) break;
        acc[0] = __builtin_amdgcn_mfma_f32_32x32x16_bf16(Wr[p % 3][0], f, acc[0], 0, 0, 0);
        SCHED();
        if (p == 15) { if constexpr (!A) BAR(); else WAITV(10); }
        if (p + 3 < 22) Wr[p % 3][0] = ldW(p + 3, 0);
        if (A && p < 8) dmak(p & 1, rs, w2s + 122880 + (p >> 1) * 8192 + kq * 2048, 65536 + (p >> 1) * 8192 + kq * 2048, lane16);
        if (p >= 11 && p < 15) rsd[2 * p - 22] = ld2(rs, rso + (2 * p - 22) * 64, rss);
        SCHED();
        acc[1] = __builtin_amdgcn_mfma_f32_32x32x16_bf16(Wr[p % 3][1], f, acc[1], 0, 0, 0);
        SCHED();
        if (p + 3 < 22) Wr[p % 3][1] = ldW(p + 3, 1);
        if (p + NHW < 22) fh[p] = ld16(rs, lane16, hof(p + NHW));
        if (p >= 11 && p < 15) rsd[2 * p - 21] = ld2(rs, rso + (2 * p - 21) * 64, rss);
        if (p == 8) {
          STAMP(3 + 3 * layer, 2);
          if (!A && B_PRIO) __builtin_amdgcn_s_setprio(0);
        }
        SCHED();
      }
      if (A && __builtin_expect(!none(acc[0][0] != acc[0][0]), 0)) {
#pragma unroll
        for (int e = 0; e < 16; ++e) acc[0][e] = acc[1][e] = 0.f;
#pragma unroll 1
        for (int ft = kq; ft < FT; ft += 4) {
          u32x4 v[1];
          const u32x4 wa = ld16(rs, rot16, O_W2F + (nt * FT + ft) * 2048), wb = ld16(rs, rot16 + 1024, O_W2F + (nt * FT + ft) * 2048);
          xload<1>(rs, v, lane16, h_off + (T2 * FT + ft) * 1024, 0);
          if (!xfull<1>(v)) xretry<1>(rs, v, lane16, h_off + (T2 * FT + ft) * 1024, 0);
          acc[0] = __builtin_amdgcn_mfma_f32_32x32x16_bf16(__builtin_bit_cast(bf16x8, wa), __builtin_bit_cast(bf16x8, v[0]), acc[0], 0, 0, 0);
          acc[1] = __builtin_amdgcn_mfma_f32_32x32x16_bf16(__builtin_bit_cast(bf16x8, wb), __builtin_bit_cast(bf16x8, v[0]), acc[1], 0, 0, 0);
        }
        RETRY(9);
      }
      STAMP(3 + 3 * layer, 3);
      if constexpr (A) BAR();
      const u32 pw = L_R3 + kq * 2048 + tt * 24576 + lane16;
#pragma unroll
      for (int q = 1; q < 4; ++q) {
        lds_st<f32x4>(pw + (q - 1) * 8192, (f32x4){acc[0][4 * q], acc[0][4 * q + 1], acc[0][4 * q + 2], acc[0][4 * q + 3]});
        lds_st<f32x4>(pw + (q - 1) * 8192 + 1024, (f32x4){acc[1][4 * q], acc[1][4 * q + 1], acc[1][4 * q + 2], acc[1][4 * q + 3]});
      }
#pragma unroll
      for (int e = 0; e < 4; ++e) { e4[e] = acc[0][e] + b2f(rsd[2 * e]); o4[e] = acc[1][e] + b2f(rsd[2 * e + 1]); }
      asm volatile("" : "+v"(e4), "+v"(o4));
    };
    asm volatile("" : "=v"(e4), "=v"(o4));
    if (w0 < 4) ffn(Role<true>{}); else ffn(Role<false>{});
    PHASE_IDS();
    {
      BAR();
      STAMP(3 + 3 * layer, 4);
#pragma unroll
      for (int q = 1; q < 4; ++q) {
        const u32 a = L_R3 + ((kq - q) & 3) * 2048 + tt * 24576 + lane16 + (q - 1) * 8192;
        e4 += lds_ld<f32x4>(a);
        o4 += lds_ld<f32x4>(a + 1024);
      }
      u32x4 o = {pkbf(e4[0], o4[0]), pkbf(e4[1], o4[1]), pkbf(e4[2], o4[2]), pkbf(e4[3], o4[3])};
      int last = layer;
      asm volatile("" : "+s"(last));
      if (last == 1) {
        *(u32x4*)(out + (size_t)(T2 * 32 + r32) * SD + nt * 64 + kq * 16 + hh * 8) = o;
        continue;
      }
      float s1 = 0.f, s2 = 0.f;
#pragma unroll
      for (int k = 0; k < 4; ++k) { s1 = dot2(o[k], 0x3f803f80u, s1); s2 = dot2(o[k], o[k], s2); }
      lds_st<float>(L_ST + tt * 1024 + lane16 + kq * 4, sw32(s1, s2));
      BAR();
      if (kq == 0) {
        const f32x4 p = lds_ld<f32x4>(L_ST + tt * 1024 + lane16);
        const float x = ((p[0] + p[1]) + (p[2] + p[3])) * (hh ? 0.0625f : 1.f), hi = floorf(x);
        const bool ok = fabsf(x) < 134217728.f;
        const u32 lo = ok ? ((u32)((x - hi) * 4294967296.f) & ~255u) | 1u : 0x11u;
        const int t = T2 * 32 + r32;
        __hip_atomic_fetch_add((GLBP u64*)(ws + O_S2 + hh * 16384 + ((t >> 1) & 1) * 8192 + (t >> 2) * 16 + (t & 1) * 8),
                               ((u64)(u32)(ok ? (int)hi : 0) << 32) | lo, __ATOMIC_RELAXED, __HIP_MEMORY_SCOPE_AGENT);
      }
      {
        const bool b0 = lane & 1, b1 = lane & 2;
        const u32 s0 = dppu<0xB1>(b0 ? o[0] : o[1]), s1_ = dppu<0xB1>(b0 ? o[2] : o[3]);
        const u32 p0 = b0 ? s0 : o[0], p1 = b0 ? o[1] : s0, p2 = b0 ? s1_ : o[2], p3 = b0 ? o[3] : s1_;
        const u32 t0 = dppu<0x4E>(b1 ? p0 : p2), t1 = dppu<0x4E>(b1 ? p1 : p3);
        o = (u32x4){b1 ? t0 : p0, b1 ? t1 : p1, b1 ? p2 : t0, b1 ? p3 : t1};
      }
      st16(rs, O_XP + (nt * 32 + kq * 8 + hh * 4 + (lane & 3)) * 8192 + (T2 * 32 + (r32 & ~3)) * 4, notempty(o));
    }
    if (S3_GATE && w == 4) st4(rs, lane == 0 ? O_F3 + b * 128 : OOB, 0, tag);
    STAMP(3, 5);
    {
      u32 kix[9];
      conv_prep(tid, w, kix);
      expand(tid, ch, kix);
    }
    delay(S3_DELAY + (n < 96 ? 2 * S3_STEP : n < 184 ? S3_STEP : 0));
    if (S3_GATE) {
      if (w < 4) poll(rs, O_F3 + tid * 128, 0, tag);
      BAR();
    }
    STAMP(3, 6);
    {
      u32x4 sv[4], xr;
      xload<4>(rs, sv, tid * 16, O_S2, 8192);
      xr = ld16(rs, tid * 16, O_XP + n * 8192);
      auto s3_ok = [&]() {
        u32 all = 8;
#pragma unroll
        for (int k = 0; k < 4; ++k) all &= sv[k][0] & sv[k][2];
        return none(all == 0 || umax4(0, xr) == EMPTY);
      };
      if (!s3_ok()) {
#pragma unroll 1
        for (int s = 0; s < SPIN_MAX; ++s) {
          asm volatile("" ::: "memory");
          u32x4 t[5];
#pragma unroll
          for (int k = 0; k < 4; ++k) t[k] = ld16(rs, (sv[k][0] & sv[k][2] & 8) ? OOB : tid * 16, O_S2 + k * 8192);
          t[4] = ld16(rs, umax4(0, xr) == EMPTY ? tid * 16 : OOB, O_XP + n * 8192);
#pragma unroll
          for (int k = 0; k < 4; ++k) sv[k] = (sv[k][0] & sv[k][2] & 8) ? sv[k] : t[k];
          xr = umax4(0, xr) == EMPTY ? t[4] : xr;
          if (s3_ok()) break;
          __builtin_amdgcn_s_sleep(4);
        }
      }
      u32 fl = 0;
#pragma unroll
      for (int k = 0; k < 4; ++k) fl |= sv[k][0] | sv[k][2];
      STAMP(3, 7);
      float s1[4], s2[4];
#pragma unroll
      for (int i = 0; i < 4; ++i) {
        s1[i] = fmaf((float)sv[i >> 1][2 * (i & 1)], 2.3283064365386963e-10f, (float)(int)sv[i >> 1][2 * (i & 1) + 1]);
        s2[i] = fmaf((float)sv[2 + (i >> 1)][2 * (i & 1)], 2.3283064365386963e-10f, (float)(int)sv[2 + (i >> 1)][2 * (i & 1) + 1]) * 16.f;
      }
      if (__builtin_expect(!none((fl & 0xf0) != 0), 0)) {
#pragma unroll
        for (int i = 0; i < 4; ++i) s1[i] = s2[i] = 0.f;
#pragma unroll 1
        for (int p = 0; p < 256; ++p) {
          u32x4 v;
#pragma unroll 1
          for (int s = 0; s < SPIN_MAX; ++s) {
            asm volatile("" ::: "memory");
            v = ld16(rs, tid * 16, O_XP + p * 8192);
            if (none(umax4(0, v) == EMPTY)) break;
          }
#pragma unroll
          for (int i = 0; i < 4; ++i) { s1[i] = dot2(v[i], 0x3f803f80u, s1[i]); s2[i] = dot2(v[i], v[i], s2[i]); }
        }
      }
      float mean[4], rstd[4];
#pragma unroll
      for (int i = 0; i < 4; ++i) {
        mean[i] = s1[i] * (1.f / SD);
        rstd[i] = __builtin_amdgcn_rsqf(fmaxf(fmaf(s2[i], 1.f / SD, -mean[i] * mean[i]), 0.f) + 1e-5f);
      }
      normalize(tid, xr, mean, rstd);
    }
    conv(Mode<1>{});
  }
}

extern "C" void kernel_launch(void* const* d_in, const int* in_sizes, int n_in, void* d_out, int out_size,
                              void* d_ws, size_t ws_size, hipStream_t stream) {
  k_all<<<256, 512, 0, stream>>>((const u16*)d_in[0], (const u16*)d_in[1], (const u16*)d_in[2], (const u16*)d_in[3],
                                 (const u16*)d_in[4], (const u16*)d_in[5], (const u16*)d_in[6], (const u16*)d_in[8],
                                 (const u16*)d_in[9], (const u16*)d_in[10], (char*)d_ws, (u16*)d_out);
}
```

```cpp
#define XK 0
#define XI 1
#include <hip/hip_runtime.h>
#include <stdint.h>

typedef unsigned short u16;
typedef uint32_t u32;
typedef unsigned long long u64;
typedef __attribute__((ext_vector_type(2))) float f32x2;
typedef __attribute__((ext_vector_type(4))) float f32x4;
typedef __attribute__((ext_vector_type(16))) float f32x16;
typedef __attribute__((ext_vector_type(2))) _Float16 f16x2;
typedef __attribute__((ext_vector_type(8))) _Float16 f16x8;
typedef __attribute__((ext_vector_type(2))) __bf16 bf16x2;
typedef __attribute__((ext_vector_type(8))) __bf16 bf16x8;
typedef __attribute__((ext_vector_type(4))) short s16x4;
typedef __attribute__((ext_vector_type(2))) u32 u32x2;
typedef __attribute__((ext_vector_type(4))) u32 u32x4;
typedef __amdgpu_buffer_rsrc_t rsrc_t;
#define LDSP __attribute__((address_space(3)))
#define GLBP __attribute__((address_space(1)))
#define DEVI __device__ __forceinline__

constexpr int SL = 2048, SD = 512, NM = 128, DF = 1376, FT = 86;
constexpr int KCOPY = 2080;
constexpr int KCH_B = 2 * KCOPY * 2;
constexpr u32 O_EP = 0;
constexpr u32 O_F3 = 4096;
constexpr u32 O_G0 = O_F3 + 32768;
constexpr u32 O_G1 = O_G0 + 32768;
constexpr u32 O_S2 = O_G1 + 65536;
constexpr u32 O_W2F = O_S2 + 32768;
constexpr u32 H_SZ = 64 * FT * 1024;
constexpr u32 O_XT = O_W2F + 8 * FT * 2048 + 4096;
constexpr u32 O_H = O_XT + (2u << 20);
constexpr u32 O_XP = O_H + H_SZ;
constexpr u32 LAY_SZ = (4u << 20) + H_SZ;
constexpr u32 O_XG = O_H + LAY_SZ + (6u << 20);
constexpr u32 WS_END = O_XG + (4u << 20);
constexpr int L_Y = 0;
constexpr int TBP = 320, L_TB = 0, TB_W = 26 * TBP;
constexpr int L_K = 32768;
constexpr int L_U = L_K + 2 * KCH_B, U_CH = (240 + 2048) * 2;
constexpr int L_F = 91392;
constexpr int L_R3 = 98304;
constexpr int L_ST = 156928;
constexpr int L_CS = L_ST + 3072;
constexpr int L_TH = L_CS + 1024;
constexpr int L_RS = L_TH + 2640;
constexpr int L_SZ = 163840;
constexpr int SPIN_MAX = 1 << 16;
#define X_DELAY 2
#define CHAIN_PRIO 0
#define RAW0 8
#define RAW1 12
#define RAW2 16
#define S3_GATE 0
#define B_PRIO 1
#define B_AHEAD 4
#define T2_DELAY 6
#define B_MID 0
#define X_ROUND 2
#define GRAN_AT 2
#define HCK 0
#define ODD_DELAY 0
#define ST_A 4
#define WROT 0
#define CLR_E 0
#define CLR_M 0
#define LATE_DELAY 9
#define ST1_AT 8
#define WT_AT 4
#define NHW 12
#define EARLY_T 44
#define S3_STEP 4
#define S3_DELAY 9

DEVI float bflo(u32 v) { return __uint_as_float(v << 16); }
DEVI float bfhi(u32 v) { return __uint_as_float(v & 0xffff0000u); }
DEVI float b2f(u16 v) { return __uint_as_float(((u32)v) << 16); }
DEVI u32 pkbf(float a, float b) { bf16x2 r = __builtin_convertvector((f32x2){a, b}, bf16x2); return __builtin_bit_cast(u32, r); }
DEVI u32 pkf16(float a, float b) { f16x2 r = __builtin_convertvector((f32x2){a, b}, f16x2); return __builtin_bit_cast(u32, r); }
DEVI float rbf(float a) { return bflo(pkbf(a, 0.f)); }
template <int C> DEVI float dppf(float v) { return __uint_as_float(__builtin_amdgcn_update_dpp(0u, __float_as_uint(v), C, 0xf, 0xf, true)); }
template <int C> DEVI u32 dppu(u32 v) { return __builtin_amdgcn_update_dpp(0u, v, C, 0xf, 0xf, true); }
DEVI float sw16(float v) { auto r = __builtin_amdgcn_permlane16_swap(__float_as_uint(v), __float_as_uint(v), false, false); return __uint_as_float(r[0]) + __uint_as_float(r[1]); }
DEVI float sw32(float a, float b) { auto r = __builtin_amdgcn_permlane32_swap(__float_as_uint(a), __float_as_uint(b), false, false); return __uint_as_float(r[0]) + __uint_as_float(r[1]); }
DEVI float wsum(float v) { v += dppf<0xB1>(v); v += dppf<0x4E>(v); v += dppf<0x141>(v); v += dppf<0x140>(v); v = sw16(v); return sw32(v, v); }
DEVI float wmax(float v) {
  v = fmaxf(v, dppf<0xB1>(v)); v = fmaxf(v, dppf<0x4E>(v)); v = fmaxf(v, dppf<0x141>(v)); v = fmaxf(v, dppf<0x140>(v));
  auto r = __builtin_amdgcn_permlane16_swap(__float_as_uint(v), __float_as_uint(v), false, false);
  v = fmaxf(__uint_as_float(r[0]), __uint_as_float(r[1]));
  r = __builtin_amdgcn_permlane32_swap(__float_as_uint(v), __float_as_uint(v), false, false);
  return fmaxf(__uint_as_float(r[0]), __uint_as_float(r[1]));
}
DEVI float dot2(u32 a, u32 b, float c) { return __builtin_amdgcn_fdot2_f32_bf16(__builtin_bit_cast(bf16x2, a), __builtin_bit_cast(bf16x2, b), c, false); }

#define WAITV(n) asm volatile("s_waitcnt vmcnt(" #n ")" ::: "memory")
#define WAITL0() asm volatile("s_waitcnt lgkmcnt(0)" ::: "memory")
#define SCHED() __builtin_amdgcn_sched_barrier(0)
#define BAR() do { asm volatile("s_waitcnt lgkmcnt(0)\n\ts_barrier" ::: "memory"); } while (0)

#define STAMP(k, i) do { if (((k) == 0 && (i) == 0) || ((k) == XK && (i) == XI)) { u64 t_; \
  asm volatile("s_memrealtime %0\n\ts_waitcnt lgkmcnt(0)" : "=s"(t_) :: "memory"); if ((k) == 0 && (i) == 0) tl0 = (u32)t_; else tl1 = (u32)t_; } } while (0)
#define RETRY(p)

DEVI u32x4 ld16(rsrc_t rs, u32 voff, u32 soff) { return __builtin_amdgcn_raw_buffer_load_b128(rs, voff, soff, 16); }
DEVI u32 ld4(rsrc_t rs, u32 voff, u32 soff) { return __builtin_amdgcn_raw_buffer_load_b32(rs, voff, soff, 16); }
DEVI u16 ld2(rsrc_t rs, u32 voff, u32 soff) { return __builtin_amdgcn_raw_buffer_load_b16(rs, voff, soff, 16); }
DEVI void st16(rsrc_t rs, u32 off, u32x4 v) { __builtin_amdgcn_raw_buffer_store_b128(v, rs, off, 0, 16); }
DEVI void st4(rsrc_t rs, u32 voff, u32 soff, u32 v) { __builtin_amdgcn_raw_buffer_store_b32(v, rs, voff, soff, 16); }
template <int K> DEVI void dma1(rsrc_t rs, u32 goff, u32 lds, u32 lane16) {
  const u32 g = goff + (K & ~3) * 1024;
  if constexpr ((K & 3) == 0)
    asm volatile("s_mov_b32 m0, %0\n\ts_nop 0\n\tbuffer_load_dwordx4 %1, %2, %3 offen sc1 lds"
                 :: "s"(lds + K * 1024), "v"(lane16), "s"(rs), "s"(g) : "memory");
  else
    asm volatile("buffer_load_dwordx4 %0, %1, %2 offen offset:%3 sc1 lds" :: "v"(lane16), "s"(rs), "s"(g), "i"((K & 3) * 1024) : "memory");
}
DEVI void dmak(int k, rsrc_t rs, u32 goff, u32 lds, u32 lane16) {
  switch (k) {
#define DK(n) case n: dma1<n>(rs, goff, lds, lane16); break;
    DK(0) DK(1) DK(2) DK(3) DK(4) DK(5) DK(6) DK(7) DK(8) DK(9) DK(10) DK(11) DK(12) DK(13)
#undef DK
  }
}
DEVI bool none(bool p) { return __builtin_amdgcn_ballot_w64(p) == 0; }
constexpr u32 OOB = 0x80000000u;
DEVI void poll(rsrc_t rs, u32 f, u32 voff, u32 soff, u32 tag) {
#pragma unroll 1
  for (int s = 0; s < SPIN_MAX; ++s) {
    if (none(f != tag)) break;
    asm volatile("" ::: "memory");
    f = ld4(rs, voff, soff);
  }
  asm volatile("" ::: "memory");
}
DEVI void poll(rsrc_t rs, u32 voff, u32 soff, u32 tag) {
  asm volatile("" ::: "memory");
  poll(rs, ld4(rs, voff, soff), voff, soff, tag);
}
constexpr u32 EMPTY = 0xffffffffu;
DEVI u32 umax4(u32 m, const u32x4& v) { return max(max(max(max(m, v[0]), v[1]), v[2]), v[3]); }
DEVI u32x4 notempty(u32x4 v) { return (u32x4){min(v[0], EMPTY - 1), min(v[1], EMPTY - 1), min(v[2], EMPTY - 1), min(v[3], EMPTY - 1)}; }
template <int N> DEVI void xload(rsrc_t rs, u32x4 (&v)[N], u32 voff, u32 soff, u32 stride) {
#pragma unroll
  for (int i = 0; i < N; ++i) v[i] = ld16(rs, voff, soff + i * stride);
}
template <int N> DEVI bool xfull(const u32x4 (&v)[N]) {
  u32 m = 0;
#pragma unroll
  for (int i = 0; i < N; ++i) m = umax4(m, v[i]);
  return none(m == EMPTY);
}
template <int N> DEVI void xretry(rsrc_t rs, u32x4 (&v)[N], u32 voff, u32 soff, u32 stride) {
#pragma unroll 1
  for (int s = 0; s < SPIN_MAX; ++s) {
    asm volatile("" ::: "memory");
    u32x4 t[N];
#pragma unroll
    for (int i = 0; i < N; ++i) t[i] = ld16(rs, umax4(0, v[i]) == EMPTY ? voff : OOB, soff + i * stride);
#pragma unroll
    for (int i = 0; i < N; ++i) v[i] = umax4(0, v[i]) == EMPTY ? t[i] : v[i];
    if (xfull<N>(v)) break;
    __builtin_amdgcn_s_sleep(4);
  }
}
template <int N> DEVI void xpark(rsrc_t rs, u32x4 (&v)[N], u32 voff, u32 soff, u32 stride, u32 lds) {
  if (!xfull<N>(v)) xretry<N>(rs, v, voff, soff, stride);
#pragma unroll
  for (int i = 0; i < N; ++i) *(LDSP u32x4*)(size_t)(lds + i * stride) = v[i];
}
DEVI void delay(int n) { for (int i = 0; i < n; ++i) __builtin_amdgcn_s_sleep(2); }

DEVI f32x2 cexp(float are, float aim, float p) {
  const float e = __builtin_amdgcn_exp2f(are * p), ph = __builtin_amdgcn_fractf(aim * p);
  return (f32x2){__builtin_amdgcn_cosf(ph), __builtin_amdgcn_sinf(ph)} * (f32x2){e, e};
}
DEVI f32x2 cmul(f32x2 a, f32x2 b) {
  f32x2 r;
  asm("v_pk_mul_f32 %0, %1, %2 op_sel:[0,0] op_sel_hi:[0,1]\n\t"
      "v_pk_fma_f32 %0, %1, %2, %0 op_sel:[1,1,0] op_sel_hi:[1,0,1] neg_lo:[1,0,0]" : "=&v"(r) : "v"(a), "v"(b));
  return r;
}
DEVI f16x8 cmul4(f32x4 a0, f32x4 a1, f32x4 b0, f32x4 b1) {
  f32x2 r0, r1, r2, r3;
  asm("v_pk_mul_f32 %0, %4, %8 op_sel:[0,0] op_sel_hi:[0,1]\n\tv_pk_mul_f32 %1, %5, %9 op_sel:[0,0] op_sel_hi:[0,1]\n\t"
      "v_pk_mul_f32 %2, %6, %10 op_sel:[0,0] op_sel_hi:[0,1]\n\tv_pk_mul_f32 %3, %7, %11 op_sel:[0,0] op_sel_hi:[0,1]\n\t"
      "v_pk_fma_f32 %0, %4, %8, %0 op_sel:[1,1,0] op_sel_hi:[1,0,1] neg_lo:[1,0,0]\n\t"
      "v_pk_fma_f32 %1, %5, %9, %1 op_sel:[1,1,0] op_sel_hi:[1,0,1] neg_lo:[1,0,0]\n\t"
      "v_pk_fma_f32 %2, %6, %10, %2 op_sel:[1,1,0] op_sel_hi:[1,0,1] neg_lo:[1,0,0]\n\t"
      "v_pk_fma_f32 %3, %7, %11, %3 op_sel:[1,1,0] op_sel_hi:[1,0,1] neg_lo:[1,0,0]"
      : "=&v"(r0), "=&v"(r1), "=&v"(r2), "=&v"(r3)
      : "v"(a0.xy), "v"(a0.zw), "v"(a1.xy), "v"(a1.zw), "v"(b0.xy), "v"(b0.zw), "v"(b1.xy), "v"(b1.zw));
  return __builtin_bit_cast(f16x8, (u32x4){pkf16(r0[0], r0[1]), pkf16(r1[0], r1[1]), pkf16(r2[0], r2[1]), pkf16(r3[0], r3[1])});
}
DEVI s16x4 trread(u32 a) { return __builtin_amdgcn_ds_read_tr16_b64_v4i16((LDSP s16x4*)(size_t)a); }
DEVI bf16x8 trfrag(u32 a) {
  const s16x4 lo = trread(a), hi = trread(a + 256);
  return __builtin_bit_cast(bf16x8, __builtin_shufflevector(lo, hi, 0, 1, 2, 3, 4, 5, 6, 7));
}
template <class T> DEVI T lds_ld(u32 a) { return *(const LDSP T*)(size_t)a; }
template <class T> DEVI void lds_st(u32 a, T v) { *(LDSP T*)(size_t)a = v; }

constexpr int PD = 4;
DEVI void g1_pre(bf16x8 (&fr)[PD], const u32 (&xq)[4], int toff) {
#pragma unroll
  for (int i = 0; i < PD; ++i) fr[i] = trfrag(xq[0] + toff + i * 1024);
}
template <int ST, class F>
DEVI float g1_chain(f32x16& acc, const bf16x8 (&W)[32], bf16x8 (&fr)[PD], const u32 (&xq)[4], int toff, u32 stdst, F side) {
#pragma unroll
  for (int e = 0; e < 16; ++e) acc[e] = 0.f;
  f32x4 q1 = {0.f, 0.f, 0.f, 0.f}, q2 = {0.f, 0.f, 0.f, 0.f};
  u32 one2 = 0x3f803f80u;
  if (ST) asm volatile("" : "+v"(one2));
  const s16x4 ones = __builtin_bit_cast(s16x4, (u32x2){one2, one2});
  auto fin = [&]() {
    const bool b0 = stdst & 16, b1 = stdst & 32;
    return sw32(q1[0], b1 ? (b0 ? q2[3] : q2[2]) : (b0 ? q2[1] : q2[0]));
  };
  if (CHAIN_PRIO) __builtin_amdgcn_s_setprio(1);
  SCHED();
#pragma unroll
  for (int i = 0; i < 32; ++i) {
    if (i < ST) {
      const u32x4 v = __builtin_bit_cast(u32x4, fr[i % PD]);
#pragma unroll
      for (int j = 0; j < 2; ++j) {
        const s16x4 x = __builtin_bit_cast(s16x4, (u32x2){v[2 * j], v[2 * j + 1]});
        q2 = __builtin_amdgcn_mfma_f32_4x4x4bf16_1k(x, x, q2, 0, 0, 0);
        q1 = __builtin_amdgcn_mfma_f32_4x4x4bf16_1k(ones, x, q1, 0, 0, 0);
      }
      SCHED();
    }
    acc = __builtin_amdgcn_mfma_f32_32x32x16_bf16(W[i], fr[i % PD], acc, 0, 0, 0);
    SCHED();
    if (ST > 0 && ST < 32 && i == ST + 1) lds_st<float>(stdst, fin());
    if (i + PD < 32) fr[i % PD] = trfrag(xq[(i + PD) >> 3] + toff + ((i + PD) & 7) * 1024);
    if (!(i & 1)) side(i >> 1);
    SCHED();
  }
  if (CHAIN_PRIO) __builtin_amdgcn_s_setprio(0);
  return ST == 32 ? fin() : 0.f;
}
template <int I> struct Idx { static constexpr int V = I; };
template <int I0, int I1, class F> DEVI void sfor(F f) {
  if constexpr (I0 < I1) { f(Idx<I0>{}); sfor<I0 + 1, I1>(f); }
}
constexpr int cv_lag(int st) { return st >= 24 ? st - 24 : (st & 1) ? 7 - ((st & 7) >> 1) : (st & 7) >> 1; }
template <bool V> struct Role { static constexpr bool A = V; };
template <int M> struct Mode { static constexpr int V = M; };
template <bool PK, class F> DEVI u32x4 g1_epi(const f32x16& acc, float nm, float v, u32 cssrc, F mid) {
  f32x4 cs[4];
#pragma unroll
  for (int q = 0; q < 4; ++q) cs[q] = lds_ld<f32x4>(cssrc + q * 16);
  const float rs = __builtin_amdgcn_rsqf(v);
  const float k0 = rs * -2.302208198f, k1 = rs * rs * rs * -0.102943239f;
  if constexpr (PK) {
    u32x4 o;
    f32x2 g[4], e[4];
#pragma unroll
    for (int p = 0; p < 4; ++p) {
      g[p] = __builtin_elementwise_fma((f32x2){cs[p >> 1][2 * (p & 1)], cs[p >> 1][2 * (p & 1) + 1]}, (f32x2){nm, nm}, (f32x2){acc[2 * p], acc[2 * p + 1]});
      const f32x2 t = g[p] * __builtin_elementwise_fma(g[p] * g[p], (f32x2){k1, k1}, (f32x2){k0, k0});
      e[p] = (f32x2){__builtin_amdgcn_exp2f(t[0]), __builtin_amdgcn_exp2f(t[1])};
    }
#pragma unroll
    for (int p = 0; p < 4; ++p) {
      const f32x2 d = __builtin_elementwise_fma(e[p], (f32x2){v, v}, (f32x2){v, v});
      const f32x2 uu = __builtin_elementwise_fma((f32x2){cs[2 + (p >> 1)][2 * (p & 1)], cs[2 + (p >> 1)][2 * (p & 1) + 1]}, (f32x2){nm, nm},
                                                 (f32x2){acc[8 + 2 * p], acc[9 + 2 * p]});
      const f32x2 r = g[p] * uu * (f32x2){__builtin_amdgcn_rcpf(d[0]), __builtin_amdgcn_rcpf(d[1])};
      o[p] = pkbf(r[0], r[1]);
    }
    return notempty(o);
  }
  u32x4 o;
#pragma unroll
  for (int q = 0; q < 2; ++q) {
    float g0 = cs[q][0], g1 = cs[q][1], g2 = cs[q][2], g3 = cs[q][3];
    float u0 = cs[2 + q][0], u1 = cs[2 + q][1], u2 = cs[2 + q][2], u3 = cs[2 + q][3];
    float t0, t1, t2, t3;
    asm("v_fma_f32 %0, %20, %0, %12\n\tv_fma_f32 %1, %20, %1, %13\n\tv_fma_f32 %2, %20, %2, %14\n\tv_fma_f32 %3, %20, %3, %15\n\t"
        "v_mul_f32 %8, %0, %0\n\tv_mul_f32 %9, %1, %1\n\tv_mul_f32 %10, %2, %2\n\tv_mul_f32 %11, %3, %3\n\t"
        "v_fma_f32 %8, %8, %22, %21\n\tv_fma_f32 %9, %9, %22, %21\n\tv_fma_f32 %10, %10, %22, %21\n\tv_fma_f32 %11, %11, %22, %21\n\t"
        "v_mul_f32 %8, %0, %8\n\tv_mul_f32 %9, %1, %9\n\tv_mul_f32 %10, %2, %10\n\tv_mul_f32 %11, %3, %11\n\t"
        "v_exp_f32 %8, %8\n\tv_exp_f32 %9, %9\n\tv_exp_f32 %10, %10\n\tv_exp_f32 %11, %11\n\t"
        "v_fma_f32 %4, %20, %4, %16\n\tv_fma_f32 %5, %20, %5, %17\n\tv_fma_f32 %6, %20, %6, %18\n\tv_fma_f32 %7, %20, %7, %19\n\t"
        "v_fma_f32 %8, %8, %23, %23\n\tv_fma_f32 %9, %9, %23, %23\n\tv_fma_f32 %10, %10, %23, %23\n\tv_fma_f32 %11, %11, %23, %23\n\t"
        "v_rcp_f32 %8, %8\n\tv_rcp_f32 %9, %9\n\tv_rcp_f32 %10, %10\n\tv_rcp_f32 %11, %11\n\t"
        "v_mul_f32 %0, %0, %4\n\tv_mul_f32 %1, %1, %5\n\tv_mul_f32 %2, %2, %6\n\tv_mul_f32 %3, %3, %7\n\t"
        "v_mul_f32 %0, %0, %8\n\tv_mul_f32 %1, %1, %9\n\tv_mul_f32 %2, %2, %10\n\tv_mul_f32 %3, %3, %11"
        : "+v"(g0), "+v"(g1), "+v"(g2), "+v"(g3), "+v"(u0), "+v"(u1), "+v"(u2), "+v"(u3), "=&v"(t0), "=&v"(t1), "=&v"(t2), "=&v"(t3)
        : "v"(acc[4 * q]), "v"(acc[4 * q + 1]), "v"(acc[4 * q + 2]), "v"(acc[4 * q + 3]),
          "v"(acc[8 + 4 * q]), "v"(acc[9 + 4 * q]), "v"(acc[10 + 4 * q]), "v"(acc[11 + 4 * q]), "v"(nm), "v"(k0), "v"(k1), "v"(v));
    o[2 * q] = pkbf(g0, g1);
    o[2 * q + 1] = pkbf(g2, g3);
    if (q == 0) mid();
  }
  return notempty(o);
}
#define MFMA_DRAIN(a) asm volatile("s_nop 15\n\ts_nop 3" : "+v"(a))

__global__ __launch_bounds__(512, 2) void k_all(const u16* __restrict__ u, const u16* __restrict__ logA,
                                                const u16* __restrict__ Aim, const u16* __restrict__ Cre,
                                                const u16* __restrict__ Cim, const u16* __restrict__ logdt,
                                                const u16* __restrict__ dsk, const u16* __restrict__ wg,
                                                const u16* __restrict__ wu, const u16* __restrict__ wd,
                                                char* ws, u16* __restrict__ out) {
  __shared__ __attribute__((aligned(16))) char sm[L_SZ];
  const int tid0 = threadIdx.x, w0 = __builtin_amdgcn_readfirstlane(tid0 >> 6), b = blockIdx.x;
  const int n0 = (b & 7) * 32 + (b >> 3);
  asm volatile("" :: "v"((LDSP char*)sm) : "memory");
  u32 tl0 = 0, tl1 = 0;
  STAMP(0, 0);
  u32 pf;
  {
    u64 pc;
    u32 len;
    asm volatile("s_getpc_b64 %0\nKCODE_BEG:\n\ts_mov_b32 %1, KCODE_END-KCODE_BEG-4\n\t.subsection 1\nKCODE_END:\n\t.subsection 0" : "=s"(pc), "=s"(len));
    const u32 o = min((u32)(((b >> 3) + 32 * (tid0 & 15)) * 128), len);
    asm volatile("global_load_dword %0, %1, %2" : "=v"(pf) : "v"(o), "s"(pc) : "memory");
  }
  asm volatile("" :: "s"(u), "s"(logA), "s"(Aim), "s"(Cre), "s"(Cim), "s"(logdt), "s"(dsk), "s"(wg), "s"(wu), "s"(wd), "s"(ws), "s"(out));
  const rsrc_t rs = __builtin_amdgcn_make_buffer_rsrc(ws, 0, WS_END, 0x00020000);
  u32 map0;
#define PHASE_IDS()                                                                               \
  int tid = tid0, w = w0, n = n0;                                                                 \
  u32 tag = tag0, map = map0;                                                                     \
  asm volatile("" : "+v"(tid));                                                                   \
  asm volatile("" : "+s"(w), "+s"(n), "+s"(map), "+s"(tag));                                      \
  const int lane = tid & 63, r32 = lane & 31, hh = lane >> 5, ch = w >> 2;                        \
  const u32 lane16 = lane * 16;                                                                   \
  const int grp = map & 15, sl = map >> 4, Ta = min(grp, 10), ntile = grp < 9 ? 6 : grp < 11 ? 5 : 0; \
  const int ftw = sl * 4 + (w & 3);                                                               \
  const int nt = n & 7, tt = w >> 2, kq = w & 3, T2 = (n >> 3) + 32 * tt;                         \
  (void)r32; (void)hh; (void)ch; (void)lane16; (void)ftw; (void)nt; (void)kq; (void)T2; (void)Ta; (void)ntile

  u32 tag0, dsk2;
  float ks0, ks1;
  float base[2][4];
  u32 kb, ub;
  auto normalize = [&](int tid, const u32x4& xr, const float (&mean)[4], const float (&rstd)[4]) {
    const f32x2 d = {bflo(dsk2), bfhi(dsk2)};
    f32x2 un[4];
#pragma unroll
    for (int i = 0; i < 4; ++i) {
      const f32x2 x = {bflo(xr[i]), bfhi(xr[i])};
      un[i] = (x - mean[i]) * rstd[i];
      const f32x2 t = __builtin_elementwise_fma(un[i], d, x);
      base[0][i] = t[0]; base[1][i] = t[1];
    }
    lds_st<u32x2>(L_U + 480 + tid * 8, (u32x2){pkf16(un[0][0], un[1][0]), pkf16(un[2][0], un[3][0])});
    lds_st<u32x2>(L_U + U_CH + 480 + tid * 8, (u32x2){pkf16(un[0][1], un[1][1]), pkf16(un[2][1], un[3][1])});
  };
  auto conv_prep = [&](int tid, int w, u32 (&kix)[9]) {
    const int i = tid & 255, ch = w >> 2;
    const int sh = min(__builtin_clz((u32)(255 - i) | 16) - 24, 3);
    const float sc = __uint_as_float((124 + sh) << 23);
    float l0 = (float)(2047 - 8 * i);
    asm volatile("" : "+v"(l0));
    const float mg = 8388608.f + (float)(384 - 128 * sh);
#pragma unroll
    for (int j = 0; j < 9; ++j) kix[j] = (__float_as_uint(fmaf(l0 - (float)j, sc, mg)) << 1) + (u32)(L_TH + ch * 1312 - 0x96000000u);
    asm volatile("" : "+v"(kix[8]));
    kix[8] -= i == 223 ? 2 : 0;
    kix[8] = i == 255 ? L_TH + 2624 : kix[8];
#pragma unroll
    for (int j = 0; j < 9; ++j) asm volatile("" : "+v"(kix[j]));
    const int r = tid & 15, h = (tid >> 4) & 3;
    kb = L_K + ((w >> 1) & 1) * KCH_B + 4 * (((2031 - r + 8 * h) >> 1) + ((r & 1) ? 0 : KCOPY / 2)) - 256 * (w & 1) - 64 * 11;
    ub = L_U + ((w >> 1) & 1) * U_CH + 2 * (224 + 16 * r + 8 * h) - 256 * (w & 1) - 192;
    asm volatile("" : "+v"(kb), "+v"(ub));
  };
  auto expand = [&](int tid, int ch, const u32 (&kix)[9]) {
    u32 z = 0;
    asm volatile("" : "+v"(z));
    if (tid < 76)
      lds_st<u32x4>(tid < 60 ? L_U + (tid >= 30 ? U_CH + (tid - 30) * 16 : tid * 16) : L_K + ((tid - 60) >> 2) * KCOPY * 2 + 4096 + (tid & 3) * 16,
                    (u32x4){z, z, z, z});
    const int i = tid & 255;
    u32 kv[9];
#pragma unroll
    for (int j = 0; j < 9; ++j) kv[j] = lds_ld<u16>(kix[j]);
    lds_st<u32x4>(L_K + ch * KCH_B + i * 16,
                  (u32x4){kv[0] | (kv[1] << 16), kv[2] | (kv[3] << 16), kv[4] | (kv[5] << 16), kv[6] | (kv[7] << 16)});
    lds_st<u32x4>(L_K + ch * KCH_B + i * 16 + KCOPY * 2,
                  (u32x4){kv[1] | (kv[2] << 16), kv[3] | (kv[4] << 16), kv[5] | (kv[6] << 16), kv[7] | (kv[8] << 16)});
  };
  bf16x8 W[32];
  rsrc_t wrs;
  u32 wvo, ra0;
  auto w_put = [&](rsrc_t src, u32 vo, int mat, int r, int j) {
    asm volatile("s_mov_b32 m0, %0\n\ts_nop 0\n\tbuffer_load_dwordx4 %1, %2, %3 offen lds"
                 :: "s"(L_F + j * 8192 + (w0 & 2) * 2048 + mat * 2048 + (w0 & 1) * 1024), "v"(vo), "s"(src),
                    "s"(((j >> 1) * 8 + 4 * r + 2 * (j & 1)) * (16 * DF * 2)) : "memory");
  };
  auto w_get = [&](int r) {
#pragma unroll
    for (int g = 0; g < 4; ++g) {
      u32 a = ra0 + ((g + w0) & 3) * 16384;
      asm volatile("" : "+v"(a));
#pragma unroll
      for (int j = 0; j < 4; ++j) {
        const s16x4 lo = trread(a + j * 4096), hi = trread(a + j * 4096 + 512);
        W[g * 8 + 4 * r + j] = __builtin_bit_cast(bf16x8, __builtin_shufflevector(lo, hi, 0, 1, 2, 3, 4, 5, 6, 7));
      }
    }
  };
  {
    const int tid = tid0, w = w0, n = n0, lane = tid & 63, r32 = lane & 31, hh = lane >> 5, ch = w >> 2, kq = w & 3;
    const u32 lane16 = lane * 16;
    const int pidx = n * 2 * NM + w * 32 + r32;
    const u16 pla = logA[pidx], pai = Aim[pidx], pcr = Cre[pidx], pci = Cim[pidx];
    const u32 car = *(const u32*)(Cre + (n * 2 + ch) * NM + lane * 2), cai = *(const u32*)(Cim + (n * 2 + ch) * NM + lane * 2);
    const u32x4 til = *(const u32x4*)(u + (size_t)((b >> 2) * 32 + r32) * SD + (b & 3) * 128 + (w * 2 + hh) * 8);
    const u32x4 row = *(const u32x4*)(u + (size_t)(4 * (8 * (b >> 2) + w) + (b & 3)) * SD + lane * 8);
    const u32 ep = ld4(rs, O_EP, 0);
    int vz = 0;
    asm volatile("" : "+v"(vz));
    const u16 ldt = logdt[2 * n + ch + vz];
    const u32 dskv = ((const u32*)dsk)[n + vz];
    SCHED();
    __builtin_amdgcn_s_barrier();
    SCHED();
    st16(rs, O_XT + b * 8192 + tid * 16, (u32x4){EMPTY, EMPTY, EMPTY, EMPTY});
    asm volatile("" ::: "memory");
    const u32 pf2 = __builtin_amdgcn_raw_buffer_load_b32(
        __builtin_amdgcn_make_buffer_rsrc((void*)(w < 2 ? wg : w < 4 ? wu : wd), 0, w < 6 ? SD * DF * 2 : 0, 0x00020000),
        lane < 43 ? lane * 64 : OOB, (b * 2 + (w & 1)) * 2752, 0);
    {
      const int pe = (n >> 6) * 32 + (n & 31), grp = pe / 11, sl = pe - grp * 11 + ((n >> 5) & 1) * 11;
      map0 = grp | (sl << 4);
      asm volatile("" : "+s"(map0));
      wrs = __builtin_amdgcn_make_buffer_rsrc((void*)(ch ? wu : wg), 0, SD * DF * 2, 0x00020000);
      wvo = (kq * 8 + (lane >> 3)) * (DF * 2) + sl * 128 + ((((lane & 7) >> 1) ^ (((lane >> 3) & 2) + ch)) * 32) + (lane & 1) * 16;
      const int mat = (lane >> 4) & 1, rq = (lane & 15) >> 2;
      ra0 = L_F + mat * 2048 + (8 * hh + rq) * 128 + ((kq ^ ((rq & 2) + mat)) * 32) + (lane & 3) * 8;
      asm volatile("" : "+v"(ra0), "+v"(wvo));
    }
    u32 kix[9];
    conv_prep(tid, w, kix);
    const bool odd = lane & 1;
    u32 xgo = O_XG + ((b & 3) * 64 + (w * 2 + hh) * 4 + (odd ? 2 : 0)) * 16384 + ((r32 >> 1) & 1) * 8192 + ((b >> 2) * 8 + (r32 >> 2)) * 16;
    const u32 tb0 = L_TB + w * TB_W + r32 * 8;
    u32 tb = tb0 + hh * 13 * TBP, tbe = tb0 + (hh ? 18 : 12) * TBP, tbf = tb0 + (hh ? 19 : 5) * TBP, tbg = tb0 + (hh ? 20 : 25) * TBP,
        tb1 = tb0 + (hh ? 17 : 0) * TBP;
    float pa = hh ? 4.f : 1.f, pb = hh ? 64.f : 256.f, pe = hh ? 16.f : 2044.f, sg = hh ? -1.f : 1.f, sc = hh ? 16384.f : 1.f;
    u32 aL = L_TB + w * TB_W + (lane >> 4) * 32 + (lane & 3) * TBP, aM = L_TB + w * TB_W + (lane >> 4) * 32 + ((lane & 15) >> 2) * TBP;
    u32 a2x = (lane & 15) == 4 ? aL + 4 * TBP : aL;
    asm volatile("" : "+v"(xgo), "+v"(tb), "+v"(tbe), "+v"(tbf), "+v"(tbg), "+v"(tb1), "+v"(pa), "+v"(pb), "+v"(pe), "+v"(sg), "+v"(sc));
    asm volatile("" : "+v"(aL), "+v"(aM), "+v"(a2x));
    lds_st<f32x2>(tb0 + 13 * TBP, (f32x2){16384.f, 0.f});
    if (tid == 0) lds_st<u32x2>(L_TH + 2624, (u32x2){0, 0});
    SCHED();
    const u32 tag = tag0 = __builtin_amdgcn_readfirstlane(ep) + 1u;
    STAMP(0, 1);
    {
      const u32 r0 = dppu<0xB1>(odd ? til[0] : til[2]), r1 = dppu<0xB1>(odd ? til[1] : til[3]);
      st16(rs, xgo, (u32x4){odd ? r0 : til[0], tag, odd ? til[2] : r0, tag});
      st16(rs, xgo + 16384, (u32x4){odd ? r1 : til[1], tag, odd ? til[3] : r1, tag});
      float s1 = 0.f, s2 = 0.f;
#pragma unroll
      for (int c = 0; c < 4; ++c) { s1 = dot2(row[c], 0x3f803f80u, s1); s2 = dot2(row[c], row[c], s2); }
      float s = sw32(s1, s2);
      s += dppf<0xB1>(s); s += dppf<0x4E>(s); s += dppf<0x141>(s); s += dppf<0x140>(s);
      s = sw16(s);
      if (r32 == 0) lds_st<float>(L_RS + w * 8 + hh * 4, s);
    }
    STAMP(0, 2);
    {
      const float dt = rbf(__expf(b2f(ldt)));
      const float ar = -rbf(__expf(b2f(pla))) * dt * 1.44269504088896f, ai = (b2f(pai) * dt) * 0.159154943091895f, aj = ai * sg;
      const int ce = __builtin_amdgcn_frexp_expf(wmax(fmaxf(fmaxf(fabsf(bflo(car)), fabsf(bfhi(car))), fmaxf(fabsf(bflo(cai)), fabsf(bfhi(cai))))));
      if (lane == 0) lds_st<float>(L_RS + 64 + ch * 4, ldexpf(dt, ce - 7));
      const f32x2 a = cexp(ar, ai, pa), bb = cexp(ar, aj, pb);
      const f32x2 e = cexp(ar, aj, pe), e1k = cexp(ar, ai, 1024.f);
      const f32x2 d = hh ? (f32x2){ldexpf(b2f(pcr), 14 - ce), ldexpf(-b2f(pci), 14 - ce)} : e1k;
      const f32x2 as = a * sc, a2 = cmul(as, a), b2 = cmul(bb, bb), b3 = cmul(b2, bb);
      const f32x2 f = cmul(hh ? e : bb, hh ? e : a2);
      lds_st<f32x2>(tb + 1 * TBP, as);
      lds_st<f32x2>(tb + 2 * TBP, a2);
      lds_st<f32x2>(tb + 3 * TBP, cmul(a2, a));
      lds_st<f32x2>(tb + 4 * TBP, bb);
      lds_st<f32x2>(tb + 6 * TBP, b2);
      lds_st<f32x2>(tb + 7 * TBP, b3);
      lds_st<f32x2>(tb + 8 * TBP, d);
      lds_st<f32x2>(tb + 9 * TBP, cmul(d, bb));
      lds_st<f32x2>(tb + 10 * TBP, cmul(d, b2));
      lds_st<f32x2>(tb + 11 * TBP, cmul(d, b3));
      asm volatile("" ::: "memory");
      lds_st<f32x2>(tbe, e);
      lds_st<f32x2>(tbf, f);
      lds_st<f32x2>(tbg, cmul(f, e));
      lds_st<f32x2>(tb1, (f32x2){1.f, 0.f});
    }
    asm volatile("" :: "v"(pf));
    dsk2 = __builtin_amdgcn_readfirstlane(dskv);
    asm volatile("" :: "v"(dskv));
    WAITV(3);
    BAR();
    if (w == 3) {
      const f32x2 v = lds_ld<f32x2>(L_RS + (lane & 7) * 8) * (1.f / SD);
      const float r = __builtin_amdgcn_rsqf(fmaxf(fmaf(-v[0], v[0], v[1]), 0.f) + 1e-5f);
      st16(rs, lane < 8 ? O_G0 + (b & 3) * 8192 + ((b >> 2) * 8 + lane) * 16 : OOB, (u32x4){__float_as_uint(v[0]), tag, __float_as_uint(r), tag});
      st16(rs, lane < 8 ? O_S2 + b * 128 + lane16 : OOB, (u32x4){0, 0, 0, 0});
    }
    STAMP(0, 3);
    u32x4 g[6];
    auto gran_loads = [&]() {
#pragma unroll
      for (int i = 0; i < 2; ++i) g[4 + i] = ld16(rs, tid * 16, O_XG + n * 16384 + i * 8192);
#pragma unroll
      for (int i = 0; i < 4; ++i) g[i] = ld16(rs, tid * 16, O_G0 + i * 8192);
    };
    {
      f32x4 a0 = {0, 0, 0, 0}, a1 = {0, 0, 0, 0}, a2 = {0, 0, 0, 0};
      f32x4 tv[2][12];
#pragma unroll
      for (int s = 0; s < 2; ++s) {
        auto L = [&](int i, u32 a) { tv[s][i] = lds_ld<f32x4>(a + s * 128); tv[s][i + 1] = lds_ld<f32x4>(a + s * 128 + 16); };
        L(0, aL + 17 * TBP); L(2, aM + 21 * TBP); L(4, aM + 13 * TBP); L(6, aL); L(8, aL + 4 * TBP); L(10, a2x + 8 * TBP);
      }
#pragma unroll
      for (int s = 0; s < 2; ++s) {
        const f32x4* t = tv[s];
        SCHED(); w_put(wrs, wvo, ch, 0, 4 * s + 0); SCHED();
        const f16x8 bb = cmul4(t[0], t[1], t[2], t[3]);
        SCHED(); w_put(wrs, wvo, ch, 0, 4 * s + 1); SCHED();
        a0 = __builtin_amdgcn_mfma_f32_16x16x32_f16(cmul4(t[6], t[7], t[4], t[5]), bb, a0, 0, 0, 0);
        SCHED(); w_put(wrs, wvo, ch, 0, 4 * s + 2); SCHED();
        a1 = __builtin_amdgcn_mfma_f32_16x16x32_f16(cmul4(t[8], t[9], t[4], t[5]), bb, a1, 0, 0, 0);
        SCHED(); w_put(wrs, wvo, ch, 0, 4 * s + 3); SCHED();
        a2 = __builtin_amdgcn_mfma_f32_16x16x32_f16(cmul4(t[10], t[11], t[4], t[5]), bb, a2, 0, 0, 0);
      }
      const u32 T = L_TB + w * TB_W + lane16;
      lds_st<f32x4>(T, a0);
      lds_st<f32x4>(T + 1024, a1);
      lds_st<f32x4>(T + 2048, a2);
    }
    if (GRAN_AT == 1) gran_loads();
    STAMP(0, 4);
    BAR();
    if (GRAN_AT == 2) gran_loads();
    if (kq < 3) {
      const u32 T = L_TB + ch * 4 * TB_W + kq * 1024 + lane16;
      const f32x4 v = ((lds_ld<f32x4>(T) + lds_ld<f32x4>(T + TB_W)) + (lds_ld<f32x4>(T + 2 * TB_W) + lds_ld<f32x4>(T + 3 * TB_W))) * 9.5367431640625e-07f;
      const u32 p0 = pkf16(v[0], v[1]), p1 = pkf16(v[2], v[3]);
      const int j = lane & 15, h = lane >> 4;
      const u32 H = L_TH + ch * 1312;
      if (kq == 0) {
        lds_st<u32x2>(H + 2 * (16 * j + 4 * h), (u32x2){p0, p1});
      } else if (kq == 1) {
        lds_st<u32>(H + 2 * (256 + 8 * j + 2 * h), p0);
        lds_st<u16>(H + 2 * (384 + 4 * j + h), (u16)p1);
        lds_st<u16>(H + 2 * (448 + 4 * j + h), (u16)(p1 >> 16));
      } else if (!(h & 1)) {
        const u32 a = H + 2 * (512 + 2 * j + (h >> 1));
        lds_st<u16>(a, (u16)p0);
        lds_st<u16>(a + 64, (u16)(p0 >> 16));
        lds_st<u16>(a + 128, (u16)p1);
        lds_st<u16>(a + 192, (u16)(p1 >> 16));
      } else if (lane == 16) {
        lds_st<u16>(H + 2 * 640, (u16)p0);
      }
    }
    {
      const f32x2 k2 = lds_ld<f32x2>(L_RS + 64);
      ks0 = __uint_as_float(__builtin_amdgcn_readfirstlane(__float_as_uint(k2[0])));
      ks1 = __uint_as_float(__builtin_amdgcn_readfirstlane(__float_as_uint(k2[1])));
    }
    BAR();
    STAMP(0, 5);
#pragma unroll
    for (int k = 0; k < CLR_M; ++k) st16(rs, O_H + b * 30208 + k * 8192 + tid * 16, (u32x4){EMPTY, EMPTY, EMPTY, EMPTY});
    expand(tid, ch, kix);
    asm volatile("" : "+v"(g[0]), "+v"(g[1]), "+v"(g[2]), "+v"(g[3]), "+v"(g[4]), "+v"(g[5]) :: "memory");
    asm volatile("" :: "v"(pf2));
    STAMP(0, 6);
    auto gran_ok = [&]() {
      u32 bad = 0;
#pragma unroll
      for (int k = 0; k < 6; ++k) bad |= (g[k][1] ^ tag) | (g[k][3] ^ tag);
      return none(bad != 0);
    };
    if (!gran_ok()) {
#pragma unroll 1
      for (int s = 0; s < SPIN_MAX; ++s) {
        asm volatile("" ::: "memory");
        gran_loads();
        if (gran_ok()) break;
      }
    }
    {
      float mean[4], rstd[4];
#pragma unroll
      for (int k = 0; k < 4; ++k) { mean[k] = __uint_as_float(g[k][0]); rstd[k] = __uint_as_float(g[k][2]); }
      normalize(tid, (u32x4){g[4][0], g[4][2], g[5][0], g[5][2]}, mean, rstd);
    }
    STAMP(0, 7);
  }

  auto conv = [&](auto mode) {
    constexpr int layer = decltype(mode)::V;
    BAR();
    if (layer == 0) {
      w_get(0);
      BAR();
    }
    STAMP(1 + 3 * layer, 0);
    if (w0 < 4) {
      constexpr int NA = 6;
      f32x4 acc[8];
#pragma unroll
      for (int g = 0; g < 8; ++g) acc[g] = (f32x4){0, 0, 0, 0};
      u32 kp[4];
#pragma unroll
      for (int p = 0; p < 4; ++p) {
        kp[p] = kb - 1024 * p;
        asm volatile("" : "+v"(kp[p]));
      }
      u32x4 A[NA];
      f16x8 B[8];
      auto ldA = [&](auto st_, auto half_) {
        constexpr int st = decltype(st_)::V, half = decltype(half_)::V, a = cv_lag(st);
        if constexpr (st < 32) {
          const u32 p = kp[a >> 1] + 64 * (11 - (8 * (a & 1) + (st >> 3))) + half * 8;
          A[st % NA][2 * half] = lds_ld<u32>(p);
          A[st % NA][2 * half + 1] = lds_ld<u32>(p + 4);
        }
      };
      auto ldB = [&](int rr, int q) { return lds_ld<f16x8>(ub + 64 * (8 * q + 3 - rr)); };
      ldA(Idx<0>{}, Idx<0>{});
      ldA(Idx<0>{}, Idx<1>{});
#pragma unroll
      for (int q = 7; q >= 0; --q) B[q] = ldB(0, q);
      ldA(Idx<1>{}, Idx<0>{});
      ldA(Idx<1>{}, Idx<1>{});
      u32 yb = tid0;
      asm volatile("" : "+v"(yb));
      yb = L_Y + w0 * 8192 + ((yb & 48) + ((yb & 15) ^ ((yb >> 2) & 12))) * 16;
      asm volatile("" : "+v"(yb));
      SCHED();
      sfor<0, 32>([&](auto st_) {
        constexpr int st = decltype(st_)::V, a = cv_lag(st), rr = st >> 3;
        sfor<0, 8 - a>([&](auto j_) {
          constexpr int j = decltype(j_)::V, q = 7 - a - j;
          acc[a + q] = __builtin_amdgcn_mfma_f32_16x16x32_f16(__builtin_bit_cast(f16x8, A[st % NA]), B[q], acc[a + q], 0, 0, 0);
          SCHED();
          if constexpr (st == 0) ldA(Idx<2 + (j >> 1)>{}, Idx<(j & 1)>{});
          if constexpr ((st & 7) == 7 && rr < 3) B[q] = ldB(rr + 1, q);
          if constexpr (!(st & 1) && st < 24 && j == 0) B[7 - a] = ldB(rr + 1, 7 - a);
          if constexpr (!(st & 1) && st >= 2 && st <= 24 && j >= 1 && j <= 4) ldA(Idx<st - 2 + ((j - 1) >> 1) + NA>{}, Idx<((j - 1) & 1)>{});
          if constexpr (st > 24 && j < 2) ldA(Idx<st - 1 + NA>{}, Idx<(j & 1)>{});
          if constexpr (st > 24 && j == (7 - a >= 2 ? 2 : 7 - a)) lds_st<f32x4>(yb + (a - 1) * 1024, acc[a - 1]);
          SCHED();
        });
      });
      lds_st<f32x4>(yb + 7 * 1024, acc[7]);
      if (layer == 0) WAITV(0);
    } else {
      u32 ce = EMPTY, t8 = tid0;
      asm volatile("" : "+v"(ce), "+v"(t8));
      t8 = (t8 & 255) * 16;
      if (layer == 0) {
        const rsrc_t wr0 = __builtin_amdgcn_make_buffer_rsrc((void*)wg, 0, SD * DF * 2, 0x00020000);
        const u32 wv0 = wvo ^ 32;
        const int ft = b >> 1, nb = (b & 1) * 4 + (w0 & 3);
        const rsrc_t wr = __builtin_amdgcn_make_buffer_rsrc((void*)wd, 0, SD * DF * 2, 0x00020000);
        u32 wt[8];
#pragma unroll
        for (int j = 0; j < 8; ++j)
          wt[j] = __builtin_amdgcn_raw_buffer_load_b32(wr, (t8 & 512) * 8 + ((t8 >> 2) & 124) + (j & 3) * (SD * 2),
                                                       (ft * 16 + 8 * (j >> 2)) * (SD * 2) + nb * 128, 0);
#pragma unroll
        for (int j = 2 * CLR_M; j < 4; ++j) st16(rs, O_H + b * 30208 + j * 4096 + t8, (u32x4){ce, ce, ce, ce});
        u32x4 lo, hi;
#pragma unroll
        for (int j = 0; j < 8; ++j) {
          if (j == WT_AT) {
#pragma unroll
            for (int i = 0; i < 4; ++i) {
              lo[i] = __builtin_amdgcn_perm(wt[2 * i + 1], wt[2 * i], 0x05040100u);
              hi[i] = __builtin_amdgcn_perm(wt[2 * i + 1], wt[2 * i], 0x07060302u);
            }
            const u32 wo = (ft < FT ? O_W2F + (nb * FT + ft) * 2048 : OOB) + (t8 & 1023);
            st16(rs, wo, lo);
            st16(rs, wo + 1024, hi);
          }
          w_put(wr0, wv0, 0, 1, j);
          w_put(wrs, wvo, 1, 1, j);
          if (j & 1) st16(rs, (j < 7 || t8 < 1536 ? O_H + b * 30208 + (4 + (j >> 1)) * 4096 : OOB) + t8, (u32x4){ce, ce, ce, ce});
        }
      } else {
#pragma unroll
        for (int j = 0; j < 6; ++j) st16(rs, (j < 5 || t8 < 1536 ? O_H + LAY_SZ + b * 22016 + j * 4096 : OOB) + t8, (u32x4){ce, ce, ce, ce});
        int ws_ = w0 & 3, nn = n0 & 7;
        asm volatile("" : "+s"(ws_), "+s"(nn));
#pragma unroll
        for (int j = 0; j < 14; ++j) dmak(j, rs, O_W2F + (nn * FT + 12) * 2048 + ws_ * 14336, L_R3 + ws_ * 14336, t8 & 1023);
      }
      if (layer == 0) WAITV(0); else WAITV(14);
    }
    STAMP(1 + 3 * layer, 1);
    {
      PHASE_IDS();
      const int c = lane >> 2, h = lane & 3;
      u32 y = L_Y + (64 * w + 16 * h + (c ^ (4 * h))) * 16;
      u32 xo = O_XT + layer * LAY_SZ + n * 128 + (tid >> 3) * 32768 + (tid & 1) * 64 + ((tid >> 1) & 3) * 16;
      asm volatile("" : "+v"(y), "+v"(xo));
      BAR();
      STAMP(1 + 3 * layer, 2);
      const f32x4 y0 = lds_ld<f32x4>(y) + lds_ld<f32x4>(y + 8192), y1 = lds_ld<f32x4>(y + 16384) + lds_ld<f32x4>(y + 24576);
      const u32 a0 = pkbf(fmaf(y0[0], ks0, base[0][0]), fmaf(y0[1], ks0, base[0][1]));
      const u32 a1 = pkbf(fmaf(y0[2], ks0, base[0][2]), fmaf(y0[3], ks0, base[0][3]));
      const u32 c0 = pkbf(fmaf(y1[0], ks1, base[1][0]), fmaf(y1[1], ks1, base[1][1]));
      const u32 c1 = pkbf(fmaf(y1[2], ks1, base[1][2]), fmaf(y1[3], ks1, base[1][3]));
      const bool odd = lane & 1;
      const u32 r0 = dppu<0xB1>(odd ? a0 : c0), r1 = dppu<0xB1>(odd ? a1 : c1);
      st16(rs, xo, notempty((u32x4){odd ? r0 : a0, odd ? r1 : a1, odd ? c0 : r0, odd ? c1 : r1}));
      STAMP(1 + 3 * layer, 3);
      if (layer == 0) {
        w_get(1);
      }
    }
  };
  conv(Mode<0>{});
#pragma unroll 1
  for (int layer = 0; layer < 2; ++layer) {
    const u32 xt_off = O_XT + layer * LAY_SZ, h_off = O_H + layer * LAY_SZ;
    f32x4 e4, o4;
    auto ffn = [&](auto role) {
      constexpr bool A = decltype(role)::A;
      PHASE_IDS();
      auto hof = [&](int p) { return h_off + (T2 * FT + min(kq + 4 * p, FT - 1)) * 1024; };
      const u32 w2s = O_W2F + (nt * FT + 12) * 2048;
      u32 xq[4];
#pragma unroll
      for (int g = 0; g < 4; ++g) xq[g] = hh * 512 + ((lane & 15) >> 2) * 64 + ((lane >> 4) & 1) * 32 + (lane & 3) * 8 + ((g + kq) & 3) * 8192;
      bf16x8 fr[PD];
      u32 csr = L_CS + kq * 128 + hh * 64, str = L_ST + lane16;
      u32 hs = (ftw < FT && ntile > 0 ? h_off + ((A ? Ta : 11 + Ta) * FT + ftw) * 1024 : OOB) + lane16;
      u32 rot16 = (((r32 + 8 * kq) & 31) + 32 * hh) * 16, r16 = r32 * 16, stw = str + kq * 4;
      asm volatile("" : "+v"(r16), "+v"(xq[0]), "+v"(xq[1]), "+v"(xq[2]), "+v"(xq[3]), "+v"(csr), "+v"(str), "+v"(rot16), "+v"(hs), "+v"(stw));
      u32 xs = xt_off + Ta * 32768 + kq * 8192;
      asm volatile("" : "+s"(xs));
      constexpr u32 XS = 11 * 32768;
      const int Tj = 11 * (1 + (sl >> 2)) + Ta;
      const bool job = sl < 4 * (ntile - 1);
      u32 ja = job ? lane * 64 : OOB;
      const u32 js = xs + (1 + (sl >> 2)) * XS + (sl & 3) * 16;
      u32x4 jv[2];
      {
        u32x4 xa[4];
        const u32 xs0 = xt_off + Ta * 32768, t16 = tid * 16;
        if (layer == 1) delay(X_DELAY);
        BAR();
        STAMP(1 + 3 * layer, 4);
        xload<4>(rs, xa, t16, xs0, 8192);
        if (!A) xload<2>(rs, jv, ja, js, 4096);
        if (layer == 0) {
          float c0 = 0.f, c1 = 0.f;
          u32 one2 = 0x3f803f80u;
          asm volatile("" : "+s"(one2));
#pragma unroll
          for (int i = 0; i < 16; ++i) {
            const u32x4 v = __builtin_bit_cast(u32x4, W[A ? i : 16 + i]);
            c0 = dot2(v[0], one2, c0); c1 = dot2(v[1], one2, c1);
            c0 = dot2(v[2], one2, c0); c1 = dot2(v[3], one2, c1);
          }
          c0 += c1;
          lds_st<float>(L_CS + w * 128 + ((r32 >> 2) & 1) * 64 + (r32 >> 3) * 16 + (r32 & 3) * 4, sw32(c0, c0));
        }
        xpark<4>(rs, xa, t16, xs0, 8192, t16);
        STAMP(1 + 3 * layer, 5);
      }
      BAR();
      STAMP(1 + 3 * layer, 6);
      bf16x8 Wr[3][2];
      u32x4 fh[NHW];
      auto slot = [](int p) { return p % NHW; };
      auto hcheck = [&](int p0, int p1) {
        u32 m = 0;
#pragma unroll
        for (int p = p0; p < p1; ++p) m = umax4(m, fh[slot(p)]);
        if (__builtin_expect(!none(m == EMPTY), 0)) {
#pragma unroll 1
          for (int s = 0; s < SPIN_MAX; ++s) {
            asm volatile("" ::: "memory");
            m = 0;
#pragma unroll
            for (int p = p0; p < (p0 + NHW < 22 ? p0 + NHW : 22); ++p)
              if (!none(umax4(0, fh[slot(p)]) == EMPTY)) fh[slot(p)] = ld16(rs, lane16, hof(p));
#pragma unroll
            for (int p = p0; p < p1; ++p) m = umax4(m, fh[slot(p)]);
            RETRY(p0);
            if (none(m == EMPTY)) break;
          }
        }
      };
      {
        f32x16 acc;
        auto nop = []() {};
        const u32 g1_off = O_G1 + layer * 32768;
        u32x4 gs;
        auto recheck = [&](int k, int toff, auto redo) __attribute__((always_inline)) {
          if (__builtin_expect(!none(acc[0] != acc[0]), 0)) {
#pragma unroll 1
            for (int j = 0; j < 32; ++j) {
              u32x4 v[1];
              const u32 so = xt_off + (11 * k + Ta) * 32768 + j * 1024;
              xload<1>(rs, v, lane16, so, 0);
              xpark<1>(rs, v, lane16, so, 0, (k % 3) * 32768 + j * 1024 + lane16);
            }
            WAITL0();
            g1_pre(fr, xq, toff);
            g1_chain<0>(acc, W, fr, xq, toff, 0, [](int) {});
            redo();
          }
        };
        auto ldst = [&](int k) { gs = ld16(rs, r16, g1_off + (11 * k + Ta) * 512); };
        auto epi = [&](int k, auto mid) {
          asm volatile("" : "+v"(gs));
          if (!none(((gs[1] ^ tag) | (gs[3] ^ tag)) != 0)) {
#pragma unroll 1
            for (int s = 0; s < SPIN_MAX; ++s) {
              asm volatile("" ::: "memory");
              ldst(k);
              if (none(((gs[1] ^ tag) | (gs[3] ^ tag)) != 0)) break;
            }
          }
          st16(rs, hs + (k >> 1) * (22 * FT * 1024), g1_epi<false>(acc, __uint_as_float(gs[0]), __uint_as_float(gs[2]), csr, mid));
        };
        auto ldWr = [&](int k) {
          Wr[k >> 1][k & 1] = __builtin_bit_cast(bf16x8, ld16(rs, rot16 + (k & 1) * 1024, O_W2F + (nt * FT + kq + 4 * (k >> 1)) * 2048));
        };
        auto shift = [&]() {
#pragma unroll
          for (int g = 0; g < 4; ++g) { xq[g] += 32768; asm volatile("" : "+v"(xq[g])); }
        };
        if constexpr (A) {
          g1_pre(fr, xq, 0);
          g1_chain<8>(acc, W, fr, xq, 0, stw, [&](int k) {
            if (k >= 2 && k < 10) dmak(k - 2, rs, xs + 2 * XS, 65536 + kq * 8192, lane16);
            else if (k >= 10 && layer == 0) dmak(k - 2, rs, w2s + kq * 14336, L_R3 + kq * 14336, lane16);
          });
          BAR();
          STAMP(2 + 3 * layer, 0);
          {
            const f32x4 p = lds_ld<f32x4>(str);
            float s = p[0] + p[1], q = p[2] + p[3];
            asm volatile("" : "+v"(s), "+v"(q));
            s = (s + q) * (1.f / SD);
            const auto sw = __builtin_amdgcn_permlane32_swap(__float_as_uint(s), __float_as_uint(s), false, false);
            const float m = __uint_as_float(sw[0]);
            st16(rs, hs, g1_epi<false>(acc, -m, fmaxf(fmaf(-m, m, __uint_as_float(sw[1])), 0.f) + 1e-5f, csr, nop));
          }
          shift();
          WAITV(1);
          g1_pre(fr, xq, 32768);
          BAR();
          STAMP(2 + 3 * layer, 1);
          g1_chain<0>(acc, W, fr, xq, 32768, 0, [&](int k) {
            if (k < 8) dmak(k, rs, xs + 4 * XS, 32768 + kq * 8192, lane16);
            else if (k == 8) ldst(2);
            else if (k < 15) ldWr(k - 9);
          });
          recheck(2, 32768, nop);
          BAR();
          STAMP(2 + 3 * layer, 2);
          epi(2, nop);
          g1_pre(fr, xq, 0);
          BAR();
          STAMP(2 + 3 * layer, 3);
          auto side = [&](int k) {
            if (k < 8) dmak(k, rs, w2s + 57344 + kq * 8192, kq * 8192, lane16);
            else if (k == 8) ldst(4);
            else fh[k - 9] = ld16(rs, lane16, hof(k - 9));
          };
          g1_chain<0>(acc, W, fr, xq, 0, 0, side);
          recheck(4, 0, [&]() {
#pragma unroll
            for (int k = 8; k < 16; ++k) side(k);
          });
          BAR();
          STAMP(2 + 3 * layer, 4);
          epi(4, [&]() {
            SCHED();
#pragma unroll
            for (int p = 7; p < 10; ++p) fh[p] = ld16(rs, lane16, hof(p));
            SCHED();
          });
#pragma unroll
          for (int p = 10; p < NHW; ++p) fh[p] = ld16(rs, lane16, hof(p));
          hcheck(0, 7);
          STAMP(2 + 3 * layer, 5);
          BAR();
        } else {
          if (!xfull<2>(jv)) xretry<2>(rs, jv, ja, js, 4096);
#pragma unroll
          for (int k = 0; k < 8; ++k) dmak(k, rs, xs + XS, 32768 + kq * 8192, lane16);
          if (job) {
            const u32x4 v0 = jv[0], v1 = jv[1];
            float r[8];
#pragma unroll
            for (int k = 0; k < 8; ++k) {
              float x = (k & 1) ? bfhi(v0[k >> 1]) : bflo(v0[k >> 1]), y = (k & 1) ? bfhi(v1[k >> 1]) : bflo(v1[k >> 1]);
              float s1 = x + y, s2 = x * x;
              asm volatile("" : "+v"(s1), "+v"(s2));
              s2 = fmaf(y, y, s2);
              r[k] = sw32(s1, s2);
              asm volatile("" : "+v"(r[k]));
            }
#pragma unroll
            for (int k = 0; k < 4; ++k) {
              const auto sw = __builtin_amdgcn_permlane16_swap(__float_as_uint(r[k]), __float_as_uint(r[4 + k]), false, false);
              float t = __uint_as_float(sw[0]) + __uint_as_float(sw[1]);
              t += dppf<0xB1>(t); t += dppf<0x4E>(t); t += dppf<0x141>(t); t += dppf<0x140>(t);
              r[k] = t;
            }
            const bool b0 = lane & 1, b1 = lane & 2;
            lds_st<float>(L_ST + 1024 + (lane >> 4) * 64 + (lane & 3) * 16 + kq * 4, b1 ? (b0 ? r[3] : r[2]) : (b0 ? r[1] : r[0]));
            WAITV(0);
            WAITL0();
            u32 cnt = 0;
            if (lane == 0) cnt = __hip_atomic_fetch_add((LDSP u32*)(size_t)(L_TH + 2628), 1u, __ATOMIC_RELAXED, __HIP_MEMORY_SCOPE_WORKGROUP);
            if ((__builtin_amdgcn_readfirstlane(cnt) & 3) == 3) {
              asm volatile("" ::: "memory");
              const f32x4 p = lds_ld<f32x4>(L_ST + 1024 + (lane & 15) * 16);
              float s = p[0] + p[1], q = p[2] + p[3];
              asm volatile("" : "+v"(s), "+v"(q));
              const float m = (s + q) * (1.f / SD);
              const float v = fmaxf(fmaf(-m, m, dppf<0x128>(m)), 0.f) + 1e-5f;
              st16(rs, lane < 8 ? g1_off + (Tj * 32 + (sl & 3) * 8 + lane) * 16 : OOB, (u32x4){__float_as_uint(-m), tag, __float_as_uint(v), tag});
            }
          }
          if (!job) WAITV(0);
          g1_pre(fr, xq, 32768);
          if (layer == 0) {
            const u32 a = L_CS + kq * 128 + r32 * 4;
            lds_st<float>(a, lds_ld<float>(a) + lds_ld<float>(a + 512));
            u32 e = EMPTY;
            asm volatile("" : "+v"(e));
            st16(rs, O_XT + LAY_SZ + b * 8192 + (tid & 255) * 16, (u32x4){e, e, e, e});
            st16(rs, O_XT + LAY_SZ + b * 8192 + 4096 + (tid & 255) * 16, (u32x4){e, e, e, e});
          }
          BAR();
          STAMP(2 + 3 * layer, 0);
          g1_chain<0>(acc, W, fr, xq, 32768, 0, [&](int k) {
            if (k < 8) dmak(k, rs, xs + 3 * XS, kq * 8192, lane16);
            else if (k == ST1_AT) ldst(1);
            if ((k == 3 || k == 7) && layer == 0) {
#pragma unroll
              for (int j = k - 3; j <= k; ++j) dmak(j, rs, w2s + kq * 14336, L_R3 + kq * 14336, lane16);
            }
          });
          recheck(1, 32768, nop);
          BAR();
          STAMP(2 + 3 * layer, 1);
          epi(1, nop);
          g1_pre(fr, xq, 0);
          if (layer == 0 && b == 0) st4(rs, tid == 256 ? O_EP : OOB, 0, tag);
          BAR();
          STAMP(2 + 3 * layer, 2);
          g1_chain<0>(acc, W, fr, xq, 0, 0, [&](int k) {
            if (k < 8) dmak(k, rs, xs + (ntile == 6 ? 5 : 4) * XS, 65536 + kq * 8192, lane16);
            else if (k == 8) ldst(3);
            else if (k < 15) ldWr(k - 9);
          });
          recheck(3, 0, nop);
          BAR();
          STAMP(2 + 3 * layer, 3);
          epi(3, nop);
          shift();
          g1_pre(fr, xq, 32768);
          BAR();
          STAMP(2 + 3 * layer, 4);
          ldst(ntile == 6 ? 5 : 3);
          u32 el = T2 < EARLY_T ? lane16 : OOB;
          asm volatile("" : "+v"(el), "=v"(fh[0]), "=v"(fh[1]), "=v"(fh[2]), "=v"(fh[3]), "=v"(fh[4]), "=v"(fh[5]), "=v"(fh[6]), "=v"(fh[7]));
          auto side = [&](int k) {
            if (k < 8) dmak(k, rs, w2s + 90112 + kq * 8192, 32768 + kq * 8192, lane16);
            else fh[k - 8] = ld16(rs, el, hof(k - 8));
          };
          if (ntile != 6) {
#pragma unroll
            for (int k = 0; k < 16; ++k) side(k);
            __builtin_amdgcn_s_waitcnt(0x0F70);
          } else {
            g1_chain<0>(acc, W, fr, xq, 32768, 0, side);
            recheck(5, 32768, [&]() {
#pragma unroll
              for (int k = 8; k < 16; ++k) side(k);
            });
          }
          WAITV(8);
          STAMP(2 + 3 * layer, 5);
          BAR();
          if (B_PRIO) __builtin_amdgcn_s_setprio(1);
          if (ntile != 6) hs = OOB;
          epi(5, nop);
          if (T2 >= EARLY_T) {
            if (T2 >= 55) delay(LATE_DELAY);
#pragma unroll
            for (int p = 0; p < 8; ++p) fh[p] = ld16(rs, lane16, hof(p));
          }
#pragma unroll
          for (int p = 8; p < NHW; ++p) fh[p] = ld16(rs, lane16, hof(p));
        }
      }
      STAMP(2 + 3 * layer, 6);
      f32x16 acc[2];
#pragma unroll
      for (int c = 0; c < 2; ++c)
#pragma unroll
        for (int e = 0; e < 16; ++e) acc[c][e] = 0.f;
      u32 wb0 = kq * 2048 + rot16, wb1 = wb0 + 65536, wb3 = wb0 + L_R3;
      asm volatile("" : "+v"(wb0), "+v"(wb1), "+v"(wb3));
      auto ldW = [&](int p, int c) {
        return lds_ld<bf16x8>((p < 10 ? wb3 + (p - 3) * 8192 : p < 18 ? wb0 + (p - 10) * 8192 : wb1 + (p - 18) * 8192) + c * 1024);
      };
      u16 rsd[8];
      const u32 rso = hh * 512 + r32 * 2, rss = xt_off + T2 * 32768 + (nt * 64 + kq * 16) * 64;
      SCHED();
#pragma unroll
      for (int p = 0; p < 22; ++p) {
        if (!A && p % 3 == 0 && p < 21) hcheck(p, p == 18 ? 22 : p + 3);
        const bf16x8 f = __builtin_bit_cast(bf16x8, fh[slot(p)]);
        if (p == 21 && kq >= 2) break;
        acc[0] = __builtin_amdgcn_mfma_f32_32x32x16_bf16(Wr[p % 3][0], f, acc[0], 0, 0, 0);
        SCHED();
        if (p == 15) { if constexpr (!A) BAR(); else WAITV(10); }
        if (p + 3 < 22) Wr[p % 3][0] = ldW(p + 3, 0);
        if (A && p < 8) dmak(p & 1, rs, w2s + 122880 + (p >> 1) * 8192 + kq * 2048, 65536 + (p >> 1) * 8192 + kq * 2048, lane16);
        if (p >= 11 && p < 15) rsd[2 * p - 22] = ld2(rs, rso + (2 * p - 22) * 64, rss);
        SCHED();
        acc[1] = __builtin_amdgcn_mfma_f32_32x32x16_bf16(Wr[p % 3][1], f, acc[1], 0, 0, 0);
        SCHED();
        if (p + 3 < 22) Wr[p % 3][1] = ldW(p + 3, 1);
        if (p + NHW < 22) fh[p] = ld16(rs, lane16, hof(p + NHW));
        if (p >= 11 && p < 15) rsd[2 * p - 21] = ld2(rs, rso + (2 * p - 21) * 64, rss);
        if (p == 8) {
          STAMP(3 + 3 * layer, 2);
          if (!A && B_PRIO) __builtin_amdgcn_s_setprio(0);
        }
        SCHED();
      }
      if (A && __builtin_expect(!none(acc[0][0] != acc[0][0]), 0)) {
#pragma unroll
        for (int e = 0; e < 16; ++e) acc[0][e] = acc[1][e] = 0.f;
#pragma unroll 1
        for (int ft = kq; ft < FT; ft += 4) {
          u32x4 v[1];
          const u32x4 wa = ld16(rs, rot16, O_W2F + (nt * FT + ft) * 2048), wb = ld16(rs, rot16 + 1024, O_W2F + (nt * FT + ft) * 2048);
          xload<1>(rs, v, lane16, h_off + (T2 * FT + ft) * 1024, 0);
          if (!xfull<1>(v)) xretry<1>(rs, v, lane16, h_off + (T2 * FT + ft) * 1024, 0);
          acc[0] = __builtin_amdgcn_mfma_f32_32x32x16_bf16(__builtin_bit_cast(bf16x8, wa), __builtin_bit_cast(bf16x8, v[0]), acc[0], 0, 0, 0);
          acc[1] = __builtin_amdgcn_mfma_f32_32x32x16_bf16(__builtin_bit_cast(bf16x8, wb), __builtin_bit_cast(bf16x8, v[0]), acc[1], 0, 0, 0);
        }
        RETRY(9);
      }
      STAMP(3 + 3 * layer, 3);
      if constexpr (A) BAR();
      const u32 pw = L_R3 + kq * 2048 + tt * 24576 + lane16;
#pragma unroll
      for (int q = 1; q < 4; ++q) {
        lds_st<f32x4>(pw + (q - 1) * 8192, (f32x4){acc[0][4 * q], acc[0][4 * q + 1], acc[0][4 * q + 2], acc[0][4 * q + 3]});
        lds_st<f32x4>(pw + (q - 1) * 8192 + 1024, (f32x4){acc[1][4 * q], acc[1][4 * q + 1], acc[1][4 * q + 2], acc[1][4 * q + 3]});
      }
#pragma unroll
      for (int e = 0; e < 4; ++e) { e4[e] = acc[0][e] + b2f(rsd[2 * e]); o4[e] = acc[1][e] + b2f(rsd[2 * e + 1]); }
      asm volatile("" : "+v"(e4), "+v"(o4));
    };
    asm volatile("" : "=v"(e4), "=v"(o4));
    if (w0 < 4) ffn(Role<true>{}); else ffn(Role<false>{});
    PHASE_IDS();
    {
      BAR();
      STAMP(3 + 3 * layer, 4);
#pragma unroll
      for (int q = 1; q < 4; ++q) {
        const u32 a = L_R3 + ((kq - q) & 3) * 2048 + tt * 24576 + lane16 + (q - 1) * 8192;
        e4 += lds_ld<f32x4>(a);
        o4 += lds_ld<f32x4>(a + 1024);
      }
      u32x4 o = {pkbf(e4[0], o4[0]), pkbf(e4[1], o4[1]), pkbf(e4[2], o4[2]), pkbf(e4[3], o4[3])};
      int last = layer;
      asm volatile("" : "+s"(last));
      if (last == 1) {
        *(u32x4*)(out + (size_t)(T2 * 32 + r32) * SD + nt * 64 + kq * 16 + hh * 8) = o;
        continue;
      }
      float s1 = 0.f, s2 = 0.f;
#pragma unroll
      for (int k = 0; k < 4; ++k) { s1 = dot2(o[k], 0x3f803f80u, s1); s2 = dot2(o[k], o[k], s2); }
      lds_st<float>(L_ST + tt * 1024 + lane16 + kq * 4, sw32(s1, s2));
      BAR();
      if (kq == 0) {
        const f32x4 p = lds_ld<f32x4>(L_ST + tt * 1024 + lane16);
        const float x = ((p[0] + p[1]) + (p[2] + p[3])) * (hh ? 0.0625f : 1.f), hi = floorf(x);
        const bool ok = fabsf(x) < 134217728.f;
        const u32 lo = ok ? ((u32)((x - hi) * 4294967296.f) & ~255u) | 1u : 0x11u;
        const int t = T2 * 32 + r32;
        __hip_atomic_fetch_add((GLBP u64*)(ws + O_S2 + hh * 16384 + ((t >> 1) & 1) * 8192 + (t >> 2) * 16 + (t & 1) * 8),
                               ((u64)(u32)(ok ? (int)hi : 0) << 32) | lo, __ATOMIC_RELAXED, __HIP_MEMORY_SCOPE_AGENT);
      }
      {
        const bool b0 = lane & 1, b1 = lane & 2;
        const u32 s0 = dppu<0xB1>(b0 ? o[0] : o[1]), s1_ = dppu<0xB1>(b0 ? o[2] : o[3]);
        const u32 p0 = b0 ? s0 : o[0], p1 = b0 ? o[1] : s0, p2 = b0 ? s1_ : o[2], p3 = b0 ? o[3] : s1_;
        const u32 t0 = dppu<0x4E>(b1 ? p0 : p2), t1 = dppu<0x4E>(b1 ? p1 : p3);
        o = (u32x4){b1 ? t0 : p0, b1 ? t1 : p1, b1 ? p2 : t0, b1 ? p3 : t1};
      }
      st16(rs, O_XP + (nt * 32 + kq * 8 + hh * 4 + (lane & 3)) * 8192 + (T2 * 32 + (r32 & ~3)) * 4, notempty(o));
    }
    if (S3_GATE && w == 4) st4(rs, lane == 0 ? O_F3 + b * 128 : OOB, 0, tag);
    STAMP(3, 5);
    {
      u32 kix[9];
      conv_prep(tid, w, kix);
      expand(tid, ch, kix);
    }
    delay(S3_DELAY + (n < 96 ? 2 * S3_STEP : n < 184 ? S3_STEP : 0));
    if (S3_GATE) {
      if (w < 4) poll(rs, O_F3 + tid * 128, 0, tag);
      BAR();
    }
    STAMP(3, 6);
    {
      u32x4 sv[4], xr;
      xload<4>(rs, sv, tid * 16, O_S2, 8192);
      xr = ld16(rs, tid * 16, O_XP + n * 8192);
      auto s3_ok = [&]() {
        u32 all = 8;
#pragma unroll
        for (int k = 0; k < 4; ++k) all &= sv[k][0] & sv[k][2];
        return none(all == 0 || umax4(0, xr) == EMPTY);
      };
      if (!s3_ok()) {
#pragma unroll 1
        for (int s = 0; s < SPIN_MAX; ++s) {
          asm volatile("" ::: "memory");
          u32x4 t[5];
#pragma unroll
          for (int k = 0; k < 4; ++k) t[k] = ld16(rs, (sv[k][0] & sv[k][2] & 8) ? OOB : tid * 16, O_S2 + k * 8192);
          t[4] = ld16(rs, umax4(0, xr) == EMPTY ? tid * 16 : OOB, O_XP + n * 8192);
#pragma unroll
          for (int k = 0; k < 4; ++k) sv[k] = (sv[k][0] & sv[k][2] & 8) ? sv[k] : t[k];
          xr = umax4(0, xr) == EMPTY ? t[4] : xr;
          if (s3_ok()) break;
          __builtin_amdgcn_s_sleep(4);
        }
      }
      u32 fl = 0;
#pragma unroll
      for (int k = 0; k < 4; ++k) fl |= sv[k][0] | sv[k][2];
      STAMP(3, 7);
      float s1[4], s2[4];
#pragma unroll
      for (int i = 0; i < 4; ++i) {
        s1[i] = fmaf((float)sv[i >> 1][2 * (i & 1)], 2.3283064365386963e-10f, (float)(int)sv[i >> 1][2 * (i & 1) + 1]);
        s2[i] = fmaf((float)sv[2 + (i >> 1)][2 * (i & 1)], 2.3283064365386963e-10f, (float)(int)sv[2 + (i >> 1)][2 * (i & 1) + 1]) * 16.f;
      }
      if (__builtin_expect(!none((fl & 0xf0) != 0), 0)) {
#pragma unroll
        for (int i = 0; i < 4; ++i) s1[i] = s2[i] = 0.f;
#pragma unroll 1
        for (int p = 0; p < 256; ++p) {
          u32x4 v;
#pragma unroll 1
          for (int s = 0; s < SPIN_MAX; ++s) {
            asm volatile("" ::: "memory");
            v = ld16(rs, tid * 16, O_XP + p * 8192);
            if (none(umax4(0, v) == EMPTY)) break;
          }
#pragma unroll
          for (int i = 0; i < 4; ++i) { s1[i] = dot2(v[i], 0x3f803f80u, s1[i]); s2[i] = dot2(v[i], v[i], s2[i]); }
        }
      }
      float mean[4], rstd[4];
#pragma unroll
      for (int i = 0; i < 4; ++i) {
        mean[i] = s1[i] * (1.f / SD);
        rstd[i] = __builtin_amdgcn_rsqf(fmaxf(fmaf(s2[i], 1.f / SD, -mean[i] * mean[i]), 0.f) + 1e-5f);
      }
      normalize(tid, xr, mean, rstd);
    }
    conv(Mode<1>{});
  }
  {
    STAMP(6, 5);
    u64 t;
    asm volatile("s_memrealtime %0\n\ts_waitcnt lgkmcnt(0)" : "=s"(t) :: "memory");
    const u32 until = (u32)t + (tl1 - tl0);
    for (int s = 0; s < 100000; ++s) {
      asm volatile("s_memrealtime %0\n\ts_waitcnt lgkmcnt(0)" : "=s"(t) :: "memory");
      if ((int)((u32)t - until) >= 0) break;
    }
  }
}

extern "C" void kernel_launch(void* const* d_in, const int* in_sizes, int n_in, void* d_out, int out_size,
                              void* d_ws, size_t ws_size, hipStream_t stream) {
  k_all<<<256, 512, 0, stream>>>((const u16*)d_in[0], (const u16*)d_in[1], (const u16*)d_in[2], (const u16*)d_in[3],
                                 (const u16*)d_in[4], (const u16*)d_in[5], (const u16*)d_in[6], (const u16*)d_in[8],
                                 (const u16*)d_in[9], (const u16*)d_in[10], (char*)d_ws, (u16*)d_out);
}
```

```cpp
#include <hip/hip_runtime.h>
#include <stdint.h>

typedef unsigned short u16;
typedef uint32_t u32;
typedef unsigned long long u64;
typedef __attribute__((ext_vector_type(2))) float f32x2;
typedef __attribute__((ext_vector_type(4))) float f32x4;
typedef __attribute__((ext_vector_type(16))) float f32x16;
typedef __attribute__((ext_vector_type(2))) _Float16 f16x2;
typedef __attribute__((ext_vector_type(8))) _Float16 f16x8;
typedef __attribute__((ext_vector_type(2))) __bf16 bf16x2;
typedef __attribute__((ext_vector_type(8))) __bf16 bf16x8;
typedef __attribute__((ext_vector_type(4))) short s16x4;
typedef __attribute__((ext_vector_type(2))) u32 u32x2;
typedef __attribute__((ext_vector_type(4))) u32 u32x4;
typedef __amdgpu_buffer_rsrc_t rsrc_t;
#define LDSP __attribute__((address_space(3)))
#define GLBP __attribute__((address_space(1)))
#define DEVI __device__ __forceinline__

constexpr int SL = 2048, SD = 512, NM = 128, DF = 1376, FT = 86;
constexpr int KCOPY = 2080;
constexpr int KCH_B = 2 * KCOPY * 2;
constexpr u32 O_EP = 0;
constexpr u32 O_F3 = 4096;
constexpr u32 O_G0 = O_F3 + 32768;
constexpr u32 O_G1 = O_G0 + 32768;
constexpr u32 O_S2 = O_G1 + 65536;
constexpr u32 O_W2F = O_S2 + 32768;
constexpr u32 H_SZ = 64 * FT * 1024;
constexpr u32 O_XT = O_W2F + 8 * FT * 2048 + 4096;
constexpr u32 O_H = O_XT + (2u << 20);
constexpr u32 O_XP = O_H + H_SZ;
constexpr u32 LAY_SZ = (4u << 20) + H_SZ;
constexpr u32 O_XG = O_H + LAY_SZ + (6u << 20);
constexpr u32 WS_END = O_XG + (4u << 20);
constexpr int L_Y = 0;
constexpr int TBP = 320, L_TB = 0, TB_W = 26 * TBP;
constexpr int L_K = 32768;
constexpr int L_U = L_K + 2 * KCH_B, U_CH = (240 + 2048) * 2;
constexpr int L_F = 91392;
constexpr int L_R3 = 98304;
constexpr int L_ST = 156928;
constexpr int L_CS = L_ST + 3072;
constexpr int L_TH = L_CS + 1024;
constexpr int L_RS = L_TH + 2640;
constexpr int L_SZ = 163840;
constexpr int SPIN_MAX = 1 << 16;
#define X_DELAY 0
#define CHAIN_PRIO 0
#define RAW0 8
#define RAW1 12
#define RAW2 16
#define S3_GATE 0
#define B_PRIO 1
#define B_AHEAD 4
#define T2_DELAY 6
#define B_MID 0
#define X_ROUND 2
#define GRAN_AT 2
#define HCK 0
#define ODD_DELAY 0
#define ST_A 4
#define WROT 0
#define CLR_E 0
#define CLR_M 0
#define LATE_DELAY 9
#define ST1_AT 8
#define WT_AT 4
#define NHW 12
#define EARLY_T 44
#define S3_STEP 2
#define ZEROW 1
#define PF_AT 1
#define PF128 0
#define TLBT 1
#define CPRE 1
#define R3_CONV 6
#define LAST_PK 0
#define S3_DELAY 8

DEVI float bflo(u32 v) { return __uint_as_float(v << 16); }
DEVI float bfhi(u32 v) { return __uint_as_float(v & 0xffff0000u); }
DEVI float b2f(u16 v) { return __uint_as_float(((u32)v) << 16); }
DEVI u32 pkbf(float a, float b) { bf16x2 r = __builtin_convertvector((f32x2){a, b}, bf16x2); return __builtin_bit_cast(u32, r); }
DEVI u32 pkf16(float a, float b) { f16x2 r = __builtin_convertvector((f32x2){a, b}, f16x2); return __builtin_bit_cast(u32, r); }
DEVI float rbf(float a) { return bflo(pkbf(a, 0.f)); }
template <int C> DEVI float dppf(float v) { return __uint_as_float(__builtin_amdgcn_update_dpp(0u, __float_as_uint(v), C, 0xf, 0xf, true)); }
template <int C> DEVI u32 dppu(u32 v) { return __builtin_amdgcn_update_dpp(0u, v, C, 0xf, 0xf, true); }
DEVI float sw16(float v) { auto r = __builtin_amdgcn_permlane16_swap(__float_as_uint(v), __float_as_uint(v), false, false); return __uint_as_float(r[0]) + __uint_as_float(r[1]); }
DEVI float sw32(float a, float b) { auto r = __builtin_amdgcn_permlane32_swap(__float_as_uint(a), __float_as_uint(b), false, false); return __uint_as_float(r[0]) + __uint_as_float(r[1]); }
DEVI float wsum(float v) { v += dppf<0xB1>(v); v += dppf<0x4E>(v); v += dppf<0x141>(v); v += dppf<0x140>(v); v = sw16(v); return sw32(v, v); }
DEVI float wmax(float v) {
  v = fmaxf(v, dppf<0xB1>(v)); v = fmaxf(v, dppf<0x4E>(v)); v = fmaxf(v, dppf<0x141>(v)); v = fmaxf(v, dppf<0x140>(v));
  auto r = __builtin_amdgcn_permlane16_swap(__float_as_uint(v), __float_as_uint(v), false, false);
  v = fmaxf(__uint_as_float(r[0]), __uint_as_float(r[1]));
  r = __builtin_amdgcn_permlane32_swap(__float_as_uint(v), __float_as_uint(v), false, false);
  return fmaxf(__uint_as_float(r[0]), __uint_as_float(r[1]));
}
DEVI float dot2(u32 a, u32 b, float c) { return __builtin_amdgcn_fdot2_f32_bf16(__builtin_bit_cast(bf16x2, a), __builtin_bit_cast(bf16x2, b), c, false); }

#define WAITV(n) asm volatile("s_waitcnt vmcnt(" #n ")" ::: "memory")
#define WAITL0() asm volatile("s_waitcnt lgkmcnt(0)" ::: "memory")
#define SCHED() __builtin_amdgcn_sched_barrier(0)
#define BAR() do { asm volatile("s_waitcnt lgkmcnt(0)\n\ts_barrier" ::: "memory"); } while (0)

#define STAMP(k, i)
#define RETRY(p)

DEVI u32x4 ld16(rsrc_t rs, u32 voff, u32 soff) { return __builtin_amdgcn_raw_buffer_load_b128(rs, voff, soff, 16); }
DEVI u32 ld4(rsrc_t rs, u32 voff, u32 soff) { return __builtin_amdgcn_raw_buffer_load_b32(rs, voff, soff, 16); }
DEVI u16 ld2(rsrc_t rs, u32 voff, u32 soff) { return __builtin_amdgcn_raw_buffer_load_b16(rs, voff, soff, 16); }
DEVI void st16(rsrc_t rs, u32 off, u32x4 v) { __builtin_amdgcn_raw_buffer_store_b128(v, rs, off, 0, 16); }
DEVI void st4(rsrc_t rs, u32 voff, u32 soff, u32 v) { __builtin_amdgcn_raw_buffer_store_b32(v, rs, voff, soff, 16); }
template <int K> DEVI void dma1(rsrc_t rs, u32 goff, u32 lds, u32 lane16) {
  const u32 g = goff + (K & ~3) * 1024;
  if constexpr ((K & 3) == 0)
    asm volatile("s_mov_b32 m0, %0\n\ts_nop 0\n\tbuffer_load_dwordx4 %1, %2, %3 offen sc1 lds"
                 :: "s"(lds + K * 1024), "v"(lane16), "s"(rs), "s"(g) : "memory");
  else
    asm volatile("buffer_load_dwordx4 %0, %1, %2 offen offset:%3 sc1 lds" :: "v"(lane16), "s"(rs), "s"(g), "i"((K & 3) * 1024) : "memory");
}
DEVI void dmak(int k, rsrc_t rs, u32 goff, u32 lds, u32 lane16) {
  switch (k) {
#define DK(n) case n: dma1<n>(rs, goff, lds, lane16); break;
    DK(0) DK(1) DK(2) DK(3) DK(4) DK(5) DK(6) DK(7) DK(8) DK(9) DK(10) DK(11) DK(12) DK(13)
#undef DK
  }
}
DEVI bool none(bool p) { return __builtin_amdgcn_ballot_w64(p) == 0; }
constexpr u32 OOB = 0x80000000u;
DEVI void poll(rsrc_t rs, u32 f, u32 voff, u32 soff, u32 tag) {
#pragma unroll 1
  for (int s = 0; s < SPIN_MAX; ++s) {
    if (none(f != tag)) break;
    asm volatile("" ::: "memory");
    f = ld4(rs, voff, soff);
  }
  asm volatile("" ::: "memory");
}
DEVI void poll(rsrc_t rs, u32 voff, u32 soff, u32 tag) {
  asm volatile("" ::: "memory");
  poll(rs, ld4(rs, voff, soff), voff, soff, tag);
}
constexpr u32 EMPTY = 0xffffffffu;
DEVI u32 umax4(u32 m, const u32x4& v) { return max(max(max(max(m, v[0]), v[1]), v[2]), v[3]); }
DEVI u32x4 notempty(u32x4 v) { return (u32x4){min(v[0], EMPTY - 1), min(v[1], EMPTY - 1), min(v[2], EMPTY - 1), min(v[3], EMPTY - 1)}; }
template <int N> DEVI void xload(rsrc_t rs, u32x4 (&v)[N], u32 voff, u32 soff, u32 stride) {
#pragma unroll
  for (int i = 0; i < N; ++i) v[i] = ld16(rs, voff, soff + i * stride);
}
template <int N> DEVI bool xfull(const u32x4 (&v)[N]) {
  u32 m = 0;
#pragma unroll
  for (int i = 0; i < N; ++i) m = umax4(m, v[i]);
  return none(m == EMPTY);
}
template <int N> DEVI void xretry(rsrc_t rs, u32x4 (&v)[N], u32 voff, u32 soff, u32 stride) {
#pragma unroll 1
  for (int s = 0; s < SPIN_MAX; ++s) {
    asm volatile("" ::: "memory");
    u32x4 t[N];
#pragma unroll
    for (int i = 0; i < N; ++i) t[i] = ld16(rs, umax4(0, v[i]) == EMPTY ? voff : OOB, soff + i * stride);
#pragma unroll
    for (int i = 0; i < N; ++i) v[i] = umax4(0, v[i]) == EMPTY ? t[i] : v[i];
    if (xfull<N>(v)) break;
    __builtin_amdgcn_s_sleep(4);
  }
}
template <int N> DEVI void xpark(rsrc_t rs, u32x4 (&v)[N], u32 voff, u32 soff, u32 stride, u32 lds) {
  if (!xfull<N>(v)) xretry<N>(rs, v, voff, soff, stride);
#pragma unroll
  for (int i = 0; i < N; ++i) *(LDSP u32x4*)(size_t)(lds + i * stride) = v[i];
}
DEVI void delay(int n) { for (int i = 0; i < n; ++i) __builtin_amdgcn_s_sleep(2); }

DEVI f32x2 cexp(float are, float aim, float p) {
  const float e = __builtin_amdgcn_exp2f(are * p), ph = __builtin_amdgcn_fractf(aim * p);
  return (f32x2){__builtin_amdgcn_cosf(ph), __builtin_amdgcn_sinf(ph)} * (f32x2){e, e};
}
DEVI f32x2 cmul(f32x2 a, f32x2 b) {
  f32x2 r;
  asm("v_pk_mul_f32 %0, %1, %2 op_sel:[0,0] op_sel_hi:[0,1]\n\t"
      "v_pk_fma_f32 %0, %1, %2, %0 op_sel:[1,1,0] op_sel_hi:[1,0,1] neg_lo:[1,0,0]" : "=&v"(r) : "v"(a), "v"(b));
  return r;
}
DEVI f16x8 cmul4(f32x4 a0, f32x4 a1, f32x4 b0, f32x4 b1) {
  f32x2 r0, r1, r2, r3;
  asm("v_pk_mul_f32 %0, %4, %8 op_sel:[0,0] op_sel_hi:[0,1]\n\tv_pk_mul_f32 %1, %5, %9 op_sel:[0,0] op_sel_hi:[0,1]\n\t"
      "v_pk_mul_f32 %2, %6, %10 op_sel:[0,0] op_sel_hi:[0,1]\n\tv_pk_mul_f32 %3, %7, %11 op_sel:[0,0] op_sel_hi:[0,1]\n\t"
      "v_pk_fma_f32 %0, %4, %8, %0 op_sel:[1,1,0] op_sel_hi:[1,0,1] neg_lo:[1,0,0]\n\t"
      "v_pk_fma_f32 %1, %5, %9, %1 op_sel:[1,1,0] op_sel_hi:[1,0,1] neg_lo:[1,0,0]\n\t"
      "v_pk_fma_f32 %2, %6, %10, %2 op_sel:[1,1,0] op_sel_hi:[1,0,1] neg_lo:[1,0,0]\n\t"
      "v_pk_fma_f32 %3, %7, %11, %3 op_sel:[1,1,0] op_sel_hi:[1,0,1] neg_lo:[1,0,0]"
      : "=&v"(r0), "=&v"(r1), "=&v"(r2), "=&v"(r3)
      : "v"(a0.xy), "v"(a0.zw), "v"(a1.xy), "v"(a1.zw), "v"(b0.xy), "v"(b0.zw), "v"(b1.xy), "v"(b1.zw));
  return __builtin_bit_cast(f16x8, (u32x4){pkf16(r0[0], r0[1]), pkf16(r1[0], r1[1]), pkf16(r2[0], r2[1]), pkf16(r3[0], r3[1])});
}
DEVI s16x4 trread(u32 a) { return __builtin_amdgcn_ds_read_tr16_b64_v4i16((LDSP s16x4*)(size_t)a); }
DEVI bf16x8 trfrag(u32 a) {
  const s16x4 lo = trread(a), hi = trread(a + 256);
  return __builtin_bit_cast(bf16x8, __builtin_shufflevector(lo, hi, 0, 1, 2, 3, 4, 5, 6, 7));
}
template <class T> DEVI T lds_ld(u32 a) { return *(const LDSP T*)(size_t)a; }
template <class T> DEVI void lds_st(u32 a, T v) { *(LDSP T*)(size_t)a = v; }

constexpr int PD = 4;
DEVI void g1_pre(bf16x8 (&fr)[PD], const u32 (&xq)[4], int toff) {
#pragma unroll
  for (int i = 0; i < PD; ++i) fr[i] = trfrag(xq[0] + toff + i * 1024);
}
template <int ST, class F>
DEVI float g1_chain(f32x16& acc, const bf16x8 (&W)[32], bf16x8 (&fr)[PD], const u32 (&xq)[4], int toff, u32 stdst, F side) {
#pragma unroll
  for (int e = 0; e < 16; ++e) acc[e] = 0.f;
  f32x4 q1 = {0.f, 0.f, 0.f, 0.f}, q2 = {0.f, 0.f, 0.f, 0.f};
  u32 one2 = 0x3f803f80u;
  if (ST) asm volatile("" : "+v"(one2));
  const s16x4 ones = __builtin_bit_cast(s16x4, (u32x2){one2, one2});
  auto fin = [&]() {
    const bool b0 = stdst & 16, b1 = stdst & 32;
    return sw32(q1[0], b1 ? (b0 ? q2[3] : q2[2]) : (b0 ? q2[1] : q2[0]));
  };
  if (CHAIN_PRIO) __builtin_amdgcn_s_setprio(1);
  SCHED();
#pragma unroll
  for (int i = 0; i < 32; ++i) {
    if (i < ST) {
      const u32x4 v = __builtin_bit_cast(u32x4, fr[i % PD]);
#pragma unroll
      for (int j = 0; j < 2; ++j) {
        const s16x4 x = __builtin_bit_cast(s16x4, (u32x2){v[2 * j], v[2 * j + 1]});
        q2 = __builtin_amdgcn_mfma_f32_4x4x4bf16_1k(x, x, q2, 0, 0, 0);
        q1 = __builtin_amdgcn_mfma_f32_4x4x4bf16_1k(ones, x, q1, 0, 0, 0);
      }
      SCHED();
    }
    acc = __builtin_amdgcn_mfma_f32_32x32x16_bf16(W[i], fr[i % PD], acc, 0, 0, 0);
    SCHED();
    if (ST > 0 && ST < 32 && i == ST + 1) lds_st<float>(stdst, fin());
    if (i + PD < 32) fr[i % PD] = trfrag(xq[(i + PD) >> 3] + toff + ((i + PD) & 7) * 1024);
    if (!(i & 1)) side(i >> 1);
    SCHED();
  }
  if (CHAIN_PRIO) __builtin_amdgcn_s_setprio(0);
  return ST == 32 ? fin() : 0.f;
}
template <int I> struct Idx { static constexpr int V = I; };
template <int I0, int I1, class F> DEVI void sfor(F f) {
  if constexpr (I0 < I1) { f(Idx<I0>{}); sfor<I0 + 1, I1>(f); }
}
constexpr int cv_lag(int st) { return st >= 24 ? st - 24 : (st & 1) ? 7 - ((st & 7) >> 1) : (st & 7) >> 1; }
template <bool V> struct Role { static constexpr bool A = V; };
template <int M> struct Mode { static constexpr int V = M; };
template <bool PK, class F> DEVI u32x4 g1_epi(const f32x16& acc, float nm, float v, u32 cssrc, F mid) {
  f32x4 cs[4];
#pragma unroll
  for (int q = 0; q < 4; ++q) cs[q] = lds_ld<f32x4>(cssrc + q * 16);
  const float rs = __builtin_amdgcn_rsqf(v);
  const float k0 = rs * -2.302208198f, k1 = rs * rs * rs * -0.102943239f;
  if constexpr (PK) {
    u32x4 o;
    f32x2 g[4], e[4];
#pragma unroll
    for (int p = 0; p < 4; ++p) {
      g[p] = __builtin_elementwise_fma((f32x2){cs[p >> 1][2 * (p & 1)], cs[p >> 1][2 * (p & 1) + 1]}, (f32x2){nm, nm}, (f32x2){acc[2 * p], acc[2 * p + 1]});
      const f32x2 t = g[p] * __builtin_elementwise_fma(g[p] * g[p], (f32x2){k1, k1}, (f32x2){k0, k0});
      e[p] = (f32x2){__builtin_amdgcn_exp2f(t[0]), __builtin_amdgcn_exp2f(t[1])};
    }
#pragma unroll
    for (int p = 0; p < 4; ++p) {
      const f32x2 d = __builtin_elementwise_fma(e[p], (f32x2){v, v}, (f32x2){v, v});
      const f32x2 uu = __builtin_elementwise_fma((f32x2){cs[2 + (p >> 1)][2 * (p & 1)], cs[2 + (p >> 1)][2 * (p & 1) + 1]}, (f32x2){nm, nm},
                                                 (f32x2){acc[8 + 2 * p], acc[9 + 2 * p]});
      const f32x2 r = g[p] * uu * (f32x2){__builtin_amdgcn_rcpf(d[0]), __builtin_amdgcn_rcpf(d[1])};
      o[p] = pkbf(r[0], r[1]);
    }
    return notempty(o);
  }
  u32x4 o;
#pragma unroll
  for (int q = 0; q < 2; ++q) {
    float g0 = cs[q][0], g1 = cs[q][1], g2 = cs[q][2], g3 = cs[q][3];
    float u0 = cs[2 + q][0], u1 = cs[2 + q][1], u2 = cs[2 + q][2], u3 = cs[2 + q][3];
    float t0, t1, t2, t3;
    asm("v_fma_f32 %0, %20, %0, %12\n\tv_fma_f32 %1, %20, %1, %13\n\tv_fma_f32 %2, %20, %2, %14\n\tv_fma_f32 %3, %20, %3, %15\n\t"
        "v_mul_f32 %8, %0, %0\n\tv_mul_f32 %9, %1, %1\n\tv_mul_f32 %10, %2, %2\n\tv_mul_f32 %11, %3, %3\n\t"
        "v_fma_f32 %8, %8, %22, %21\n\tv_fma_f32 %9, %9, %22, %21\n\tv_fma_f32 %10, %10, %22, %21\n\tv_fma_f32 %11, %11, %22, %21\n\t"
        "v_mul_f32 %8, %0, %8\n\tv_mul_f32 %9, %1, %9\n\tv_mul_f32 %10, %2, %10\n\tv_mul_f32 %11, %3, %11\n\t"
        "v_exp_f32 %8, %8\n\tv_exp_f32 %9, %9\n\tv_exp_f32 %10, %10\n\tv_exp_f32 %11, %11\n\t"
        "v_fma_f32 %4, %20, %4, %16\n\tv_fma_f32 %5, %20, %5, %17\n\tv_fma_f32 %6, %20, %6, %18\n\tv_fma_f32 %7, %20, %7, %19\n\t"
        "v_fma_f32 %8, %8, %23, %23\n\tv_fma_f32 %9, %9, %23, %23\n\tv_fma_f32 %10, %10, %23, %23\n\tv_fma_f32 %11, %11, %23, %23\n\t"
        "v_rcp_f32 %8, %8\n\tv_rcp_f32 %9, %9\n\tv_rcp_f32 %10, %10\n\tv_rcp_f32 %11, %11\n\t"
        "v_mul_f32 %0, %0, %4\n\tv_mul_f32 %1, %1, %5\n\tv_mul_f32 %2, %2, %6\n\tv_mul_f32 %3, %3, %7\n\t"
        "v_mul_f32 %0, %0, %8\n\tv_mul_f32 %1, %1, %9\n\tv_mul_f32 %2, %2, %10\n\tv_mul_f32 %3, %3, %11"
        : "+v"(g0), "+v"(g1), "+v"(g2), "+v"(g3), "+v"(u0), "+v"(u1), "+v"(u2), "+v"(u3), "=&v"(t0), "=&v"(t1), "=&v"(t2), "=&v"(t3)
        : "v"(acc[4 * q]), "v"(acc[4 * q + 1]), "v"(acc[4 * q + 2]), "v"(acc[4 * q + 3]),
          "v"(acc[8 + 4 * q]), "v"(acc[9 + 4 * q]), "v"(acc[10 + 4 * q]), "v"(acc[11 + 4 * q]), "v"(nm), "v"(k0), "v"(k1), "v"(v));
    o[2 * q] = pkbf(g0, g1);
    o[2 * q + 1] = pkbf(g2, g3);
    if (q == 0) mid();
  }
  return notempty(o);
}
#define MFMA_DRAIN(a) asm volatile("s_nop 15\n\ts_nop 3" : "+v"(a))

__global__ __launch_bounds__(512, 2) void k_all(const u16* __restrict__ u, const u16* __restrict__ logA,
                                                const u16* __restrict__ Aim, const u16* __restrict__ Cre,
                                                const u16* __restrict__ Cim, const u16* __restrict__ logdt,
                                                const u16* __restrict__ dsk, const u16* __restrict__ wg,
                                                const u16* __restrict__ wu, const u16* __restrict__ wd,
                                                char* ws, u16* __restrict__ out) {
  __shared__ __attribute__((aligned(16))) char sm[L_SZ];
  const int tid0 = threadIdx.x, w0 = __builtin_amdgcn_readfirstlane(tid0 >> 6), b = blockIdx.x;
  const int n0 = (b & 7) * 32 + (b >> 3);
  asm volatile("" :: "v"((LDSP char*)sm) : "memory");
  STAMP(0, 0);
  u32 pf;
  {
    u64 pc;
    u32 len;
    asm volatile("s_getpc_b64 %0\nKCODE_BEG:\n\ts_mov_b32 %1, KCODE_END-KCODE_BEG-4\n\t.subsection 1\nKCODE_END:\n\t.subsection 0" : "=s"(pc), "=s"(len));
    const u32 o = min((u32)(((b >> 3) + 32 * (tid0 & 15)) * 128), len);
    asm volatile("global_load_dword %0, %1, %2" : "=v"(pf) : "v"(o), "s"(pc) : "memory");
  }
  asm volatile("" :: "s"(u), "s"(logA), "s"(Aim), "s"(Cre), "s"(Cim), "s"(logdt), "s"(dsk), "s"(wg), "s"(wu), "s"(wd), "s"(ws), "s"(out));
  const rsrc_t rs = __builtin_amdgcn_make_buffer_rsrc(ws, 0, WS_END, 0x00020000);
  u32 map0;
#define PHASE_IDS()                                                                               \
  int tid = tid0, w = w0, n = n0;                                                                 \
  u32 tag = tag0, map = map0;                                                                     \
  asm volatile("" : "+v"(tid));                                                                   \
  asm volatile("" : "+s"(w), "+s"(n), "+s"(map), "+s"(tag));                                      \
  const int lane = tid & 63, r32 = lane & 31, hh = lane >> 5, ch = w >> 2;                        \
  const u32 lane16 = lane * 16;                                                                   \
  const int grp = map & 15, sl = map >> 4, Ta = min(grp, 10), ntile = grp < 9 ? 6 : grp < 11 ? 5 : 0; \
  const int ftw = sl * 4 + (w & 3);                                                               \
  const int nt = n & 7, tt = w >> 2, kq = w & 3, T2 = (n >> 3) + 32 * tt;                         \
  (void)r32; (void)hh; (void)ch; (void)lane16; (void)ftw; (void)nt; (void)kq; (void)T2; (void)Ta; (void)ntile

  u32 tag0, dsk2;
  float ks0, ks1;
  float base[2][4];
  u32 kb, ub;
  auto normalize = [&](int tid, const u32x4& xr, const float (&mean)[4], const float (&rstd)[4]) {
    const f32x2 d = {bflo(dsk2), bfhi(dsk2)};
    f32x2 un[4];
#pragma unroll
    for (int i = 0; i < 4; ++i) {
      const f32x2 x = {bflo(xr[i]), bfhi(xr[i])};
      un[i] = (x - mean[i]) * rstd[i];
      const f32x2 t = __builtin_elementwise_fma(un[i], d, x);
      base[0][i] = t[0]; base[1][i] = t[1];
    }
    lds_st<u32x2>(L_U + 480 + tid * 8, (u32x2){pkf16(un[0][0], un[1][0]), pkf16(un[2][0], un[3][0])});
    lds_st<u32x2>(L_U + U_CH + 480 + tid * 8, (u32x2){pkf16(un[0][1], un[1][1]), pkf16(un[2][1], un[3][1])});
  };
  auto conv_prep = [&](int tid, int w, u32 (&kix)[9]) {
    const int i = tid & 255, ch = w >> 2;
    const int sh = min(__builtin_clz((u32)(255 - i) | 16) - 24, 3);
    const float sc = __uint_as_float((124 + sh) << 23);
    float l0 = (float)(2047 - 8 * i);
    asm volatile("" : "+v"(l0));
    const float mg = 8388608.f + (float)(384 - 128 * sh);
#pragma unroll
    for (int j = 0; j < 9; ++j) kix[j] = (__float_as_uint(fmaf(l0 - (float)j, sc, mg)) << 1) + (u32)(L_TH + ch * 1312 - 0x96000000u);
    asm volatile("" : "+v"(kix[8]));
    kix[8] -= i == 223 ? 2 : 0;
    kix[8] = i == 255 ? L_TH + 2624 : kix[8];
#pragma unroll
    for (int j = 0; j < 9; ++j) asm volatile("" : "+v"(kix[j]));
    const int r = tid & 15, h = (tid >> 4) & 3;
    kb = L_K + ((w >> 1) & 1) * KCH_B + 4 * (((2031 - r + 8 * h) >> 1) + ((r & 1) ? 0 : KCOPY / 2)) - 256 * (w & 1) - 64 * 11;
    ub = L_U + ((w >> 1) & 1) * U_CH + 2 * (224 + 16 * r + 8 * h) - 256 * (w & 1) - 192;
    asm volatile("" : "+v"(kb), "+v"(ub));
  };
  auto expand = [&](int tid, int ch, const u32 (&kix)[9]) {
    u32 z = 0;
    asm volatile("" : "+v"(z));
    if (tid < 76)
      lds_st<u32x4>(tid < 60 ? L_U + (tid >= 30 ? U_CH + (tid - 30) * 16 : tid * 16) : L_K + ((tid - 60) >> 2) * KCOPY * 2 + 4096 + (tid & 3) * 16,
                    (u32x4){z, z, z, z});
    const int i = tid & 255;
    u32 kv[9];
#pragma unroll
    for (int j = 0; j < 9; ++j) kv[j] = lds_ld<u16>(kix[j]);
    lds_st<u32x4>(L_K + ch * KCH_B + i * 16,
                  (u32x4){kv[0] | (kv[1] << 16), kv[2] | (kv[3] << 16), kv[4] | (kv[5] << 16), kv[6] | (kv[7] << 16)});
    lds_st<u32x4>(L_K + ch * KCH_B + i * 16 + KCOPY * 2,
                  (u32x4){kv[1] | (kv[2] << 16), kv[3] | (kv[4] << 16), kv[5] | (kv[6] << 16), kv[7] | (kv[8] << 16)});
  };
  bf16x8 W[32];
  rsrc_t wrs;
  u32 wvo, ra0;
  auto w_put = [&](rsrc_t src, u32 vo, int mat, int r, int j) {
    asm volatile("s_mov_b32 m0, %0\n\ts_nop 0\n\tbuffer_load_dwordx4 %1, %2, %3 offen lds"
                 :: "s"(L_F + j * 8192 + (w0 & 2) * 2048 + mat * 2048 + (w0 & 1) * 1024), "v"(vo), "s"(src),
                    "s"(((j >> 1) * 8 + 4 * r + 2 * (j & 1)) * (16 * DF * 2)) : "memory");
  };
  auto w_get = [&](int r) {
#pragma unroll
    for (int g = 0; g < 4; ++g) {
      u32 a = ra0 + ((g + w0) & 3) * 16384;
      asm volatile("" : "+v"(a));
#pragma unroll
      for (int j = 0; j < 4; ++j) {
        const s16x4 lo = trread(a + j * 4096), hi = trread(a + j * 4096 + 512);
        W[g * 8 + 4 * r + j] = __builtin_bit_cast(bf16x8, __builtin_shufflevector(lo, hi, 0, 1, 2, 3, 4, 5, 6, 7));
      }
    }
  };
  {
    const int tid = tid0, w = w0, n = n0, lane = tid & 63, r32 = lane & 31, hh = lane >> 5, ch = w >> 2, kq = w & 3;
    const u32 lane16 = lane * 16;
    const int pidx = n * 2 * NM + w * 32 + r32;
    const u16 pla = logA[pidx], pai = Aim[pidx], pcr = Cre[pidx], pci = Cim[pidx];
    const u32 car = *(const u32*)(Cre + (n * 2 + ch) * NM + lane * 2), cai = *(const u32*)(Cim + (n * 2 + ch) * NM + lane * 2);
    const u32x4 til = *(const u32x4*)(u + (size_t)((b >> 2) * 32 + r32) * SD + (b & 3) * 128 + (w * 2 + hh) * 8);
    const u32x4 row = *(const u32x4*)(u + (size_t)(4 * (8 * (b >> 2) + w) + (b & 3)) * SD + lane * 8);
    const u32 ep = ld4(rs, O_EP, 0);
    int vz = 0;
    asm volatile("" : "+v"(vz));
    const u16 ldt = logdt[2 * n + ch + vz];
    const u32 dskv = ((const u32*)dsk)[n + vz];
    SCHED();
    __builtin_amdgcn_s_barrier();
    SCHED();
    st16(rs, O_XT + b * 8192 + tid * 16, (u32x4){EMPTY, EMPTY, EMPTY, EMPTY});
    asm volatile("" ::: "memory");
    u32 pf2;
    const rsrc_t pfr = __builtin_amdgcn_make_buffer_rsrc(w < 2 ? (void*)wg : w < 4 ? (void*)wu : w < 6 ? (void*)wd : w == 6 ? (void*)ws : (void*)out, 0,
                                                         w < 6 ? SD * DF * 2 : !TLBT ? 0 : w == 6 ? WS_END : SL * SD * 2, 0x00020000);
    auto wpre = [&]() {
      pf2 = __builtin_amdgcn_raw_buffer_load_b32(pfr, w < 6 ? (lane < 43 ? lane * 64 : OOB) : lane << 19,
                                                 w < 6 ? (b * 2 + (w & 1)) * 2752 : 2048 + ((b >> 3) & 7) * 65536, 0);
    };
    if (PF_AT == 0) wpre();
    {
      const int pe = (n >> 6) * 30 + (n & 31), pos = pe / 11, sl = pe - pos * 11 + ((n >> 5) & 1) * 11;
      const int grp = (n & 31) >= (n >= 192 ? 31 : 30) ? 11 : (int)((0x87a65439210ull >> (4 * pos)) & 15);
      map0 = grp | (sl << 4);
      asm volatile("" : "+s"(map0));
      wrs = __builtin_amdgcn_make_buffer_rsrc((void*)(ch ? wu : wg), 0, SD * DF * 2, 0x00020000);
      wvo = (kq * 8 + (lane >> 3)) * (DF * 2) + sl * 128 + ((((lane & 7) >> 1) ^ (((lane >> 3) & 2) + ch)) * 32) + (lane & 1) * 16;
      if (ZEROW && (grp > 10 || (sl == 21 && (((lane >> 1) ^ (lane >> 3)) & 2)))) wvo = OOB;
      const int mat = (lane >> 4) & 1, rq = (lane & 15) >> 2;
      ra0 = L_F + mat * 2048 + (8 * hh + rq) * 128 + ((kq ^ ((rq & 2) + mat)) * 32) + (lane & 3) * 8;
      asm volatile("" : "+v"(ra0), "+v"(wvo));
    }
    u32 kix[9];
    conv_prep(tid, w, kix);
    const bool odd = lane & 1;
    u32 xgo = O_XG + ((b & 3) * 64 + (w * 2 + hh) * 4 + (odd ? 2 : 0)) * 16384 + ((r32 >> 1) & 1) * 8192 + ((b >> 2) * 8 + (r32 >> 2)) * 16;
    const u32 tb0 = L_TB + w * TB_W + r32 * 8;
    u32 tb = tb0 + hh * 13 * TBP, tbe = tb0 + (hh ? 18 : 12) * TBP, tbf = tb0 + (hh ? 19 : 5) * TBP, tbg = tb0 + (hh ? 20 : 25) * TBP,
        tb1 = tb0 + (hh ? 17 : 0) * TBP;
    float pa = hh ? 4.f : 1.f, pb = hh ? 64.f : 256.f, pe = hh ? 16.f : 2044.f, sg = hh ? -1.f : 1.f, sc = hh ? 16384.f : 1.f;
    u32 aL = L_TB + w * TB_W + (lane >> 4) * 32 + (lane & 3) * TBP, aM = L_TB + w * TB_W + (lane >> 4) * 32 + ((lane & 15) >> 2) * TBP;
    u32 a2x = (lane & 15) == 4 ? aL + 4 * TBP : aL;
    asm volatile("" : "+v"(xgo), "+v"(tb), "+v"(tbe), "+v"(tbf), "+v"(tbg), "+v"(tb1), "+v"(pa), "+v"(pb), "+v"(pe), "+v"(sg), "+v"(sc));
    asm volatile("" : "+v"(aL), "+v"(aM), "+v"(a2x));
    lds_st<f32x2>(tb0 + 13 * TBP, (f32x2){16384.f, 0.f});
    if (tid == 0) lds_st<u32x2>(L_TH + 2624, (u32x2){0, 0});
    SCHED();
    const u32 tag = tag0 = __builtin_amdgcn_readfirstlane(ep) + 1u;
    STAMP(0, 1);
    {
      const u32 r0 = dppu<0xB1>(odd ? til[0] : til[2]), r1 = dppu<0xB1>(odd ? til[1] : til[3]);
      st16(rs, xgo, (u32x4){odd ? r0 : til[0], tag, odd ? til[2] : r0, tag});
      st16(rs, xgo + 16384, (u32x4){odd ? r1 : til[1], tag, odd ? til[3] : r1, tag});
      float s1 = 0.f, s2 = 0.f;
#pragma unroll
      for (int c = 0; c < 4; ++c) { s1 = dot2(row[c], 0x3f803f80u, s1); s2 = dot2(row[c], row[c], s2); }
      float s = sw32(s1, s2);
      s += dppf<0xB1>(s); s += dppf<0x4E>(s); s += dppf<0x141>(s); s += dppf<0x140>(s);
      s = sw16(s);
      if (r32 == 0) lds_st<float>(L_RS + w * 8 + hh * 4, s);
    }
    if (PF_AT == 1) { __builtin_amdgcn_s_barrier(); asm volatile("" ::: "memory"); wpre(); }
    if (PF_AT == 2) { asm volatile("" ::: "memory"); wpre(); }
    STAMP(0, 2);
    {
      const float dt = rbf(__expf(b2f(ldt)));
      const float ar = -rbf(__expf(b2f(pla))) * dt * 1.44269504088896f, ai = (b2f(pai) * dt) * 0.159154943091895f, aj = ai * sg;
      const int ce = __builtin_amdgcn_frexp_expf(wmax(fmaxf(fmaxf(fabsf(bflo(car)), fabsf(bfhi(car))), fmaxf(fabsf(bflo(cai)), fabsf(bfhi(cai))))));
      if (lane == 0) lds_st<float>(L_RS + 64 + ch * 4, ldexpf(dt, ce - 7));
      const f32x2 a = cexp(ar, ai, pa), bb = cexp(ar, aj, pb);
      const f32x2 e = cexp(ar, aj, pe), e1k = cexp(ar, ai, 1024.f);
      const f32x2 d = hh ? (f32x2){ldexpf(b2f(pcr), 14 - ce), ldexpf(-b2f(pci), 14 - ce)} : e1k;
      const f32x2 as = a * sc, a2 = cmul(as, a), b2 = cmul(bb, bb), b3 = cmul(b2, bb);
      const f32x2 f = cmul(hh ? e : bb, hh ? e : a2);
      lds_st<f32x2>(tb + 1 * TBP, as);
      lds_st<f32x2>(tb + 2 * TBP, a2);
      lds_st<f32x2>(tb + 3 * TBP, cmul(a2, a));
      lds_st<f32x2>(tb + 4 * TBP, bb);
      lds_st<f32x2>(tb + 6 * TBP, b2);
      lds_st<f32x2>(tb + 7 * TBP, b3);
      lds_st<f32x2>(tb + 8 * TBP, d);
      lds_st<f32x2>(tb + 9 * TBP, cmul(d, bb));
      lds_st<f32x2>(tb + 10 * TBP, cmul(d, b2));
      lds_st<f32x2>(tb + 11 * TBP, cmul(d, b3));
      asm volatile("" ::: "memory");
      lds_st<f32x2>(tbe, e);
      lds_st<f32x2>(tbf, f);
      lds_st<f32x2>(tbg, cmul(f, e));
      lds_st<f32x2>(tb1, (f32x2){1.f, 0.f});
    }
    asm volatile("" :: "v"(pf));
    dsk2 = __builtin_amdgcn_readfirstlane(dskv);
    asm volatile("" :: "v"(dskv));
    WAITV(3);
    BAR();
    if (w == 3) {
      const f32x2 v = lds_ld<f32x2>(L_RS + (lane & 7) * 8) * (1.f / SD);
      const float r = __builtin_amdgcn_rsqf(fmaxf(fmaf(-v[0], v[0], v[1]), 0.f) + 1e-5f);
      st16(rs, lane < 8 ? O_G0 + (b & 3) * 8192 + ((b >> 2) * 8 + lane) * 16 : OOB, (u32x4){__float_as_uint(v[0]), tag, __float_as_uint(r), tag});
      st16(rs, lane < 8 ? O_S2 + b * 128 + lane16 : OOB, (u32x4){0, 0, 0, 0});
    }
    STAMP(0, 3);
    u32x4 g[6];
    auto gran_loads = [&]() {
#pragma unroll
      for (int i = 0; i < 2; ++i) g[4 + i] = ld16(rs, tid * 16, O_XG + n * 16384 + i * 8192);
#pragma unroll
      for (int i = 0; i < 4; ++i) g[i] = ld16(rs, tid * 16, O_G0 + i * 8192);
    };
    {
      f32x4 a0 = {0, 0, 0, 0}, a1 = {0, 0, 0, 0}, a2 = {0, 0, 0, 0};
      f32x4 tv[2][12];
#pragma unroll
      for (int s = 0; s < 2; ++s) {
        auto L = [&](int i, u32 a) { tv[s][i] = lds_ld<f32x4>(a + s * 128); tv[s][i + 1] = lds_ld<f32x4>(a + s * 128 + 16); };
        L(0, aL + 17 * TBP); L(2, aM + 21 * TBP); L(4, aM + 13 * TBP); L(6, aL); L(8, aL + 4 * TBP); L(10, a2x + 8 * TBP);
      }
#pragma unroll
      for (int s = 0; s < 2; ++s) {
        const f32x4* t = tv[s];
        SCHED(); w_put(wrs, wvo, ch, 0, 4 * s + 0); SCHED();
        const f16x8 bb = cmul4(t[0], t[1], t[2], t[3]);
        SCHED(); w_put(wrs, wvo, ch, 0, 4 * s + 1); SCHED();
        a0 = __builtin_amdgcn_mfma_f32_16x16x32_f16(cmul4(t[6], t[7], t[4], t[5]), bb, a0, 0, 0, 0);
        SCHED(); w_put(wrs, wvo, ch, 0, 4 * s + 2); SCHED();
        a1 = __builtin_amdgcn_mfma_f32_16x16x32_f16(cmul4(t[8], t[9], t[4], t[5]), bb, a1, 0, 0, 0);
        SCHED(); w_put(wrs, wvo, ch, 0, 4 * s + 3); SCHED();
        a2 = __builtin_amdgcn_mfma_f32_16x16x32_f16(cmul4(t[10], t[11], t[4], t[5]), bb, a2, 0, 0, 0);
      }
      const u32 T = L_TB + w * TB_W + lane16;
      lds_st<f32x4>(T, a0);
      lds_st<f32x4>(T + 1024, a1);
      lds_st<f32x4>(T + 2048, a2);
    }
    if (GRAN_AT == 1) gran_loads();
    STAMP(0, 4);
    BAR();
    if (GRAN_AT == 2) gran_loads();
    if (kq < 3) {
      const u32 T = L_TB + ch * 4 * TB_W + kq * 1024 + lane16;
      const f32x4 v = ((lds_ld<f32x4>(T) + lds_ld<f32x4>(T + TB_W)) + (lds_ld<f32x4>(T + 2 * TB_W) + lds_ld<f32x4>(T + 3 * TB_W))) * 9.5367431640625e-07f;
      const u32 p0 = pkf16(v[0], v[1]), p1 = pkf16(v[2], v[3]);
      const int j = lane & 15, h = lane >> 4;
      const u32 H = L_TH + ch * 1312;
      if (kq == 0) {
        lds_st<u32x2>(H + 2 * (16 * j + 4 * h), (u32x2){p0, p1});
      } else if (kq == 1) {
        lds_st<u32>(H + 2 * (256 + 8 * j + 2 * h), p0);
        lds_st<u16>(H + 2 * (384 + 4 * j + h), (u16)p1);
        lds_st<u16>(H + 2 * (448 + 4 * j + h), (u16)(p1 >> 16));
      } else if (!(h & 1)) {
        const u32 a = H + 2 * (512 + 2 * j + (h >> 1));
        lds_st<u16>(a, (u16)p0);
        lds_st<u16>(a + 64, (u16)(p0 >> 16));
        lds_st<u16>(a + 128, (u16)p1);
        lds_st<u16>(a + 192, (u16)(p1 >> 16));
      } else if (lane == 16) {
        lds_st<u16>(H + 2 * 640, (u16)p0);
      }
    }
    {
      const f32x2 k2 = lds_ld<f32x2>(L_RS + 64);
      ks0 = __uint_as_float(__builtin_amdgcn_readfirstlane(__float_as_uint(k2[0])));
      ks1 = __uint_as_float(__builtin_amdgcn_readfirstlane(__float_as_uint(k2[1])));
    }
    BAR();
    STAMP(0, 5);
#pragma unroll
    for (int k = 0; k < CLR_M; ++k) st16(rs, O_H + b * 30208 + k * 8192 + tid * 16, (u32x4){EMPTY, EMPTY, EMPTY, EMPTY});
    expand(tid, ch, kix);
    asm volatile("" : "+v"(g[0]), "+v"(g[1]), "+v"(g[2]), "+v"(g[3]), "+v"(g[4]), "+v"(g[5]) :: "memory");
    asm volatile("" :: "v"(pf2));
    STAMP(0, 6);
    auto gran_ok = [&]() {
      u32 bad = 0;
#pragma unroll
      for (int k = 0; k < 6; ++k) bad |= (g[k][1] ^ tag) | (g[k][3] ^ tag);
      return none(bad != 0);
    };
    if (!gran_ok()) {
#pragma unroll 1
      for (int s = 0; s < SPIN_MAX; ++s) {
        asm volatile("" ::: "memory");
        gran_loads();
        if (gran_ok()) break;
      }
    }
    {
      float mean[4], rstd[4];
#pragma unroll
      for (int k = 0; k < 4; ++k) { mean[k] = __uint_as_float(g[k][0]); rstd[k] = __uint_as_float(g[k][2]); }
      normalize(tid, (u32x4){g[4][0], g[4][2], g[5][0], g[5][2]}, mean, rstd);
    }
    STAMP(0, 7);
  }

  auto conv = [&](auto mode) {
    constexpr int layer = decltype(mode)::V;
    BAR();
    if (layer == 0) {
      w_get(0);
      BAR();
    }
    STAMP(1 + 3 * layer, 0);
    if (w0 < 4) {
      constexpr int NA = 6;
      f32x4 acc[8];
#pragma unroll
      for (int g = 0; g < 8; ++g) acc[g] = (f32x4){0, 0, 0, 0};
      u32 kp[4];
#pragma unroll
      for (int p = 0; p < 4; ++p) {
        kp[p] = kb - 1024 * p;
        asm volatile("" : "+v"(kp[p]));
      }
      u32x4 A[NA];
      f16x8 B[8];
      auto ldA = [&](auto st_, auto half_) {
        constexpr int st = decltype(st_)::V, half = decltype(half_)::V, a = cv_lag(st);
        if constexpr (st < 32) {
          const u32 p = kp[a >> 1] + 64 * (11 - (8 * (a & 1) + (st >> 3))) + half * 8;
          A[st % NA][2 * half] = lds_ld<u32>(p);
          A[st % NA][2 * half + 1] = lds_ld<u32>(p + 4);
        }
      };
      auto ldB = [&](int rr, int q) { return lds_ld<f16x8>(ub + 64 * (8 * q + 3 - rr)); };
      ldA(Idx<0>{}, Idx<0>{});
      ldA(Idx<0>{}, Idx<1>{});
#pragma unroll
      for (int q = 7; q >= 0; --q) B[q] = ldB(0, q);
      ldA(Idx<1>{}, Idx<0>{});
      ldA(Idx<1>{}, Idx<1>{});
      u32 yb = tid0;
      asm volatile("" : "+v"(yb));
      yb = L_Y + w0 * 8192 + ((yb & 48) + ((yb & 15) ^ ((yb >> 2) & 12))) * 16;
      asm volatile("" : "+v"(yb));
      SCHED();
      sfor<0, 32>([&](auto st_) {
        constexpr int st = decltype(st_)::V, a = cv_lag(st), rr = st >> 3;
        sfor<0, 8 - a>([&](auto j_) {
          constexpr int j = decltype(j_)::V, q = 7 - a - j;
          acc[a + q] = __builtin_amdgcn_mfma_f32_16x16x32_f16(__builtin_bit_cast(f16x8, A[st % NA]), B[q], acc[a + q], 0, 0, 0);
          SCHED();
          if constexpr (st == 0) ldA(Idx<2 + (j >> 1)>{}, Idx<(j & 1)>{});
          if constexpr ((st & 7) == 7 && rr < 3) B[q] = ldB(rr + 1, q);
          if constexpr (!(st & 1) && st < 24 && j == 0) B[7 - a] = ldB(rr + 1, 7 - a);
          if constexpr (!(st & 1) && st >= 2 && st <= 24 && j >= 1 && j <= 4) ldA(Idx<st - 2 + ((j - 1) >> 1) + NA>{}, Idx<((j - 1) & 1)>{});
          if constexpr (st > 24 && j < 2) ldA(Idx<st - 1 + NA>{}, Idx<(j & 1)>{});
          if constexpr (st > 24 && j == (7 - a >= 2 ? 2 : 7 - a)) lds_st<f32x4>(yb + (a - 1) * 1024, acc[a - 1]);
          SCHED();
        });
      });
      lds_st<f32x4>(yb + 7 * 1024, acc[7]);
      if (layer == 0) WAITV(0);
    } else {
      u32 ce = EMPTY, t8 = tid0;
      asm volatile("" : "+v"(ce), "+v"(t8));
      t8 = (t8 & 255) * 16;
      if (layer == 0) {
        const rsrc_t wr0 = __builtin_amdgcn_make_buffer_rsrc((void*)wg, 0, SD * DF * 2, 0x00020000);
        const u32 wv0 = wvo ^ 32;
        const int ft = b >> 1, nb = (b & 1) * 4 + (w0 & 3);
        const rsrc_t wr = __builtin_amdgcn_make_buffer_rsrc((void*)wd, 0, SD * DF * 2, 0x00020000);
        u32 wt[8];
#pragma unroll
        for (int j = 0; j < 8; ++j)
          wt[j] = __builtin_amdgcn_raw_buffer_load_b32(wr, (t8 & 512) * 8 + ((t8 >> 2) & 124) + (j & 3) * (SD * 2),
                                                       (ft * 16 + 8 * (j >> 2)) * (SD * 2) + nb * 128, 0);
#pragma unroll
        for (int j = 2 * CLR_M; j < 4; ++j) st16(rs, O_H + b * 30208 + j * 4096 + t8, (u32x4){ce, ce, ce, ce});
        u32x4 lo, hi;
#pragma unroll
        for (int j = 0; j < 8; ++j) {
          if (j == WT_AT) {
#pragma unroll
            for (int i = 0; i < 4; ++i) {
              lo[i] = __builtin_amdgcn_perm(wt[2 * i + 1], wt[2 * i], 0x05040100u);
              hi[i] = __builtin_amdgcn_perm(wt[2 * i + 1], wt[2 * i], 0x07060302u);
            }
            const u32 wo = (ft < FT ? O_W2F + (nb * FT + ft) * 2048 : OOB) + (t8 & 1023);
            st16(rs, wo, lo);
            st16(rs, wo + 1024, hi);
          }
          w_put(wr0, wv0, 0, 1, j);
          w_put(wrs, wvo, 1, 1, j);
          if (j & 1) st16(rs, (j < 7 || t8 < 1536 ? O_H + b * 30208 + (4 + (j >> 1)) * 4096 : OOB) + t8, (u32x4){ce, ce, ce, ce});
        }
      } else {
#pragma unroll
        for (int j = 0; j < 6; ++j) st16(rs, (j < 5 || t8 < 1536 ? O_H + LAY_SZ + b * 22016 + j * 4096 : OOB) + t8, (u32x4){ce, ce, ce, ce});
        if (R3_CONV) {
          int ws_ = w0 & 3, nn = n0 & 7;
          asm volatile("" : "+s"(ws_), "+s"(nn));
#pragma unroll
          for (int j = 0; j < R3_CONV; ++j) dmak(j, rs, O_W2F + (nn * FT + 12) * 2048 + ws_ * (R3_CONV * 1024), L_R3 + ws_ * (R3_CONV * 1024), t8 & 1023);
        }
      }
      if (layer == 0) WAITV(0); else asm volatile("s_waitcnt vmcnt(%0)" :: "i"(R3_CONV) : "memory");
    }
    STAMP(1 + 3 * layer, 1);
    {
      PHASE_IDS();
      const int c = lane >> 2, h = lane & 3;
      u32 y = L_Y + (64 * w + 16 * h + (c ^ (4 * h))) * 16;
      u32 xo = O_XT + layer * LAY_SZ + n * 128 + (tid >> 3) * 32768 + (tid & 1) * 64 + ((tid >> 1) & 3) * 16;
      asm volatile("" : "+v"(y), "+v"(xo));
      BAR();
      STAMP(1 + 3 * layer, 2);
      const f32x4 y0 = lds_ld<f32x4>(y) + lds_ld<f32x4>(y + 8192), y1 = lds_ld<f32x4>(y + 16384) + lds_ld<f32x4>(y + 24576);
      const u32 a0 = pkbf(fmaf(y0[0], ks0, base[0][0]), fmaf(y0[1], ks0, base[0][1]));
      const u32 a1 = pkbf(fmaf(y0[2], ks0, base[0][2]), fmaf(y0[3], ks0, base[0][3]));
      const u32 c0 = pkbf(fmaf(y1[0], ks1, base[1][0]), fmaf(y1[1], ks1, base[1][1]));
      const u32 c1 = pkbf(fmaf(y1[2], ks1, base[1][2]), fmaf(y1[3], ks1, base[1][3]));
      const bool odd = lane & 1;
      const u32 r0 = dppu<0xB1>(odd ? a0 : c0), r1 = dppu<0xB1>(odd ? a1 : c1);
      st16(rs, xo, notempty((u32x4){odd ? r0 : a0, odd ? r1 : a1, odd ? c0 : r0, odd ? c1 : r1}));
      STAMP(1 + 3 * layer, 3);
      if (layer == 0) {
        w_get(1);
      }
    }
  };
  conv(Mode<0>{});
#pragma unroll 1
  for (int layer = 0; layer < 2; ++layer) {
    const u32 xt_off = O_XT + layer * LAY_SZ, h_off = O_H + layer * LAY_SZ;
    f32x4 e4, o4;
    auto ffn = [&](auto role) {
      constexpr bool A = decltype(role)::A;
      PHASE_IDS();
      auto hof = [&](int p) { return h_off + (T2 * FT + min(kq + 4 * p, FT - 1)) * 1024; };
      const u32 w2s = O_W2F + (nt * FT + 12) * 2048;
      u32 xq[4];
#pragma unroll
      for (int g = 0; g < 4; ++g) xq[g] = hh * 512 + ((lane & 15) >> 2) * 64 + ((lane >> 4) & 1) * 32 + (lane & 3) * 8 + ((g + kq) & 3) * 8192;
      bf16x8 fr[PD];
      u32 csr = L_CS + kq * 128 + hh * 64, str = L_ST + lane16;
      u32 hs = (ftw < FT && ntile > 0 ? h_off + ((A ? Ta : 11 + Ta) * FT + ftw) * 1024 : OOB) + lane16;
      u32 rot16 = (((r32 + 8 * kq) & 31) + 32 * hh) * 16, r16 = r32 * 16, stw = str + kq * 4;
      asm volatile("" : "+v"(r16), "+v"(xq[0]), "+v"(xq[1]), "+v"(xq[2]), "+v"(xq[3]), "+v"(csr), "+v"(str), "+v"(rot16), "+v"(hs), "+v"(stw));
      u32 xs = xt_off + Ta * 32768 + kq * 8192;
      asm volatile("" : "+s"(xs));
      constexpr u32 XS = 11 * 32768;
      const int Tj = 11 * (1 + (sl >> 2)) + Ta;
      const bool job = sl < 4 * (ntile - 1);
      u32 ja = job ? lane * 64 : OOB;
      const u32 js = xs + (1 + (sl >> 2)) * XS + (sl & 3) * 16;
      u32x4 jv[2];
      {
        u32x4 xa[4];
        const u32 xs0 = xt_off + Ta * 32768, t16 = tid * 16;
        if (layer == 1) {
          constexpr int RN = (56 - 4 * R3_CONV) / 8;
#pragma unroll
          for (int j = 0; j < RN; ++j) dmak(j, rs, w2s + R3_CONV * 4096 + w * (RN * 1024), L_R3 + R3_CONV * 4096 + w * (RN * 1024), lane16);
          delay(X_DELAY);
        }
        BAR();
        STAMP(1 + 3 * layer, 4);
        xload<4>(rs, xa, t16, xs0, 8192);
        if (!A) xload<2>(rs, jv, ja, js, 4096);
        if (layer == 0) {
          float c0 = 0.f, c1 = 0.f;
          u32 one2 = 0x3f803f80u;
          asm volatile("" : "+s"(one2));
#pragma unroll
          for (int i = 0; i < 16; ++i) {
            const u32x4 v = __builtin_bit_cast(u32x4, W[A ? i : 16 + i]);
            c0 = dot2(v[0], one2, c0); c1 = dot2(v[1], one2, c1);
            c0 = dot2(v[2], one2, c0); c1 = dot2(v[3], one2, c1);
          }
          c0 += c1;
          lds_st<float>(L_CS + w * 128 + ((r32 >> 2) & 1) * 64 + (r32 >> 3) * 16 + (r32 & 3) * 4, sw32(c0, c0));
        }
        xpark<4>(rs, xa, t16, xs0, 8192, t16);
        STAMP(1 + 3 * layer, 5);
      }
      BAR();
      STAMP(1 + 3 * layer, 6);
      bf16x8 Wr[3][2];
      u32x4 fh[NHW];
      auto slot = [](int p) { return p % NHW; };
      auto hcheck = [&](int p0, int p1) {
        u32 m = 0;
#pragma unroll
        for (int p = p0; p < p1; ++p) m = umax4(m, fh[slot(p)]);
        if (__builtin_expect(!none(m == EMPTY), 0)) {
#pragma unroll 1
          for (int s = 0; s < SPIN_MAX; ++s) {
            asm volatile("" ::: "memory");
            m = 0;
#pragma unroll
            for (int p = p0; p < (p0 + NHW < 22 ? p0 + NHW : 22); ++p)
              if (!none(umax4(0, fh[slot(p)]) == EMPTY)) fh[slot(p)] = ld16(rs, lane16, hof(p));
#pragma unroll
            for (int p = p0; p < p1; ++p) m = umax4(m, fh[slot(p)]);
            RETRY(p0);
            if (none(m == EMPTY)) break;
          }
        }
      };
      {
        f32x16 acc;
        auto nop = []() {};
        const u32 g1_off = O_G1 + layer * 32768;
        u32x4 gs;
        auto recheck = [&](int k, int toff, auto redo) __attribute__((always_inline)) {
          if (__builtin_expect(!none(acc[0] != acc[0]), 0)) {
#pragma unroll 1
            for (int j = 0; j < 32; ++j) {
              u32x4 v[1];
              const u32 so = xt_off + (11 * k + Ta) * 32768 + j * 1024;
              xload<1>(rs, v, lane16, so, 0);
              xpark<1>(rs, v, lane16, so, 0, (k % 3) * 32768 + j * 1024 + lane16);
            }
            WAITL0();
            g1_pre(fr, xq, toff);
            g1_chain<0>(acc, W, fr, xq, toff, 0, [](int) {});
            redo();
            __builtin_amdgcn_s_waitcnt(0x0F70);
          }
        };
        auto ldst = [&](int k) { gs = ld16(rs, r16, g1_off + (11 * k + Ta) * 512); };
        auto epi = [&](int k, auto mid, auto pk) {
          asm volatile("" : "+v"(gs));
          if (!none(((gs[1] ^ tag) | (gs[3] ^ tag)) != 0)) {
#pragma unroll 1
            for (int s = 0; s < SPIN_MAX; ++s) {
              asm volatile("" ::: "memory");
              ldst(k);
              if (none(((gs[1] ^ tag) | (gs[3] ^ tag)) != 0)) break;
            }
          }
          st16(rs, hs + (k >> 1) * (22 * FT * 1024), g1_epi<decltype(pk)::A>(acc, __uint_as_float(gs[0]), __uint_as_float(gs[2]), csr, mid));
        };
        auto ldWr = [&](int k) {
          Wr[k >> 1][k & 1] = __builtin_bit_cast(bf16x8, ld16(rs, rot16 + (k & 1) * 1024, O_W2F + (nt * FT + kq + 4 * (k >> 1)) * 2048));
        };
        auto shift = [&]() {
#pragma unroll
          for (int g = 0; g < 4; ++g) { xq[g] += 32768; asm volatile("" : "+v"(xq[g])); }
        };
        if constexpr (A) {
          g1_pre(fr, xq, 0);
          g1_chain<8>(acc, W, fr, xq, 0, stw, [&](int k) {
            if (k >= 2 && k < 10) dmak(k - 2, rs, xs + 2 * XS, 65536 + kq * 8192, lane16);
            else if (k >= 10 && layer == 0) dmak(k - 2, rs, w2s + kq * 14336, L_R3 + kq * 14336, lane16);
          });
          BAR();
          STAMP(2 + 3 * layer, 0);
          {
            const f32x4 p = lds_ld<f32x4>(str);
            float s = p[0] + p[1], q = p[2] + p[3];
            asm volatile("" : "+v"(s), "+v"(q));
            s = (s + q) * (1.f / SD);
            const auto sw = __builtin_amdgcn_permlane32_swap(__float_as_uint(s), __float_as_uint(s), false, false);
            const float m = __uint_as_float(sw[0]);
            st16(rs, hs, g1_epi<false>(acc, -m, fmaxf(fmaf(-m, m, __uint_as_float(sw[1])), 0.f) + 1e-5f, csr, nop));
          }
          shift();
          WAITV(1);
          g1_pre(fr, xq, 32768);
          BAR();
          STAMP(2 + 3 * layer, 1);
          g1_chain<0>(acc, W, fr, xq, 32768, 0, [&](int k) {
            if (k < 8) dmak(k, rs, xs + 4 * XS, 32768 + kq * 8192, lane16);
            else if (k == 8) ldst(2);
            else if (k < 15) ldWr(k - 9);
          });
          recheck(2, 32768, nop);
          BAR();
          STAMP(2 + 3 * layer, 2);
          epi(2, nop, Role<false>{});
          g1_pre(fr, xq, 0);
          BAR();
          STAMP(2 + 3 * layer, 3);
          auto side = [&](int k) {
            if (k < 8) dmak(k, rs, w2s + 57344 + kq * 8192, kq * 8192, lane16);
            else if (k == 8) ldst(4);
            else fh[k - 9] = ld16(rs, lane16, hof(k - 9));
          };
          g1_chain<0>(acc, W, fr, xq, 0, 0, side);
          recheck(4, 0, [&]() {
#pragma unroll
            for (int k = 8; k < 16; ++k) side(k);
          });
          BAR();
          STAMP(2 + 3 * layer, 4);
          epi(4, [&]() {
            SCHED();
#pragma unroll
            for (int p = 7; p < 10; ++p) fh[p] = ld16(rs, lane16, hof(p));
            SCHED();
          }, Role<false>{});
#pragma unroll
          for (int p = 10; p < NHW; ++p) fh[p] = ld16(rs, lane16, hof(p));
          hcheck(0, 7);
          STAMP(2 + 3 * layer, 5);
          BAR();
        } else {
          asm volatile("" : "+v"(jv[0]), "+v"(jv[1]));
          if (!xfull<2>(jv)) xretry<2>(rs, jv, ja, js, 4096);
#pragma unroll
          for (int k = 0; k < 8; ++k) dmak(k, rs, xs + XS, 32768 + kq * 8192, lane16);
          if (job) {
            const u32x4 v0 = jv[0], v1 = jv[1];
            float r[8];
#pragma unroll
            for (int k = 0; k < 8; ++k) {
              float x = (k & 1) ? bfhi(v0[k >> 1]) : bflo(v0[k >> 1]), y = (k & 1) ? bfhi(v1[k >> 1]) : bflo(v1[k >> 1]);
              float s1 = x + y, s2 = x * x;
              asm volatile("" : "+v"(s1), "+v"(s2));
              s2 = fmaf(y, y, s2);
              r[k] = sw32(s1, s2);
              asm volatile("" : "+v"(r[k]));
            }
#pragma unroll
            for (int k = 0; k < 4; ++k) {
              const auto sw = __builtin_amdgcn_permlane16_swap(__float_as_uint(r[k]), __float_as_uint(r[4 + k]), false, false);
              float t = __uint_as_float(sw[0]) + __uint_as_float(sw[1]);
              t += dppf<0xB1>(t); t += dppf<0x4E>(t); t += dppf<0x141>(t); t += dppf<0x140>(t);
              r[k] = t;
            }
            const bool b0 = lane & 1, b1 = lane & 2;
            lds_st<float>(L_ST + 1024 + (lane >> 4) * 64 + (lane & 3) * 16 + kq * 4, b1 ? (b0 ? r[3] : r[2]) : (b0 ? r[1] : r[0]));
            WAITV(0);
            WAITL0();
            u32 cnt = 0;
            if (lane == 0) cnt = __hip_atomic_fetch_add((LDSP u32*)(size_t)(L_TH + 2628), 1u, __ATOMIC_RELAXED, __HIP_MEMORY_SCOPE_WORKGROUP);
            if ((__builtin_amdgcn_readfirstlane(cnt) & 3) == 3) {
              asm volatile("" ::: "memory");
              const f32x4 p = lds_ld<f32x4>(L_ST + 1024 + (lane & 15) * 16);
              float s = p[0] + p[1], q = p[2] + p[3];
              asm volatile("" : "+v"(s), "+v"(q));
              const float m = (s + q) * (1.f / SD);
              const float v = fmaxf(fmaf(-m, m, dppf<0x128>(m)), 0.f) + 1e-5f;
              st16(rs, lane < 8 ? g1_off + (Tj * 32 + (sl & 3) * 8 + lane) * 16 : OOB, (u32x4){__float_as_uint(-m), tag, __float_as_uint(v), tag});
            }
          }
          if (!job) WAITV(0);
          g1_pre(fr, xq, 32768);
          if (layer == 0) {
            const u32 a = L_CS + kq * 128 + r32 * 4;
            lds_st<float>(a, lds_ld<float>(a) + lds_ld<float>(a + 512));
            u32 e = EMPTY;
            asm volatile("" : "+v"(e));
            st16(rs, O_XT + LAY_SZ + b * 8192 + (tid & 255) * 16, (u32x4){e, e, e, e});
            st16(rs, O_XT + LAY_SZ + b * 8192 + 4096 + (tid & 255) * 16, (u32x4){e, e, e, e});
          }
          BAR();
          STAMP(2 + 3 * layer, 0);
          g1_chain<0>(acc, W, fr, xq, 32768, 0, [&](int k) {
            if (k < 8) dmak(k, rs, xs + 3 * XS, kq * 8192, lane16);
            else if (k == ST1_AT) ldst(1);
            if ((k == 3 || k == 7) && layer == 0) {
#pragma unroll
              for (int j = k - 3; j <= k; ++j) dmak(j, rs, w2s + kq * 14336, L_R3 + kq * 14336, lane16);
            }
          });
          recheck(1, 32768, nop);
          BAR();
          STAMP(2 + 3 * layer, 1);
          epi(1, nop, Role<false>{});
          g1_pre(fr, xq, 0);
          if (layer == 0 && b == 0) st4(rs, tid == 256 ? O_EP : OOB, 0, tag);
          BAR();
          STAMP(2 + 3 * layer, 2);
          u32x4 g5;
          const u32 g5o = g1_off + ((ntile == 6 ? 55 : 33) + Ta) * 512;
          g1_chain<0>(acc, W, fr, xq, 0, 0, [&](int k) {
            if (k < 8) dmak(k, rs, xs + (ntile == 6 ? 5 : 4) * XS, 65536 + kq * 8192, lane16);
            else if (k == 8) g5 = ld16(rs, r16, g5o);
            else if (k == 9) ldst(3);
            else ldWr(k - 10);
          });
          recheck(3, 0, nop);
          BAR();
          STAMP(2 + 3 * layer, 3);
          asm volatile("" : "+v"(g5));
          if (!none(((g5[1] ^ tag) | (g5[3] ^ tag)) != 0)) {
#pragma unroll 1
            for (int s = 0; s < SPIN_MAX; ++s) {
              asm volatile("" ::: "memory");
              g5 = ld16(rs, r16, g5o);
              if (none(((g5[1] ^ tag) | (g5[3] ^ tag)) != 0)) break;
            }
          }
          epi(3, nop, Role<false>{});
          shift();
          g1_pre(fr, xq, 32768);
          BAR();
          STAMP(2 + 3 * layer, 4);
          auto side = [&](int k) {
            if (k < 8) dmak(k, rs, w2s + 90112 + kq * 8192, 32768 + kq * 8192, lane16);
          };
          if (ntile != 6) {
#pragma unroll
            for (int k = 0; k < 16; ++k) side(k);
          } else {
            g1_chain<0>(acc, W, fr, xq, 32768, 0, side);
            recheck(5, 32768, nop);
          }
          STAMP(2 + 3 * layer, 5);
          if (ntile != 6) hs = OOB;
          if (LAST_PK) {
            __builtin_amdgcn_s_setprio(1);
            st16(rs, hs + 2 * (22 * FT * 1024), g1_epi<true>(acc, __uint_as_float(g5[0]), __uint_as_float(g5[2]), csr, nop));
            WAITV(1);
            BAR();
          } else {
            WAITV(0);
            BAR();
            __builtin_amdgcn_s_setprio(1);
            st16(rs, hs + 2 * (22 * FT * 1024), g1_epi<false>(acc, __uint_as_float(g5[0]), __uint_as_float(g5[2]), csr, nop));
          }
          if (T2 >= 55) delay(LATE_DELAY);
#pragma unroll
          for (int p = 0; p < NHW; ++p) fh[p] = ld16(rs, lane16, hof(p));
        }
      }
      STAMP(2 + 3 * layer, 6);
      f32x16 acc[2];
#pragma unroll
      for (int c = 0; c < 2; ++c)
#pragma unroll
        for (int e = 0; e < 16; ++e) acc[c][e] = 0.f;
      u32 wb0 = kq * 2048 + rot16, wb1 = wb0 + 65536, wb3 = wb0 + L_R3;
      asm volatile("" : "+v"(wb0), "+v"(wb1), "+v"(wb3));
      auto ldW = [&](int p, int c) {
        return lds_ld<bf16x8>((p < 10 ? wb3 + (p - 3) * 8192 : p < 18 ? wb0 + (p - 10) * 8192 : wb1 + (p - 18) * 8192) + c * 1024);
      };
      u16 rsd[8];
      const u32 rso = hh * 512 + r32 * 2, rss = xt_off + T2 * 32768 + (nt * 64 + kq * 16) * 64;
      SCHED();
#pragma unroll
      for (int p = 0; p < 22; ++p) {
        if (!A && p % 3 == 0 && p < 21) hcheck(p, p == 18 ? 22 : p + 3);
        const bf16x8 f = __builtin_bit_cast(bf16x8, fh[slot(p)]);
        if (p == 21 && kq >= 2) break;
        acc[0] = __builtin_amdgcn_mfma_f32_32x32x16_bf16(Wr[p % 3][0], f, acc[0], 0, 0, 0);
        SCHED();
        if (p == 15) { if constexpr (!A) BAR(); else WAITV(10); }
        if (p + 3 < 22) Wr[p % 3][0] = ldW(p + 3, 0);
        if (A && p < 8) dmak(p & 1, rs, w2s + 122880 + (p >> 1) * 8192 + kq * 2048, 65536 + (p >> 1) * 8192 + kq * 2048, lane16);
        if (p >= 11 && p < 15) rsd[2 * p - 22] = ld2(rs, rso + (2 * p - 22) * 64, rss);
        SCHED();
        acc[1] = __builtin_amdgcn_mfma_f32_32x32x16_bf16(Wr[p % 3][1], f, acc[1], 0, 0, 0);
        SCHED();
        if (p + 3 < 22) Wr[p % 3][1] = ldW(p + 3, 1);
        if (p + NHW < 22) fh[p] = ld16(rs, lane16, hof(p + NHW));
        if (p >= 11 && p < 15) rsd[2 * p - 21] = ld2(rs, rso + (2 * p - 21) * 64, rss);
        if (p == 8) {
          STAMP(3 + 3 * layer, 2);
          if (!A && B_PRIO) __builtin_amdgcn_s_setprio(0);
        }
        SCHED();
      }
      if (A && __builtin_expect(!none(acc[0][0] != acc[0][0]), 0)) {
#pragma unroll
        for (int e = 0; e < 16; ++e) acc[0][e] = acc[1][e] = 0.f;
#pragma unroll 1
        for (int ft = kq; ft < FT; ft += 4) {
          u32x4 v[1];
          const u32x4 wa = ld16(rs, rot16, O_W2F + (nt * FT + ft) * 2048), wb = ld16(rs, rot16 + 1024, O_W2F + (nt * FT + ft) * 2048);
          xload<1>(rs, v, lane16, h_off + (T2 * FT + ft) * 1024, 0);
          if (!xfull<1>(v)) xretry<1>(rs, v, lane16, h_off + (T2 * FT + ft) * 1024, 0);
          acc[0] = __builtin_amdgcn_mfma_f32_32x32x16_bf16(__builtin_bit_cast(bf16x8, wa), __builtin_bit_cast(bf16x8, v[0]), acc[0], 0, 0, 0);
          acc[1] = __builtin_amdgcn_mfma_f32_32x32x16_bf16(__builtin_bit_cast(bf16x8, wb), __builtin_bit_cast(bf16x8, v[0]), acc[1], 0, 0, 0);
        }
        RETRY(9);
      }
      STAMP(3 + 3 * layer, 3);
      if constexpr (A) BAR();
      const u32 pw = L_R3 + kq * 2048 + tt * 24576 + lane16;
#pragma unroll
      for (int q = 1; q < 4; ++q) {
        lds_st<f32x4>(pw + (q - 1) * 8192, (f32x4){acc[0][4 * q], acc[0][4 * q + 1], acc[0][4 * q + 2], acc[0][4 * q + 3]});
        lds_st<f32x4>(pw + (q - 1) * 8192 + 1024, (f32x4){acc[1][4 * q], acc[1][4 * q + 1], acc[1][4 * q + 2], acc[1][4 * q + 3]});
      }
#pragma unroll
      for (int e = 0; e < 4; ++e) { e4[e] = acc[0][e] + b2f(rsd[2 * e]); o4[e] = acc[1][e] + b2f(rsd[2 * e + 1]); }
      asm volatile("" : "+v"(e4), "+v"(o4));
    };
    asm volatile("" : "=v"(e4), "=v"(o4));
    if (w0 < 4) ffn(Role<true>{}); else ffn(Role<false>{});
    PHASE_IDS();
    {
      BAR();
      STAMP(3 + 3 * layer, 4);
#pragma unroll
      for (int q = 1; q < 4; ++q) {
        const u32 a = L_R3 + ((kq - q) & 3) * 2048 + tt * 24576 + lane16 + (q - 1) * 8192;
        e4 += lds_ld<f32x4>(a);
        o4 += lds_ld<f32x4>(a + 1024);
      }
      u32x4 o = {pkbf(e4[0], o4[0]), pkbf(e4[1], o4[1]), pkbf(e4[2], o4[2]), pkbf(e4[3], o4[3])};
      int last = layer;
      asm volatile("" : "+s"(last));
      if (last == 1) {
        *(u32x4*)(out + (size_t)(T2 * 32 + r32) * SD + nt * 64 + kq * 16 + hh * 8) = o;
        continue;
      }
      float s1 = 0.f, s2 = 0.f;
#pragma unroll
      for (int k = 0; k < 4; ++k) { s1 = dot2(o[k], 0x3f803f80u, s1); s2 = dot2(o[k], o[k], s2); }
      lds_st<float>(L_ST + tt * 1024 + lane16 + kq * 4, sw32(s1, s2));
      auto put_x = [&]() {
      {
        const bool b0 = lane & 1, b1 = lane & 2;
        const u32 s0 = dppu<0xB1>(b0 ? o[0] : o[1]), s1_ = dppu<0xB1>(b0 ? o[2] : o[3]);
        const u32 p0 = b0 ? s0 : o[0], p1 = b0 ? o[1] : s0, p2 = b0 ? s1_ : o[2], p3 = b0 ? o[3] : s1_;
        const u32 t0 = dppu<0x4E>(b1 ? p0 : p2), t1 = dppu<0x4E>(b1 ? p1 : p3);
        o = (u32x4){b1 ? t0 : p0, b1 ? t1 : p1, b1 ? p2 : t0, b1 ? p3 : t1};
      }
      st16(rs, O_XP + (nt * 32 + kq * 8 + hh * 4 + (lane & 3)) * 8192 + (T2 * 32 + (r32 & ~3)) * 4, notempty(o));
      };
      if (kq != 0) put_x();
      BAR();
      if (kq == 0) {
        const f32x4 p = lds_ld<f32x4>(L_ST + tt * 1024 + lane16);
        const float x = ((p[0] + p[1]) + (p[2] + p[3])) * (hh ? 0.0625f : 1.f), hi = floorf(x);
        const bool ok = fabsf(x) < 134217728.f;
        const u32 lo = ok ? ((u32)((x - hi) * 4294967296.f) & ~255u) | 1u : 0x11u;
        const int t = T2 * 32 + r32;
        __hip_atomic_fetch_add((GLBP u64*)(ws + O_S2 + hh * 16384 + ((t >> 1) & 1) * 8192 + (t >> 2) * 16 + (t & 1) * 8),
                               ((u64)(u32)(ok ? (int)hi : 0) << 32) | lo, __ATOMIC_RELAXED, __HIP_MEMORY_SCOPE_AGENT);
        put_x();
      }
    }
    if (S3_GATE && w == 4) st4(rs, lane == 0 ? O_F3 + b * 128 : OOB, 0, tag);
    STAMP(3, 5);
    {
      u32 kix[9];
      conv_prep(tid, w, kix);
      expand(tid, ch, kix);
    }
    delay(S3_DELAY + (n < 184 ? S3_STEP : 0));
    if (S3_GATE) {
      if (w < 4) poll(rs, O_F3 + tid * 128, 0, tag);
      BAR();
    }
    STAMP(3, 6);
    {
      u32x4 sv[4], xr;
      xload<4>(rs, sv, tid * 16, O_S2, 8192);
      xr = ld16(rs, tid * 16, O_XP + n * 8192);
      auto s3_ok = [&]() {
        u32 all = 8;
#pragma unroll
        for (int k = 0; k < 4; ++k) all &= sv[k][0] & sv[k][2];
        return none(all == 0 || umax4(0, xr) == EMPTY);
      };
      if (!s3_ok()) {
#pragma unroll 1
        for (int s = 0; s < SPIN_MAX; ++s) {
          asm volatile("" ::: "memory");
          u32x4 t[5];
#pragma unroll
          for (int k = 0; k < 4; ++k) t[k] = ld16(rs, (sv[k][0] & sv[k][2] & 8) ? OOB : tid * 16, O_S2 + k * 8192);
          t[4] = ld16(rs, umax4(0, xr) == EMPTY ? tid * 16 : OOB, O_XP + n * 8192);
#pragma unroll
          for (int k = 0; k < 4; ++k) sv[k] = (sv[k][0] & sv[k][2] & 8) ? sv[k] : t[k];
          xr = umax4(0, xr) == EMPTY ? t[4] : xr;
          if (s3_ok()) break;
          __builtin_amdgcn_s_sleep(4);
        }
      }
      u32 fl = 0;
#pragma unroll
      for (int k = 0; k < 4; ++k) fl |= sv[k][0] | sv[k][2];
      STAMP(3, 7);
      float s1[4], s2[4];
#pragma unroll
      for (int i = 0; i < 4; ++i) {
        s1[i] = fmaf((float)sv[i >> 1][2 * (i & 1)], 2.3283064365386963e-10f, (float)(int)sv[i >> 1][2 * (i & 1) + 1]);
        s2[i] = fmaf((float)sv[2 + (i >> 1)][2 * (i & 1)], 2.3283064365386963e-10f, (float)(int)sv[2 + (i >> 1)][2 * (i & 1) + 1]) * 16.f;
      }
      if (__builtin_expect(!none((fl & 0xf0) != 0), 0)) {
#pragma unroll
        for (int i = 0; i < 4; ++i) s1[i] = s2[i] = 0.f;
#pragma unroll 1
        for (int p = 0; p < 256; ++p) {
          u32x4 v;
#pragma unroll 1
          for (int s = 0; s < SPIN_MAX; ++s) {
            asm volatile("" ::: "memory");
            v = ld16(rs, tid * 16, O_XP + p * 8192);
            if (none(umax4(0, v) == EMPTY)) break;
          }
#pragma unroll
          for (int i = 0; i < 4; ++i) { s1[i] = dot2(v[i], 0x3f803f80u, s1[i]); s2[i] = dot2(v[i], v[i], s2[i]); }
        }
      }
      float mean[4], rstd[4];
#pragma unroll
      for (int i = 0; i < 4; ++i) {
        mean[i] = s1[i] * (1.f / SD);
        rstd[i] = __builtin_amdgcn_rsqf(fmaxf(fmaf(s2[i], 1.f / SD, -mean[i] * mean[i]), 0.f) + 1e-5f);
      }
      normalize(tid, xr, mean, rstd);
    }
    conv(Mode<1>{});
  }
}

extern "C" void kernel_launch(void* const* d_in, const int* in_sizes, int n_in, void* d_out, int out_size,
                              void* d_ws, size_t ws_size, hipStream_t stream) {
  k_all<<<256, 512, 0, stream>>>((const u16*)d_in[0], (const u16*)d_in[1], (const u16*)d_in[2], (const u16*)d_in[3],
                                 (const u16*)d_in[4], (const u16*)d_in[5], (const u16*)d_in[6], (const u16*)d_in[8],
                                 (const u16*)d_in[9], (const u16*)d_in[10], (char*)d_ws, (u16*)d_out);
}
```
